```python
import torch

def _softmax(x, axis=-1):
    return torch.softmax(x, dim=axis)
def _gelu(x):
    return torch.nn.functional.gelu(x, approximate="tanh")
def _silu(x):
    return torch.nn.functional.silu(x)
def _sigmoid(x):
    return torch.sigmoid(x)
def _layernorm(x, axis=-1, eps=1e-5):
    m = x.mean(dim=axis, keepdim=True)
    v = x.var(dim=axis, keepdim=True, unbiased=False)
    return (x - m) / torch.sqrt(v + eps)
def _scatter_add(tgt, idx, src, axis=0):
    return tgt.clone().index_add_(axis, idx, src)
def _segment_sum(src, seg, n):
    out = torch.zeros((n,) + src.shape[1:], dtype=src.dtype)
    return out.index_add_(0, seg, src)
def _pad(x, pw):
    flat = tuple(v for pair in reversed(pw) for v in pair)
    return torch.nn.functional.pad(x, flat)

kernel_name = 'concat_heads__ssm_conv_s4__softmax_full_mha__prompt'

def setup_inputs():

    _w = (lambda a: (lambda t: t.to(torch.bfloat16) if t.is_floating_point() else t)(torch.as_tensor(a)))
    def _A_setup():
        B, L, D, N = 1, 2048, 448, 256
        A_re = -(lambda _x: torch.exp(torch.as_tensor(_x)))((lambda *s: torch.randn(*s, dtype=torch.bfloat16) * 3.2)(D, N))
        A_im = torch.pi * (lambda *s: torch.randn(*s, dtype=torch.bfloat16) * 0.05)(D, N)
        B_re = (lambda *s: torch.randn(*s, dtype=torch.bfloat16) * 0.2)(D, N)
        B_im = (lambda *s: torch.randn(*s, dtype=torch.bfloat16) * 0.2)(D, N)
        C_re = (lambda *s: torch.randn(*s, dtype=torch.bfloat16) * 0.2)(D, N)
        C_im = (lambda *s: torch.randn(*s, dtype=torch.bfloat16) * 0.2)(D, N)
        log_dt = (lambda *s: torch.randn(*s, dtype=torch.bfloat16) * 0.05)(D) * 0.5 - 4.0
        Dskip = (lambda *s: torch.randn(*s, dtype=torch.bfloat16) * 0.1)(D)
        return {
            "u": _w((lambda *s: torch.randn(*s, dtype=torch.bfloat16) * 0.05)(B, L, D)),
            "A_re": _w(A_re),
            "A_im": _w(A_im),
            "B_re": _w(B_re),
            "B_im": _w(B_im),
            "C_re": _w(C_re),
            "C_im": _w(C_im),
            "log_dt": _w(log_dt),
            "Dskip": _w(Dskip),
        }
    _A_in = _A_setup()

    def _B_setup():
        B, S, D = 1, 2048, 448
        return {
            "x": _w((lambda *s: torch.randn(*s, dtype=torch.bfloat16) * 0.2)(B, S, D)),
            "wqkv": _w((lambda *s: torch.randn(*s, dtype=torch.bfloat16) * 0.2)(D, 3 * D)),
            "wo": _w((lambda *s: torch.randn(*s, dtype=torch.bfloat16) * 0.05)(D, D)),
        }
    _B_in = _B_setup()

    return {
        "A_u": _A_in["u"],
        "A_A_re": _A_in["A_re"],
        "A_A_im": _A_in["A_im"],
        "A_B_re": _A_in["B_re"],
        "A_B_im": _A_in["B_im"],
        "A_C_re": _A_in["C_re"],
        "A_C_im": _A_in["C_im"],
        "A_log_dt": _A_in["log_dt"],
        "A_Dskip": _A_in["Dskip"],
        "B_x": _B_in["x"],
        "B_wqkv": _B_in["wqkv"],
        "B_wo": _B_in["wo"],
    }

def reference(A_u, A_A_re, A_A_im, A_B_re, A_B_im, A_C_re, A_C_im, A_log_dt, A_Dskip, B_x, B_wqkv, B_wo):
    def _A_ref(u, A_re, A_im, B_re, B_im, C_re, C_im, log_dt, Dskip):
        Bsz, L, D = u.shape
        dt = torch.exp(log_dt)[:, None]
        A = A_re + 1j * A_im
        Bc = B_re + 1j * B_im
        Cc = C_re + 1j * C_im
        logdA = dt * A
        dB = dt * Bc
        l = (lambda x, dt: x.to(dt))(torch.arange(L), A_re.dtype)
        P = torch.exp(logdA[:, :, None] * l[None, None, :])
        K = torch.einsum("dn,dnl,dn->dl", Cc, P, dB).real
        L2 = 2 * L
        Kp = _pad(K, ((0, 0), (0, L)))
        up = (lambda x, *d: x.permute(*d))(u, 0, 2, 1)
        up = _pad(up, ((0, 0), (0, 0), (0, L)))
        Kf = (lambda x, axes=(-2,-1): torch.fft.rfft2(x.float(), dim=axes))(Kp[:, None, :], axes=(-2, -1))
        Uf = (lambda x, axes=(-2,-1): torch.fft.rfft2(x.float(), dim=axes))(up[:, :, None, :], axes=(-2, -1))
        Yf = Uf * Kf[None, :, :, :]
        y = (lambda x, s, axes=(-2,-1): torch.fft.irfft2(x, s=s, dim=axes).to(torch.bfloat16))(Yf, (1, L2), axes=(-2, -1))[:, :, 0, :L]
        y = (lambda x, *d: x.permute(*d))(y, 0, 2, 1) + u * Dskip[None, None, :]
        return y

    def _B_ref(x, wqkv, wo):
        B, S, D = x.shape
        H = 16; Dh = D // H
        qkv = (lambda x, *d: x.permute(*d))((x @ wqkv).reshape(B, S, 3, H, Dh), 2, 0, 3, 1, 4)
        q, k, v = qkv[0], qkv[1], qkv[2]
        scale = Dh ** -0.5
        scores = torch.einsum("bhqd,bhkd->bhqk", q, k) * scale
        p = _softmax(scores, axis=-1)
        o = torch.einsum("bhqk,bhkd->bhqd", p, v)
        return (lambda x, *d: x.permute(*d))(o, 0, 2, 1, 3).reshape(B, S, D) @ wo

    a = _A_ref(A_u, A_A_re, A_A_im, A_B_re, A_B_im, A_C_re, A_C_im, A_log_dt, A_Dskip)
    b = _B_ref(B_x, B_wqkv, B_wo)
    return torch.concatenate([a, b], axis=-1)
```

```cpp
#include <hip/hip_runtime.h>
#include <hip/hip_bf16.h>
#include <stdint.h>
#define ROUND_L 0
typedef unsigned short u16;
__device__ __forceinline__ float bf2f(u16 b){ return __uint_as_float(((unsigned)b)<<16); }
__device__ __forceinline__ u16 f2bf(float f){ unsigned u=__float_as_uint(f); u += 0x7FFF + ((u>>16)&1); return (u16)(u>>16); }
__device__ __forceinline__ float rbf(float f){ return bf2f(f2bf(f)); }
constexpr int L=2048, D=448, N=256, H=16, DH=28;

__global__ void kgen(const u16* Are,const u16* Aim,const u16* Bre,const u16* Bim,const u16* Cre,const u16* Cim,const u16* logdt,float* K){
  int d=blockIdx.y; int l=blockIdx.x*256+threadIdx.x;
  float dt=rbf(expf(bf2f(logdt[d])));
  float lf = ROUND_L ? rbf((float)l) : (float)l;
  float acc=0;
  for(int n=0;n<N;n++){
    float a=dt*bf2f(Are[d*N+n]), b=dt*bf2f(Aim[d*N+n]);
    float br=dt*bf2f(Bre[d*N+n]), bi=dt*bf2f(Bim[d*N+n]);
    float cr=bf2f(Cre[d*N+n]), ci=bf2f(Cim[d*N+n]);
    float wr=cr*br-ci*bi, wi=cr*bi+ci*br;
    float e=expf(a*lf); float s,c; sincosf(b*lf,&s,&c);
    acc += e*(wr*c-wi*s);
  }
  K[d*L+l]=acc;
}
__global__ void conv(const float* K,const u16* u,const u16* Dsk,u16* out){
  int d=blockIdx.y; int t=blockIdx.x*256+threadIdx.x;
  float acc=0;
  for(int s=0;s<=t;s++) acc += K[d*L+t-s]*bf2f(u[s*D+d]);
  float y=rbf(acc); float sk=rbf(bf2f(u[t*D+d])*bf2f(Dsk[d]));
  out[t*896+d]=f2bf(y+sk);
}
__global__ void gemm(const u16* A,const u16* B,u16* C,int M,int Nn,int Kk,int ldc){
  int n=blockIdx.x*64+threadIdx.x%64; int m=blockIdx.y*4+threadIdx.x/64;
  if(n>=Nn) return;
  float acc=0;
  for(int k=0;k<Kk;k++) acc+=bf2f(A[m*Kk+k])*bf2f(B[k*Nn+n]);
  C[m*ldc+n]=f2bf(acc);
}
__global__ void attn(const u16* qkv,u16* o){
  int h=blockIdx.y; int q=blockIdx.x*64+threadIdx.x;
  float qv[DH];
  #pragma unroll
  for(int i=0;i<DH;i++) qv[i]=bf2f(qkv[q*1344+h*DH+i]);
  float scale=0.1889822365046136f;
  float m=-1e30f,l=0; float acc[DH];
  #pragma unroll
  for(int i=0;i<DH;i++) acc[i]=0;
  for(int k=0;k<L;k++){
    float s=0;
    #pragma unroll
    for(int i=0;i<DH;i++) s+=qv[i]*bf2f(qkv[k*1344+448+h*DH+i]);
    s*=scale;
    float mn=fmaxf(m,s); float al=expf(m-mn); float p=expf(s-mn);
    l=l*al+p; m=mn;
    #pragma unroll
    for(int i=0;i<DH;i++) acc[i]=acc[i]*al+p*bf2f(qkv[k*1344+896+h*DH+i]);
  }
  #pragma unroll
  for(int i=0;i<DH;i++) o[q*448+h*DH+i]=f2bf(acc[i]/l);
}
extern "C" void kernel_launch(void* const* d_in, const int* in_sizes, int n_in, void* d_out, int out_size, void* d_ws, size_t ws_size, hipStream_t stream){
  const u16* u=(const u16*)d_in[0];
  u16* out=(u16*)d_out;
  float* K=(float*)d_ws;
  u16* qkv=(u16*)((char*)d_ws+8*1024*1024);
  u16* o=(u16*)((char*)d_ws+24*1024*1024);
  kgen<<<dim3(8,D),256,0,stream>>>((const u16*)d_in[1],(const u16*)d_in[2],(const u16*)d_in[3],(const u16*)d_in[4],(const u16*)d_in[5],(const u16*)d_in[6],(const u16*)d_in[7],K);
  conv<<<dim3(8,D),256,0,stream>>>(K,u,(const u16*)d_in[8],out);
  gemm<<<dim3(21,512),256,0,stream>>>((const u16*)d_in[9],(const u16*)d_in[10],qkv,2048,1344,448,1344);
  attn<<<dim3(32,16),64,0,stream>>>(qkv,o);
  gemm<<<dim3(7,512),256,0,stream>>>(o,(const u16*)d_in[11],out+448,2048,448,448,896);
}
```

```cpp
#include <hip/hip_runtime.h>
typedef unsigned short u16; typedef unsigned u32; typedef unsigned long long u64;
#define GAS __attribute__((address_space(1)))

__global__ void __launch_bounds__(256, 2) fused_ssm_mha(const u16* x, const u16* wqkv, char* ws_, const u16* Are, const u16* Aim, const u16* logdt, const u16* wo, u16* out,
                                                        const u16* u, const u16* Bre, const u16* Bim, const u16* Cre, const u16* Cim, const u16* Dsk) {
    __shared__ __attribute__((aligned(16))) char smem[81920];
    u32 tid = threadIdx.x, wv; u64 tag; char* ws;
    asm volatile(
  "s_load_dword s30, s[0:1], 0\n"
  "s_load_dword s31, s[0:1], 64\n"
  "s_cmp_gt_u32 s2, 255\n"
  "s_cbranch_scc0 LB%=\n"
  "s_getpc_b64 s[4:5]\n"
  "LY%=:\n"
  "s_bfe_u32 s6, s2, 0x50003\n"
  "v_lshrrev_b32 v1, 6, v0\n"
  "v_and_b32 v2, 63, v0\n"
  "v_lshl_add_u32 v1, s6, 2, v1\n"
  "v_lshl_add_u32 v1, v2, 7, v1\n"
  "v_lshlrev_b32 v1, 7, v1\n"
  "v_min_u32 v1, LW%=-LY%=, v1\n"
  "s_mov_b64 exec, 15\n"
  "global_load_dword v225, v1, s[4:5]\n"
  "s_mov_b64 exec, -1\n"
  "s_branch LA%=\n"
  "LB%=:\n"
  "s_load_dwordx2 s[40:41], s[0:1], 8\n"
  "s_load_dwordx2 s[36:37], s[0:1], 0\n"
  "s_load_dwordx2 s[44:45], s[0:1], 16\n"
  "s_load_dwordx2 s[48:49], s[0:1], 48\n"
  "s_load_dwordx2 s[8:9], s[0:1], 56\n"
  "v_readfirstlane_b32 s54, v0\n"
  "s_and_b32 s18, s2, 1\n"
  "s_bfe_u32 s19, s2, 0x30003\n"
  "s_lshr_b32 s28, s2, 6\n"
  "s_and_b32 s29, s2, 6\n"
  "v_lshrrev_b32 v1, 3, v0\n"
  "v_and_b32 v2, 7, v0\n"
  "v_lshrrev_b32 v3, 2, v0\n"
  "v_bfe_u32 v4, v0, 4, 2\n"
  "s_lshr_b32 s54, s54, 6\n"
  "s_lshl1_add_u32 s20, s29, s28\n"
  "s_min_u32 s64, s19, 6\n"
  "s_lshl_b32 s64, s64, 7\n"
  "v_lshl_add_u32 v1, s54, 3, v1\n"
  "v_and_b32 v3, 4, v3\n"
  "v_xor_b32 v4, v4, v2\n"
  "v_xor_b32 v3, v3, v2\n"
  "s_mul_i32 s65, s18, 896\n"
  "s_add_u32 s65, s65, s64\n"
  "s_add_u32 s65, s65, 896\n"
  "s_cmp_eq_u32 s19, 7\n"
  "s_cselect_b32 s65, 1073741824, s65\n"
  "s_mul_i32 s66, s20, 114688\n"
  "v_mul_u32_u24 v5, 2688, v1\n"
  "v_mul_u32_u24 v6, 896, v1\n"
  "v_lshl_add_u32 v3, v3, 4, s65\n"
  "v_lshl_add_u32 v4, v4, 4, s66\n"
  "v_add_u32 v232, v5, v3\n"
  "v_add_u32 v234, v6, v4\n"
  "v_add_u32 v233, 0x5400, v232\n"
  "v_xor_b32 v235, 64, v234\n"
  "v_add_u32 v235, 0x1c00, v235\n"
  "s_lshl_b32 s53, s54, 11\n"
  "s_mul_i32 s52, s18, 4\n"
  "s_add_u32 s52, s52, s29\n"
  "s_cmp_ge_u32 s52, 7\n"
  "s_cselect_b32 s28, 7, 0\n"
  "s_sub_u32 s52, s52, s28\n"
  "s_lshl_b32 s52, s52, 7\n"
  "s_mov_b32 s42, 1204224\n"
  "s_mov_b32 s43, 0x20000\n"
  "s_mov_b32 s38, 1835008\n"
  "s_mov_b32 s39, 0x20000\n"
  "s_mov_b32 s46, 11534336\n"
  "s_mov_b32 s47, 0x20000\n"
  "s_waitcnt lgkmcnt(0)\n"
  "s_add_u32 m0, s53, 0\n"
  "s_mul_i32 s65, s52, 1344\n"
  "buffer_load_dwordx4 v232, s[40:43], s65 offen lds\n"
  "s_load_dwordx2 s[26:27], s[44:45], 0\n"
  "s_add_u32 m0, s53, 1024\n"
  "s_nop 0\n"
  "buffer_load_dwordx4 v233, s[40:43], s65 offen lds\n"
  "s_add_u32 m0, s53, 8192\n"
  "s_nop 0\n"
  "buffer_load_dwordx4 v234, s[36:39], s52 offen lds\n"
  "s_add_u32 m0, s53, 9216\n"
  "s_nop 0\n"
  "buffer_load_dwordx4 v235, s[36:39], s52 offen lds\n"
  "s_add_u32 s52, s52, 128\n"
  "s_cmp_eq_u32 s52, 896\n"
  "s_cselect_b32 s52, 0, s52\n"
  "s_setprio 3\n"
  "s_add_u32 m0, s53, 16384\n"
  "s_mul_i32 s65, s52, 1344\n"
  "buffer_load_dwordx4 v232, s[40:43], s65 offen lds\n"
  "s_lshl3_add_u32 s21, s18, s19\n"
  "s_and_b32 s23, s19, 1\n"
  "s_mul_i32 s22, s21, 7\n"
  "s_lshr_b32 s22, s22, 1\n"
  "s_lshr_b32 s24, s54, 1\n"
  "s_and_b32 s25, s54, 1\n"
  "v_and_b32 v0, 63, v0\n"
  "v_and_b32 v1, 31, v0\n"
  "v_lshrrev_b32 v2, 5, v0\n"
  "v_bfe_u32 v3, v0, 2, 2\n"
  "s_add_u32 m0, s53, 17408\n"
  "s_nop 0\n"
  "buffer_load_dwordx4 v233, s[40:43], s65 offen lds\n"
  "v_bfe_u32 v4, v0, 1, 3\n"
  "v_xor_b32 v4, v4, v2\n"
  "s_lshl_b32 s28, s25, 12\n"
  "v_lshl_add_u32 v5, v1, 7, s28\n"
  "v_lshl_add_u32 v237, v4, 4, v5\n"
  "v_xor_b32 v238, 32, v237\n"
  "v_xor_b32 v239, 64, v237\n"
  "v_xor_b32 v240, 0x60, v237\n"
  "v_bfe_u32 v4, v0, 1, 1\n"
  "v_and_b32 v5, 1, v0\n"
  "s_add_u32 m0, s53, 24576\n"
  "s_nop 0\n"
  "buffer_load_dwordx4 v234, s[36:39], s52 offen lds\n"
  "v_lshl_or_b32 v4, v5, 1, v4\n"
  "v_bfe_u32 v5, v0, 4, 1\n"
  "v_lshl_or_b32 v4, v5, 2, v4\n"
  "v_lshl_add_u32 v5, v2, 3, v3\n"
  "v_bfe_u32 v6, v0, 3, 1\n"
  "v_xor_b32 v6, s24, v6\n"
  "v_lshlrev_b32 v6, 6, v6\n"
  "v_lshl_add_u32 v6, v5, 7, v6\n"
  "v_lshl_add_u32 v241, v4, 3, v6\n"
  "s_mul_i32 s28, s23, 6\n"
  "s_add_u32 m0, s53, 25600\n"
  "s_nop 0\n"
  "buffer_load_dwordx4 v235, s[36:39], s52 offen lds\n"
  "s_add_u32 s52, s52, 128\n"
  "s_cmp_eq_u32 s52, 896\n"
  "s_cselect_b32 s52, 0, s52\n"
  "v_xor_b32 v4, s28, v4\n"
  "v_max_u32 v4, s23, v4\n"
  "v_lshlrev_b32 v5, 6, v5\n"
  "v_lshl_add_u32 v242, v4, 3, v5\n"
  "v_add_u32 v245, 0x8000, v237\n"
  "v_add_u32 v246, 0x8000, v238\n"
  "v_add_u32 v247, 0x8000, v239\n"
  "v_add_u32 v248, 0x8000, v240\n"
  "v_add_u32 v249, 0x8000, v241\n"
  "v_add_u32 v250, 0x8000, v242\n"
  "s_add_u32 m0, s53, 32768\n"
  "s_mul_i32 s65, s52, 1344\n"
  "buffer_load_dwordx4 v232, s[40:43], s65 offen lds\n"
  "s_lshl1_add_u32 s55, s18, s24\n"
  "s_lshr_b32 s68, s53, 1\n"
  "v_lshrrev_b32 v4, 2, v0\n"
  "v_lshl_add_u32 v4, s54, 4, v4\n"
  "v_and_b32 v5, 3, v0\n"
  "v_add_lshl_u32 v5, v5, s22, 4\n"
  "v_mul_u32_u24 v4, 2688, v4\n"
  "v_add_u32 v236, v4, v5\n"
  "v_and_b32 v4, 0x13, v0\n"
  "v_bfe_u32 v5, v0, 2, 1\n"
  "s_add_u32 m0, s53, 33792\n"
  "s_nop 0\n"
  "buffer_load_dwordx4 v233, s[40:43], s65 offen lds\n"
  "v_lshl_or_b32 v4, v5, 3, v4\n"
  "v_bfe_u32 v5, v0, 3, 1\n"
  "v_lshl_or_b32 v4, v5, 2, v4\n"
  "s_lshl1_add_u32 s28, s19, s24\n"
  "s_lshl_b32 s29, s28, 9\n"
  "s_add_u32 s29, s29, 28672\n"
  "s_lshl_b32 s28, s28, 11\n"
  "s_cmp_eq_u32 s18, 0\n"
  "s_cselect_b32 s28, s28, s29\n"
  "s_xor_b32 s17, s19, 7\n"
  "s_cselect_b32 s28, s28, 1073741824\n"
  "s_add_u32 m0, s53, 40960\n"
  "s_nop 0\n"
  "buffer_load_dwordx4 v234, s[36:39], s52 offen lds\n"
  "s_mul_i32 s29, s18, 6656\n"
  "s_add_u32 s29, s29, 512\n"
  "s_lshl_b32 s57, s29, 1\n"
  "v_lshl_add_u32 v4, v4, 4, s28\n"
  "v_mad_u32_u24 v243, v2, s29, v4\n"
  "s_lshl4_add_u32 s28, s21, s20\n"
  "s_lshl_b32 s28, s28, 7\n"
  "s_lshl_b32 s29, s54, 24\n"
  "s_add_u32 s28, s28, s29\n"
  "s_add_u32 s28, s28, 4096\n"
  "s_add_u32 m0, s53, 41984\n"
  "s_nop 0\n"
  "buffer_load_dwordx4 v235, s[36:39], s52 offen lds\n"
  "s_add_u32 s52, s52, 128\n"
  "s_cmp_eq_u32 s52, 896\n"
  "s_cselect_b32 s52, 0, s52\n"
  "v_lshrrev_b32 v4, 0, v0\n"
  "v_lshl_add_u32 v4, v4, 23, v0\n"
  "v_lshl_add_u32 v244, v4, 3, s28\n"
  "v_lshl_add_u32 v251, v0, 4, s53\n"
  "s_lshl2_add_u32 s28, s20, s25\n"
  "s_mul_i32 s56, s28, 57344\n"
  "s_add_u32 s56, s56, 1048576\n"
  "s_lshl1_add_u32 s28, s18, s28\n"
  "s_lshl2_add_u32 s28, s28, s24\n"
  "s_lshl_b32 s14, s28, 13\n"
  "s_add_u32 m0, s53, 49152\n"
  "s_mul_i32 s65, s52, 1344\n"
  "buffer_load_dwordx4 v232, s[40:43], s65 offen lds\n"
  "s_add_u32 s14, s14, 5242880\n"
  "s_lshl_b32 s15, s28, 9\n"
  "s_add_u32 s15, s15, 262144\n"
  "s_lshl2_add_u32 s28, s20, s54\n"
  "s_lshl4_add_u32 s28, s28, s21\n"
  "s_lshl_b32 s12, s28, 11\n"
  "s_add_u32 s12, s12, 5242880\n"
  "s_lshl_b32 s13, s28, 7\n"
  "s_add_u32 s13, s13, 262144\n"
  "s_lshl1_add_u32 s28, s20, s18\n"
  "s_add_u32 m0, s53, 50176\n"
  "s_nop 0\n"
  "buffer_load_dwordx4 v233, s[40:43], s65 offen lds\n"
  "s_mul_i32 s16, s28, 114688\n"
  "s_add_u32 s16, s16, s64\n"
  "s_add_u32 s16, s16, 896\n"
  "s_mul_i32 s28, s23, 3\n"
  "s_add_u32 s28, s28, s22\n"
  "s_lshl_b32 s58, s28, 9\n"
  "s_lshl_b32 s28, s23, 10\n"
  "s_sub_u32 s59, 512, s28\n"
  "s_lshl_b32 s60, s23, 3\n"
  "s_mul_i32 s61, s21, 448\n"
  "s_add_u32 m0, s53, 57344\n"
  "s_nop 0\n"
  "buffer_load_dwordx4 v234, s[36:39], s52 offen lds\n"
  "s_add_u32 s61, s61, 28672\n"
  "s_lshr_b32 s28, s22, 3\n"
  "s_add_u32 s29, s22, 3\n"
  "s_lshr_b32 s29, s29, 3\n"
  "s_sub_u32 s29, s29, s28\n"
  "s_lshl_b32 s63, s29, 11\n"
  "s_lshl2_add_u32 s28, s28, s54\n"
  "s_lshl_b32 s62, s28, 9\n"
  "s_add_u32 s62, s62, 4096\n"
  "s_mov_b32 s50, 401408\n"
  "s_add_u32 m0, s53, 58368\n"
  "s_nop 0\n"
  "buffer_load_dwordx4 v235, s[36:39], s52 offen lds\n"
  "s_add_u32 s52, s52, 128\n"
  "s_cmp_eq_u32 s52, 896\n"
  "s_cselect_b32 s52, 0, s52\n"
  "s_mov_b32 s51, 0x20000\n"
  "s_mov_b32 s10, 3670016\n"
  "s_mov_b32 s11, 0x20000\n"
  "s_bitcmp1_b32 s55, 1\n"
  "s_cbranch_scc1 LV%=\n"
  "s_waitcnt vmcnt(12) lgkmcnt(0)\n"
  "s_barrier\n"
  "ds_read_b128 v[80:83], v237 offset:8192\n"
  "ds_read_b64_tr_b16 v[0:1], v241 offset:0\n"
  "ds_read_b64_tr_b16 v[2:3], v241 offset:512\n"
  "ds_read_b128 v[84:87], v238 offset:8192\n"
  "ds_read_b64_tr_b16 v[4:5], v241 offset:2048\n"
  "ds_read_b64_tr_b16 v[6:7], v241 offset:2560\n"
  "ds_read_b128 v[88:91], v239 offset:8192\n"
  "ds_read_b64_tr_b16 v[8:9], v241 offset:4096\n"
  "ds_read_b64_tr_b16 v[10:11], v241 offset:4608\n"
  "ds_read_b128 v[92:95], v240 offset:8192\n"
  "ds_read_b64_tr_b16 v[12:13], v241 offset:6144\n"
  "ds_read_b64_tr_b16 v[14:15], v241 offset:6656\n"
  "s_add_u32 m0, s53, 65536\n"
  "s_mul_i32 s65, s52, 1344\n"
  "buffer_load_dwordx4 v232, s[40:43], s65 offen lds\n"
  "s_add_u32 m0, s53, 66560\n"
  "s_nop 0\n"
  "buffer_load_dwordx4 v233, s[40:43], s65 offen lds\n"
  "s_add_u32 m0, s53, 73728\n"
  "s_nop 0\n"
  "buffer_load_dwordx4 v234, s[36:39], s52 offen lds\n"
  "s_add_u32 m0, s53, 74752\n"
  "s_nop 0\n"
  "buffer_load_dwordx4 v235, s[36:39], s52 offen lds\n"
  "s_add_u32 s52, s52, 128\n"
  "s_cmp_eq_u32 s52, 896\n"
  "s_cselect_b32 s52, 0, s52\n"
  "s_waitcnt vmcnt(12) lgkmcnt(0)\n"
  "s_barrier\n"
  "ds_read_b128 v[96:99], v237 offset:24576\n"
  "ds_read_b64_tr_b16 v[16:17], v241 offset:16384\n"
  "ds_read_b64_tr_b16 v[18:19], v241 offset:16896\n"
  "ds_read_b128 v[100:103], v238 offset:24576\n"
  "ds_read_b64_tr_b16 v[20:21], v241 offset:18432\n"
  "ds_read_b64_tr_b16 v[22:23], v241 offset:18944\n"
  "ds_read_b128 v[104:107], v239 offset:24576\n"
  "ds_read_b64_tr_b16 v[24:25], v241 offset:20480\n"
  "ds_read_b64_tr_b16 v[26:27], v241 offset:20992\n"
  "v_mfma_f32_32x32x16_bf16 v[160:175], v[0:3], v[80:83], 0\n"
  "ds_read_b128 v[108:111], v240 offset:24576\n"
  "ds_read_b64_tr_b16 v[28:29], v241 offset:22528\n"
  "ds_read_b64_tr_b16 v[30:31], v241 offset:23040\n"
  "v_mfma_f32_32x32x16_bf16 v[160:175], v[4:7], v[84:87], v[160:175]\n"
  "s_add_u32 m0, s53, 0\n"
  "s_mul_i32 s65, s52, 1344\n"
  "buffer_load_dwordx4 v232, s[40:43], s65 offen lds\n"
  "v_mfma_f32_32x32x16_bf16 v[160:175], v[8:11], v[88:91], v[160:175]\n"
  "s_add_u32 m0, s53, 1024\n"
  "s_nop 0\n"
  "buffer_load_dwordx4 v233, s[40:43], s65 offen lds\n"
  "v_mfma_f32_32x32x16_bf16 v[160:175], v[12:15], v[92:95], v[160:175]\n"
  "s_add_u32 m0, s53, 8192\n"
  "s_nop 0\n"
  "buffer_load_dwordx4 v234, s[36:39], s52 offen lds\n"
  "s_add_u32 m0, s53, 9216\n"
  "s_nop 0\n"
  "buffer_load_dwordx4 v235, s[36:39], s52 offen lds\n"
  "s_add_u32 s52, s52, 128\n"
  "s_cmp_eq_u32 s52, 896\n"
  "s_cselect_b32 s52, 0, s52\n"
  "s_waitcnt vmcnt(12) lgkmcnt(0)\n"
  "s_barrier\n"
  "ds_read_b128 v[112:115], v237 offset:40960\n"
  "ds_read_b64_tr_b16 v[32:33], v241 offset:32768\n"
  "ds_read_b64_tr_b16 v[34:35], v241 offset:33280\n"
  "ds_read_b128 v[116:119], v238 offset:40960\n"
  "ds_read_b64_tr_b16 v[36:37], v241 offset:34816\n"
  "ds_read_b64_tr_b16 v[38:39], v241 offset:35328\n"
  "ds_read_b128 v[120:123], v239 offset:40960\n"
  "ds_read_b64_tr_b16 v[40:41], v241 offset:36864\n"
  "ds_read_b64_tr_b16 v[42:43], v241 offset:37376\n"
  "v_mfma_f32_32x32x16_bf16 v[160:175], v[16:19], v[96:99], v[160:175]\n"
  "ds_read_b128 v[124:127], v240 offset:40960\n"
  "ds_read_b64_tr_b16 v[44:45], v241 offset:38912\n"
  "ds_read_b64_tr_b16 v[46:47], v241 offset:39424\n"
  "v_mfma_f32_32x32x16_bf16 v[160:175], v[20:23], v[100:103], v[160:175]\n"
  "s_add_u32 m0, s53, 16384\n"
  "s_mul_i32 s65, s52, 1344\n"
  "buffer_load_dwordx4 v232, s[40:43], s65 offen lds\n"
  "v_mfma_f32_32x32x16_bf16 v[160:175], v[24:27], v[104:107], v[160:175]\n"
  "s_add_u32 m0, s53, 17408\n"
  "s_nop 0\n"
  "buffer_load_dwordx4 v233, s[40:43], s65 offen lds\n"
  "v_mfma_f32_32x32x16_bf16 v[160:175], v[28:31], v[108:111], v[160:175]\n"
  "s_add_u32 m0, s53, 24576\n"
  "s_nop 0\n"
  "buffer_load_dwordx4 v234, s[36:39], s52 offen lds\n"
  "s_add_u32 m0, s53, 25600\n"
  "s_nop 0\n"
  "buffer_load_dwordx4 v235, s[36:39], s52 offen lds\n"
  "s_add_u32 s52, s52, 128\n"
  "s_cmp_eq_u32 s52, 896\n"
  "s_cselect_b32 s52, 0, s52\n"
  "s_waitcnt vmcnt(12) lgkmcnt(0)\n"
  "s_barrier\n"
  "ds_read_b128 v[128:131], v237 offset:57344\n"
  "ds_read_b64_tr_b16 v[48:49], v241 offset:49152\n"
  "ds_read_b64_tr_b16 v[50:51], v241 offset:49664\n"
  "ds_read_b128 v[132:135], v238 offset:57344\n"
  "ds_read_b64_tr_b16 v[52:53], v241 offset:51200\n"
  "ds_read_b64_tr_b16 v[54:55], v241 offset:51712\n"
  "ds_read_b128 v[136:139], v239 offset:57344\n"
  "ds_read_b64_tr_b16 v[56:57], v241 offset:53248\n"
  "ds_read_b64_tr_b16 v[58:59], v241 offset:53760\n"
  "v_mfma_f32_32x32x16_bf16 v[160:175], v[32:35], v[112:115], v[160:175]\n"
  "ds_read_b128 v[140:143], v240 offset:57344\n"
  "ds_read_b64_tr_b16 v[60:61], v241 offset:55296\n"
  "ds_read_b64_tr_b16 v[62:63], v241 offset:55808\n"
  "v_mfma_f32_32x32x16_bf16 v[160:175], v[36:39], v[116:119], v[160:175]\n"
  "s_add_u32 m0, s53, 32768\n"
  "s_add_u32 s66, s52, 57344\n"
  "buffer_load_dwordx4 v234, s[36:39], s66 offen lds\n"
  "v_mfma_f32_32x32x16_bf16 v[160:175], v[40:43], v[120:123], v[160:175]\n"
  "s_add_u32 m0, s53, 33792\n"
  "s_nop 0\n"
  "buffer_load_dwordx4 v235, s[36:39], s66 offen lds\n"
  "v_mfma_f32_32x32x16_bf16 v[160:175], v[44:47], v[124:127], v[160:175]\n"
  "s_add_u32 m0, s68, 40960\n"
  "s_mul_i32 s65, s52, 1344\n"
  "buffer_load_dwordx4 v236, s[40:43], s65 offen lds\n"
  "s_add_u32 s52, s52, 128\n"
  "s_cmp_eq_u32 s52, 896\n"
  "s_cselect_b32 s52, 0, s52\n"
  "s_waitcnt vmcnt(11) lgkmcnt(0)\n"
  "s_barrier\n"
  "s_add_u32 s26, s26, 1\n"
  "s_addc_u32 s27, s27, 0\n"
  "v_mov_b32 v252, s26\n"
  "v_mov_b32 v253, s27\n"
  "ds_read_b128 v[144:147], v245 offset:40960\n"
  "ds_read_b64_tr_b16 v[64:65], v249 offset:32768\n"
  "ds_read_b64_tr_b16 v[66:67], v249 offset:33280\n"
  "ds_read_b128 v[148:151], v246 offset:40960\n"
  "ds_read_b64_tr_b16 v[68:69], v249 offset:34816\n"
  "ds_read_b64_tr_b16 v[70:71], v249 offset:35328\n"
  "ds_read_b128 v[152:155], v247 offset:40960\n"
  "ds_read_b64_tr_b16 v[72:73], v249 offset:36864\n"
  "ds_read_b64_tr_b16 v[74:75], v249 offset:37376\n"
  "v_mfma_f32_32x32x16_bf16 v[160:175], v[48:51], v[128:131], v[160:175]\n"
  "ds_read_b128 v[156:159], v248 offset:40960\n"
  "ds_read_b64_tr_b16 v[76:77], v249 offset:38912\n"
  "ds_read_b64_tr_b16 v[78:79], v249 offset:39424\n"
  "v_mfma_f32_32x32x16_bf16 v[160:175], v[52:55], v[132:135], v[160:175]\n"
  "s_add_u32 m0, s53, 45056\n"
  "s_add_u32 s66, s52, 57344\n"
  "buffer_load_dwordx4 v234, s[36:39], s66 offen lds\n"
  "v_mfma_f32_32x32x16_bf16 v[160:175], v[56:59], v[136:139], v[160:175]\n"
  "s_add_u32 m0, s53, 46080\n"
  "s_nop 0\n"
  "buffer_load_dwordx4 v235, s[36:39], s66 offen lds\n"
  "v_mfma_f32_32x32x16_bf16 v[160:175], v[60:63], v[140:143], v[160:175]\n"
  "s_add_u32 m0, s68, 53248\n"
  "s_mul_i32 s65, s52, 1344\n"
  "buffer_load_dwordx4 v236, s[40:43], s65 offen lds\n"
  "s_add_u32 s52, s52, 128\n"
  "s_cmp_eq_u32 s52, 896\n"
  "s_cselect_b32 s52, 0, s52\n"
  "s_waitcnt vmcnt(10) lgkmcnt(0)\n"
  "s_barrier\n"
  "ds_read_b128 v[196:199], v237 offset:8192\n"
  "ds_read_b64_tr_b16 v[192:193], v241 offset:0\n"
  "ds_read_b64_tr_b16 v[194:195], v241 offset:512\n"
  "ds_read_b128 v[204:207], v238 offset:8192\n"
  "ds_read_b64_tr_b16 v[200:201], v241 offset:2048\n"
  "ds_read_b64_tr_b16 v[202:203], v241 offset:2560\n"
  "ds_read_b128 v[212:215], v239 offset:8192\n"
  "ds_read_b64_tr_b16 v[208:209], v241 offset:4096\n"
  "ds_read_b64_tr_b16 v[210:211], v241 offset:4608\n"
  "v_mfma_f32_32x32x16_bf16 v[160:175], v[64:67], v[144:147], v[160:175]\n"
  "ds_read_b128 v[220:223], v240 offset:8192\n"
  "ds_read_b64_tr_b16 v[216:217], v241 offset:6144\n"
  "ds_read_b64_tr_b16 v[218:219], v241 offset:6656\n"
  "v_mfma_f32_32x32x16_bf16 v[160:175], v[68:71], v[148:151], v[160:175]\n"
  "s_add_u32 m0, s53, 57344\n"
  "s_add_u32 s66, s52, 57344\n"
  "buffer_load_dwordx4 v234, s[36:39], s66 offen lds\n"
  "v_mfma_f32_32x32x16_bf16 v[160:175], v[72:75], v[152:155], v[160:175]\n"
  "s_add_u32 m0, s53, 58368\n"
  "s_nop 0\n"
  "buffer_load_dwordx4 v235, s[36:39], s66 offen lds\n"
  "v_mfma_f32_32x32x16_bf16 v[160:175], v[76:79], v[156:159], v[160:175]\n"
  "s_add_u32 m0, s68, 65536\n"
  "s_mul_i32 s65, s52, 1344\n"
  "buffer_load_dwordx4 v236, s[40:43], s65 offen lds\n"
  "s_add_u32 s52, s52, 128\n"
  "s_cmp_eq_u32 s52, 896\n"
  "s_cselect_b32 s52, 0, s52\n"
  "s_waitcnt lgkmcnt(9)\n"
  "v_mfma_f32_32x32x16_bf16 v[160:175], v[192:195], v[196:199], v[160:175]\n"
  "s_waitcnt lgkmcnt(6)\n"
  "v_mfma_f32_32x32x16_bf16 v[160:175], v[200:203], v[204:207], v[160:175]\n"
  "s_waitcnt lgkmcnt(3)\n"
  "v_mfma_f32_32x32x16_bf16 v[160:175], v[208:211], v[212:215], v[160:175]\n"
  "s_waitcnt lgkmcnt(0)\n"
  "v_mfma_f32_32x32x16_bf16 v[160:175], v[216:219], v[220:223], v[160:175]\n"
  "s_waitcnt vmcnt(9) lgkmcnt(0)\n"
  "s_barrier\n"
  "ds_read_b128 v[228:231], v237 offset:24576\n"
  "ds_read_b64_tr_b16 v[224:225], v241 offset:16384\n"
  "ds_read_b64_tr_b16 v[226:227], v241 offset:16896\n"
  "ds_read_b128 v[196:199], v238 offset:24576\n"
  "ds_read_b64_tr_b16 v[192:193], v241 offset:18432\n"
  "ds_read_b64_tr_b16 v[194:195], v241 offset:18944\n"
  "ds_read_b128 v[204:207], v239 offset:24576\n"
  "ds_read_b64_tr_b16 v[200:201], v241 offset:20480\n"
  "ds_read_b64_tr_b16 v[202:203], v241 offset:20992\n"
  "ds_read_b128 v[212:215], v240 offset:24576\n"
  "ds_read_b64_tr_b16 v[208:209], v241 offset:22528\n"
  "ds_read_b64_tr_b16 v[210:211], v241 offset:23040\n"
  "s_waitcnt lgkmcnt(9)\n"
  "v_mfma_f32_32x32x16_bf16 v[160:175], v[224:227], v[228:231], v[160:175]\n"
  "s_waitcnt lgkmcnt(6)\n"
  "v_mfma_f32_32x32x16_bf16 v[160:175], v[192:195], v[196:199], v[160:175]\n"
  "s_waitcnt lgkmcnt(3)\n"
  "v_mfma_f32_32x32x16_bf16 v[160:175], v[200:203], v[204:207], v[160:175]\n"
  "s_waitcnt lgkmcnt(0)\n"
  "v_mfma_f32_32x32x16_bf16 v[160:175], v[208:211], v[212:215], v[160:175]\n"
  "s_add_u32 s67, s56, s57\n"
  "s_bitcmp1_b32 s54, 0\n"
  "s_nop 9\n"
  "v_cvt_pk_bf16_f32 v216, v160, v161\n"
  "v_cvt_pk_bf16_f32 v217, v162, v163\n"
  "v_cvt_pk_bf16_f32 v218, v164, v165\n"
  "v_cvt_pk_bf16_f32 v219, v166, v167\n"
  "v_cvt_pk_bf16_f32 v220, v168, v169\n"
  "v_cvt_pk_bf16_f32 v221, v170, v171\n"
  "v_cvt_pk_bf16_f32 v222, v172, v173\n"
  "v_cvt_pk_bf16_f32 v223, v174, v175\n"
  "s_cbranch_scc1 LS0%=\n"
  "buffer_store_dwordx4 v[216:219], v243, s[44:47], s56 offen sc1\n"
  "buffer_store_dwordx4 v[220:223], v243, s[44:47], s67 offen sc1\n"
  "LS0%=:\n"
  "s_cmp_eq_u32 s54, 2\n"
  "s_cbranch_scc1 LT02%=\n"
  "s_cmp_eq_u32 s54, 1\n"
  "s_cbranch_scc1 LT01%=\n"
  "s_cmp_eq_u32 s54, 3\n"
  "s_cbranch_scc1 LT03%=\n"
  "s_waitcnt vmcnt(8) lgkmcnt(0)\n"
  "s_barrier\n"
  "ds_read_b128 v[192:195], v237 offset:32768\n"
  "ds_read_b64_tr_b16 v[196:197], v242 offset:40960\n"
  "ds_read_b64_tr_b16 v[198:199], v242 offset:41216\n"
  "ds_read_b128 v[200:203], v238 offset:32768\n"
  "ds_read_b64_tr_b16 v[204:205], v242 offset:41984\n"
  "ds_read_b64_tr_b16 v[206:207], v242 offset:42240\n"
  "ds_read_b128 v[208:211], v239 offset:32768\n"
  "ds_read_b64_tr_b16 v[212:213], v242 offset:43008\n"
  "ds_read_b64_tr_b16 v[214:215], v242 offset:43264\n"
  "ds_read_b128 v[224:227], v240 offset:32768\n"
  "ds_read_b64_tr_b16 v[228:229], v242 offset:44032\n"
  "ds_read_b64_tr_b16 v[230:231], v242 offset:44288\n"
  "v_mbcnt_lo_u32_b32 v254, -1, 0\n"
  "v_mbcnt_hi_u32_b32 v254, -1, v254\n"
  "v_bfe_u32 v255, v254, 4, 1\n"
  "v_mul_u32_u24 v232, s63, v255\n"
  "v_lshrrev_b32 v255, 5, v254\n"
  "v_lshl_add_u32 v232, v255, 14, v232\n"
  "v_bfe_u32 v255, v254, 2, 2\n"
  "v_lshl_add_u32 v232, v255, 7, v232\n"
  "v_add_u32 v232, s62, v232\n"
  "s_waitcnt vmcnt(5) lgkmcnt(0)\n"
  "s_barrier\n"
  "v_mfma_f32_32x32x16_bf16 v[160:175], v[0:3], v[192:195], 0\n"
  "ds_read_b128 v[216:219], v237 offset:45056\n"
  "ds_read_b64_tr_b16 v[220:221], v242 offset:53248\n"
  "ds_read_b64_tr_b16 v[222:223], v242 offset:53504\n"
  "v_mfma_f32_32x32x16_bf16 v[176:191], v[196:199], v[80:83], 0\n"
  "v_mfma_f32_32x32x16_bf16 v[160:175], v[4:7], v[200:203], v[160:175]\n"
  "ds_read_b128 v[192:195], v238 offset:45056\n"
  "ds_read_b64_tr_b16 v[196:197], v242 offset:54272\n"
  "ds_read_b64_tr_b16 v[198:199], v242 offset:54528\n"
  "v_mfma_f32_32x32x16_bf16 v[176:191], v[204:207], v[84:87], v[176:191]\n"
  "v_mfma_f32_32x32x16_bf16 v[160:175], v[8:11], v[208:211], v[160:175]\n"
  "ds_read_b128 v[80:83], v239 offset:45056\n"
  "ds_read_b64_tr_b16 v[200:201], v242 offset:55296\n"
  "ds_read_b64_tr_b16 v[202:203], v242 offset:55552\n"
  "v_mfma_f32_32x32x16_bf16 v[176:191], v[212:215], v[88:91], v[176:191]\n"
  "ds_read_b128 v[204:207], v240 offset:45056\n"
  "ds_read_b64_tr_b16 v[84:85], v242 offset:56320\n"
  "ds_read_b64_tr_b16 v[86:87], v242 offset:56576\n"
  "v_mfma_f32_32x32x16_bf16 v[160:175], v[12:15], v[224:227], v[160:175]\n"
  "v_mfma_f32_32x32x16_bf16 v[176:191], v[228:231], v[92:95], v[176:191]\n"
  "s_waitcnt vmcnt(0) lgkmcnt(0)\n"
  "s_barrier\n"
  "buffer_store_dwordx2 v[252:253], v244, s[44:47], 0 offen sc1\n"
  "v_mfma_f32_32x32x16_bf16 v[160:175], v[16:19], v[216:219], v[160:175]\n"
  "ds_read_b128 v[208:211], v237 offset:57344\n"
  "ds_read_b64_tr_b16 v[212:213], v250 offset:32768\n"
  "ds_read_b64_tr_b16 v[214:215], v250 offset:33024\n"
  "v_mfma_f32_32x32x16_bf16 v[176:191], v[220:223], v[96:99], v[176:191]\n"
  "ds_read_b128 v[88:91], v238 offset:57344\n"
  "ds_read_b64_tr_b16 v[224:225], v250 offset:33792\n"
  "ds_read_b64_tr_b16 v[226:227], v250 offset:34048\n"
  "v_mfma_f32_32x32x16_bf16 v[160:175], v[20:23], v[192:195], v[160:175]\n"
  "ds_read_b128 v[228:231], v239 offset:57344\n"
  "ds_read_b64_tr_b16 v[92:93], v250 offset:34816\n"
  "ds_read_b64_tr_b16 v[94:95], v250 offset:35072\n"
  "v_mfma_f32_32x32x16_bf16 v[176:191], v[196:199], v[100:103], v[176:191]\n"
  "ds_read_b128 v[216:219], v240 offset:57344\n"
  "ds_read_b64_tr_b16 v[220:221], v250 offset:35840\n"
  "ds_read_b64_tr_b16 v[222:223], v250 offset:36096\n"
  "v_mfma_f32_32x32x16_bf16 v[160:175], v[24:27], v[80:83], v[160:175]\n"
  "v_mov_b32 v8, 0x3f803f80\n"
  "v_mov_b32 v9, 0x3f803f80\n"
  "v_mov_b32 v10, 0x3f803f80\n"
  "v_mov_b32 v11, 0x3f803f80\n"
  "v_mfma_f32_32x32x16_bf16 v[176:191], v[200:203], v[104:107], v[176:191]\n"
  "v_mov_b32 v12, 0x3f803f80\n"
  "v_mov_b32 v13, 0x3f803f80\n"
  "v_mov_b32 v14, 0x3f803f80\n"
  "v_mov_b32 v15, 0x3f803f80\n"
  "v_mfma_f32_32x32x16_bf16 v[160:175], v[28:31], v[204:207], v[160:175]\n"
  "v_mfma_f32_32x32x16_bf16 v[176:191], v[84:87], v[108:111], v[176:191]\n"
  "s_waitcnt lgkmcnt(0)\n"
  "s_barrier\n"
  "v_mfma_f32_32x32x16_bf16 v[160:175], v[32:35], v[208:211], v[160:175]\n"
  "ds_read_b128 v[96:99], v245 offset:36864\n"
  "ds_read_b64_tr_b16 v[192:193], v250 offset:45056\n"
  "ds_read_b64_tr_b16 v[194:195], v250 offset:45312\n"
  "v_mfma_f32_32x32x16_bf16 v[176:191], v[212:215], v[112:115], v[176:191]\n"
  "ds_read_b128 v[196:199], v246 offset:36864\n"
  "ds_read_b64_tr_b16 v[100:101], v250 offset:46080\n"
  "ds_read_b64_tr_b16 v[102:103], v250 offset:46336\n"
  "v_mfma_f32_32x32x16_bf16 v[160:175], v[36:39], v[88:91], v[160:175]\n"
  "ds_read_b128 v[80:83], v247 offset:36864\n"
  "ds_read_b64_tr_b16 v[200:201], v250 offset:47104\n"
  "ds_read_b64_tr_b16 v[202:203], v250 offset:47360\n"
  "v_mfma_f32_32x32x16_bf16 v[176:191], v[224:227], v[116:119], v[176:191]\n"
  "ds_read_b128 v[104:107], v248 offset:36864\n"
  "ds_read_b64_tr_b16 v[204:205], v250 offset:48128\n"
  "ds_read_b64_tr_b16 v[206:207], v250 offset:48384\n"
  "v_mfma_f32_32x32x16_bf16 v[160:175], v[40:43], v[228:231], v[160:175]\n"
  "v_mov_b32 v24, 0x3f803f80\n"
  "v_mov_b32 v25, 0x3f803f80\n"
  "v_mov_b32 v26, 0x3f803f80\n"
  "v_mov_b32 v27, 0x3f803f80\n"
  "v_mfma_f32_32x32x16_bf16 v[176:191], v[92:95], v[120:123], v[176:191]\n"
  "v_mov_b32 v28, 0x3f803f80\n"
  "v_mov_b32 v29, 0x3f803f80\n"
  "v_mov_b32 v30, 0x3f803f80\n"
  "v_mov_b32 v31, 0x3f803f80\n"
  "v_mfma_f32_32x32x16_bf16 v[160:175], v[44:47], v[216:219], v[160:175]\n"
  "v_mfma_f32_32x32x16_bf16 v[176:191], v[220:223], v[124:127], v[176:191]\n"
  "s_waitcnt lgkmcnt(0)\n"
  "s_barrier\n"
  "v_mfma_f32_32x32x16_bf16 v[160:175], v[48:51], v[96:99], v[160:175]\n"
  "ds_read_b128 v[84:87], v237 offset:32768\n"
  "ds_read_b64_tr_b16 v[108:109], v242 offset:40960\n"
  "ds_read_b64_tr_b16 v[110:111], v242 offset:41216\n"
  "v_mfma_f32_32x32x16_bf16 v[176:191], v[192:195], v[128:131], v[176:191]\n"
  "ds_read_b128 v[208:211], v238 offset:32768\n"
  "ds_read_b64_tr_b16 v[212:213], v242 offset:41984\n"
  "ds_read_b64_tr_b16 v[214:215], v242 offset:42240\n"
  "v_mfma_f32_32x32x16_bf16 v[160:175], v[52:55], v[196:199], v[160:175]\n"
  "ds_read_b128 v[112:115], v239 offset:32768\n"
  "ds_read_b64_tr_b16 v[88:89], v242 offset:43008\n"
  "ds_read_b64_tr_b16 v[90:91], v242 offset:43264\n"
  "v_mfma_f32_32x32x16_bf16 v[176:191], v[100:103], v[132:135], v[176:191]\n"
  "ds_read_b128 v[224:227], v240 offset:32768\n"
  "ds_read_b64_tr_b16 v[116:117], v242 offset:44032\n"
  "ds_read_b64_tr_b16 v[118:119], v242 offset:44288\n"
  "v_mfma_f32_32x32x16_bf16 v[160:175], v[56:59], v[80:83], v[160:175]\n"
  "v_mbcnt_lo_u32_b32 v236, -1, 0\n"
  "v_mbcnt_hi_u32_b32 v236, -1, v236\n"
  "v_and_b32 v235, 31, v236\n"
  "v_lshrrev_b32 v236, 5, v236\n"
  "v_lshl_add_u32 v233, v235, 4, s58\n"
  "v_mad_i32_i24 v233, v236, s59, v233\n"
  "v_lshl_add_u32 v234, s59, 1, v233\n"
  "v_mad_i32_i24 v234, v236, s60, v234\n"
  "v_mfma_f32_32x32x16_bf16 v[176:191], v[200:203], v[136:139], v[176:191]\n"
  "v_lshl_add_u32 v235, v235, 4, s61\n"
  "v_mul_u32_u24 v236, 0x1c00, v236\n"
  "v_add_u32 v235, v235, v236\n"
  "v_add_u32 v236, 0x3800, v235\n"
  "s_mul_i32 s52, s54, 917504\n"
  "s_add_u32 s52, s52, 1048576\n"
  "s_add_u32 s6, s44, s52\n"
  "s_addc_u32 s7, s45, 0\n"
  "v_mfma_f32_32x32x16_bf16 v[160:175], v[60:63], v[104:107], v[160:175]\n"
  "s_mov_b32 s70, 0x0fffffff\n"
  "s_mov_b32 s71, 0x0fffffff\n"
  "v_mov_b32 v40, 0x3f803f80\n"
  "v_mov_b32 v41, 0x3f803f80\n"
  "v_mov_b32 v42, 0x3f803f80\n"
  "v_mov_b32 v43, 0x3f803f80\n"
  "v_mfma_f32_32x32x16_bf16 v[176:191], v[204:207], v[140:143], v[176:191]\n"
  "v_mov_b32 v44, 0x3f803f80\n"
  "v_mov_b32 v45, 0x3f803f80\n"
  "v_mov_b32 v46, 0x3f803f80\n"
  "v_mov_b32 v47, 0x3f803f80\n"
  "s_waitcnt lgkmcnt(0)\n"
  "s_barrier\n"
  "s_mov_b32 s28, 0x8000\n"
  "buffer_store_dwordx2 v[252:253], v244, s[44:47], s28 offen sc1\n"
  "v_mfma_f32_32x32x16_bf16 v[160:175], v[64:67], v[84:87], v[160:175]\n"
  "ds_read_b128 v[228:231], v237 offset:45056\n"
  "ds_read_b64_tr_b16 v[92:93], v242 offset:53248\n"
  "ds_read_b64_tr_b16 v[94:95], v242 offset:53504\n"
  "ds_read_b64_tr_b16 v[120:121], v241 offset:0\n"
  "ds_read_b64_tr_b16 v[122:123], v241 offset:512\n"
  "ds_read_b128 v[216:219], v237 offset:8192\n"
  "v_mfma_f32_32x32x16_bf16 v[176:191], v[108:111], v[144:147], v[176:191]\n"
  "ds_read_b128 v[220:223], v238 offset:45056\n"
  "ds_read_b64_tr_b16 v[124:125], v242 offset:54272\n"
  "ds_read_b64_tr_b16 v[126:127], v242 offset:54528\n"
  "ds_read_b64_tr_b16 v[96:97], v241 offset:2048\n"
  "ds_read_b64_tr_b16 v[98:99], v241 offset:2560\n"
  "ds_read_b128 v[192:195], v238 offset:8192\n"
  "v_mfma_f32_32x32x16_bf16 v[160:175], v[68:71], v[208:211], v[160:175]\n"
  "ds_read_b128 v[128:131], v239 offset:45056\n"
  "ds_read_b64_tr_b16 v[196:197], v242 offset:55296\n"
  "ds_read_b64_tr_b16 v[198:199], v242 offset:55552\n"
  "ds_read_b64_tr_b16 v[100:101], v241 offset:4096\n"
  "ds_read_b64_tr_b16 v[102:103], v241 offset:4608\n"
  "ds_read_b128 v[132:135], v239 offset:8192\n"
  "v_mfma_f32_32x32x16_bf16 v[176:191], v[212:215], v[148:151], v[176:191]\n"
  "ds_read_b128 v[80:83], v240 offset:45056\n"
  "ds_read_b64_tr_b16 v[200:201], v242 offset:56320\n"
  "ds_read_b64_tr_b16 v[202:203], v242 offset:56576\n"
  "ds_read_b64_tr_b16 v[136:137], v241 offset:6144\n"
  "ds_read_b64_tr_b16 v[138:139], v241 offset:6656\n"
  "ds_read_b128 v[104:107], v240 offset:8192\n"
  "v_mfma_f32_32x32x16_bf16 v[160:175], v[72:75], v[112:115], v[160:175]\n"
  "buffer_load_dwordx2 v[254:255], v232, s[44:47], 0 offen sc1\n"
  "v_mov_b32 v56, 0x3f803f80\n"
  "v_mov_b32 v57, 0x3f803f80\n"
  "v_mov_b32 v58, 0x3f803f80\n"
  "v_mov_b32 v59, 0x3f803f80\n"
  "v_mfma_f32_32x32x16_bf16 v[176:191], v[88:91], v[152:155], v[176:191]\n"
  "v_mov_b32 v60, 0x3f803f80\n"
  "v_mov_b32 v61, 0x3f803f80\n"
  "v_mov_b32 v62, 0x3f803f80\n"
  "v_mov_b32 v63, 0x3f803f80\n"
  "s_add_u32 s22, s54, 1\n"
  "s_and_b32 s22, s22, 3\n"
  "s_mul_i32 s55, s22, 12288\n"
  "s_add_u32 s23, s54, 2\n"
  "s_and_b32 s23, s23, 3\n"
  "s_mul_i32 s60, s23, 12288\n"
  "v_mfma_f32_32x32x16_bf16 v[160:175], v[76:79], v[224:227], v[160:175]\n"
  "s_add_u32 s60, s60, 4096\n"
  "s_add_u32 s21, s54, 3\n"
  "s_and_b32 s21, s21, 3\n"
  "s_mul_i32 s61, s21, 12288\n"
  "s_add_u32 s61, s61, 8192\n"
  "s_mul_i32 s63, s54, 12288\n"
  "s_mul_i32 s28, s54, 7168\n"
  "s_add_u32 s64, s64, s28\n"
  "s_lshl_b32 s28, s24, 6\n"
  "s_add_u32 s28, s28, s20\n"
  "s_lshl_b32 s28, s28, 7\n"
  "s_add_u32 s62, s28, 69632\n"
  "v_mfma_f32_32x32x16_bf16 v[176:191], v[116:119], v[156:159], v[176:191]\n"
  "s_mov_b32 s67, 0x8000\n"
  "s_mov_b32 s29, 0x3e8b97f6\n"
  "s_mov_b32 s4, 0\n"
  "s_mov_b32 s5, -1\n"
  "s_mov_b32 s28, 0x8000\n"
  "v_mov_b32 v48, 0\n"
  "v_mov_b32 v49, 0\n"
  "v_mov_b32 v50, 0\n"
  "v_mov_b32 v51, 0\n"
  "s_waitcnt lgkmcnt(0)\n"
  "s_barrier\n"
  "v_mfma_f32_32x32x16_bf16 v[160:175], v[120:123], v[228:231], v[160:175]\n"
  "ds_read_b128 v[204:207], v237 offset:57344\n"
  "ds_read_b64_tr_b16 v[140:141], v250 offset:32768\n"
  "ds_read_b64_tr_b16 v[142:143], v250 offset:33024\n"
  "ds_read_b64_tr_b16 v[64:65], v241 offset:16384\n"
  "ds_read_b64_tr_b16 v[66:67], v241 offset:16896\n"
  "ds_read_b128 v[84:87], v237 offset:24576\n"
  "v_mfma_f32_32x32x16_bf16 v[176:191], v[92:95], v[216:219], v[176:191]\n"
  "ds_read_b128 v[108:111], v238 offset:57344\n"
  "ds_read_b64_tr_b16 v[144:145], v250 offset:33792\n"
  "ds_read_b64_tr_b16 v[146:147], v250 offset:34048\n"
  "ds_read_b64_tr_b16 v[68:69], v241 offset:18432\n"
  "ds_read_b64_tr_b16 v[70:71], v241 offset:18944\n"
  "ds_read_b128 v[208:211], v238 offset:24576\n"
  "v_mfma_f32_32x32x16_bf16 v[160:175], v[96:99], v[220:223], v[160:175]\n"
  "ds_read_b128 v[212:215], v239 offset:57344\n"
  "ds_read_b64_tr_b16 v[148:149], v250 offset:34816\n"
  "ds_read_b64_tr_b16 v[150:151], v250 offset:35072\n"
  "ds_read_b64_tr_b16 v[72:73], v241 offset:20480\n"
  "ds_read_b64_tr_b16 v[74:75], v241 offset:20992\n"
  "ds_read_b128 v[112:115], v239 offset:24576\n"
  "v_mfma_f32_32x32x16_bf16 v[176:191], v[124:127], v[192:195], v[176:191]\n"
  "ds_read_b128 v[88:91], v240 offset:57344\n"
  "ds_read_b64_tr_b16 v[152:153], v250 offset:35840\n"
  "ds_read_b64_tr_b16 v[154:155], v250 offset:36096\n"
  "ds_read_b64_tr_b16 v[76:77], v241 offset:22528\n"
  "ds_read_b64_tr_b16 v[78:79], v241 offset:23040\n"
  "ds_read_b128 v[224:227], v240 offset:24576\n"
  "v_mfma_f32_32x32x16_bf16 v[160:175], v[100:103], v[128:131], v[160:175]\n"
  "v_and_b32 v238, 0x3f0, v251\n"
  "v_lshl_add_u32 v249, s22, 11, v238\n"
  "v_lshl_add_u32 v250, s23, 11, v238\n"
  "v_lshl_add_u32 v241, s21, 11, v238\n"
  "v_add_u32 v245, s55, v238\n"
  "v_mfma_f32_32x32x16_bf16 v[176:191], v[196:199], v[132:135], v[176:191]\n"
  "v_add_u32 v246, s60, v238\n"
  "v_add_u32 v247, s61, v238\n"
  "v_add_u32 v248, s63, v238\n"
  "v_mov_b32 v237, 0xdddddddd\n"
  "v_lshrrev_b32 v240, 7, v238\n"
  "v_mfma_f32_32x32x16_bf16 v[160:175], v[136:139], v[80:83], v[160:175]\n"
  "v_lshrrev_b32 v242, 2, v238\n"
  "v_and_b32 v242, 64, v242\n"
  "v_and_b32 v239, 0x70, v238\n"
  "v_mul_u32_u24 v240, 0x380, v240\n"
  "v_xad_u32 v239, v239, v242, v240\n"
  "v_mfma_f32_32x32x16_bf16 v[176:191], v[200:203], v[104:107], v[176:191]\n"
  "v_add_u32 v239, s64, v239\n"
  "v_bfe_u32 v240, v238, 4, 3\n"
  "v_and_b32 v242, 4, v240\n"
  "v_add_lshl_u32 v240, v240, v242, 11\n"
  "s_waitcnt vmcnt(0)\n"
  "v_xor_b32 v254, v254, v252\n"
  "v_xor_b32 v255, v255, v253\n"
  "v_or_b32 v254, v254, v255\n"
  "v_cmp_ne_u32 vcc, 0, v254\n"
  "s_cbranch_vccz LDa000%=\n"
  "s_mov_b32 s69, 0x40000\n"
  "LPa000%=:\n"
  "s_sleep 1\n"
  "buffer_load_dwordx2 v[254:255], v232, s[44:47], 0 offen sc1\n"
  "s_waitcnt vmcnt(0)\n"
  "v_xor_b32 v254, v254, v252\n"
  "v_xor_b32 v255, v255, v253\n"
  "v_or_b32 v254, v254, v255\n"
  "v_cmp_ne_u32 vcc, 0, v254\n"
  "s_cbranch_vccz LDa000%=\n"
  "s_sub_u32 s69, s69, 1\n"
  "s_cmp_lg_u32 s69, 0\n"
  "s_cbranch_scc1 LPa000%=\n"
  "LDa000%=:\n"
  "s_add_u32 s30, s6, 0\n"
  "s_addc_u32 s31, s7, 0\n"
  "global_load_dwordx4 v[0:3], v233, s[30:31] sc1\n"
  "global_load_dwordx4 v[4:7], v234, s[30:31] sc1\n"
  "s_waitcnt lgkmcnt(14)\n"
  "v_mfma_f32_32x32x16_bf16 v[176:191], v[140:143], v[84:87], v[176:191]\n"
  "s_add_u32 s30, s6, 229376\n"
  "s_addc_u32 s31, s7, 0\n"
  "global_load_dwordx4 v[16:19], v233, s[30:31] sc1\n"
  "global_load_dwordx4 v[20:23], v234, s[30:31] sc1\n"
  "s_waitcnt lgkmcnt(12)\n"
  "v_mfma_f32_32x32x16_bf16 v[176:191], v[144:147], v[208:211], v[176:191]\n"
  "s_add_u32 s30, s6, 0\n"
  "s_addc_u32 s31, s7, 0\n"
  "s_mov_b64 exec, s[70:71]\n"
  "global_load_dwordx4 v[8:11], v235, s[30:31] sc1\n"
  "global_load_dwordx4 v[12:15], v236, s[30:31] sc1\n"
  "s_mov_b64 exec, -1\n"
  "s_waitcnt lgkmcnt(6)\n"
  "v_mfma_f32_32x32x16_bf16 v[176:191], v[148:151], v[112:115], v[176:191]\n"
  "s_add_u32 s30, s6, 229376\n"
  "s_addc_u32 s31, s7, 0\n"
  "s_mov_b64 exec, s[70:71]\n"
  "global_load_dwordx4 v[24:27], v235, s[30:31] sc1\n"
  "global_load_dwordx4 v[28:31], v236, s[30:31] sc1\n"
  "s_mov_b64 exec, -1\n"
  "s_waitcnt lgkmcnt(0)\n"
  "v_mfma_f32_32x32x16_bf16 v[176:191], v[152:155], v[224:227], v[176:191]\n"
  "v_mfma_f32_32x32x16_bf16 v[160:175], v[64:67], v[204:207], v[160:175]\n"
  "v_mfma_f32_32x32x16_bf16 v[160:175], v[68:71], v[108:111], v[160:175]\n"
  "s_nop 9\n"
  "v_mul_f32 v176, s29, v176\n"
  "v_mul_f32 v177, s29, v177\n"
  "v_mul_f32 v178, s29, v178\n"
  "v_mul_f32 v179, s29, v179\n"
  "v_mul_f32 v180, s29, v180\n"
  "v_mul_f32 v181, s29, v181\n"
  "v_mfma_f32_32x32x16_bf16 v[160:175], v[72:75], v[212:215], v[160:175]\n"
  "v_mul_f32 v182, s29, v182\n"
  "v_mul_f32 v183, s29, v183\n"
  "v_mul_f32 v184, s29, v184\n"
  "v_mul_f32 v185, s29, v185\n"
  "v_mul_f32 v186, s29, v186\n"
  "v_mul_f32 v187, s29, v187\n"
  "v_mfma_f32_32x32x16_bf16 v[160:175], v[76:79], v[88:91], v[160:175]\n"
  "v_mul_f32 v188, s29, v188\n"
  "v_mul_f32 v189, s29, v189\n"
  "v_mul_f32 v190, s29, v190\n"
  "v_mul_f32 v191, s29, v191\n"
  "v_cvt_pk_bf16_f32 v176, v176, v177\n"
  "v_cvt_pk_bf16_f32 v177, v178, v179\n"
  "v_mfma_f32_32x32x16_bf16 v[96:111], v[48:51], v[48:51], 0\n"
  "v_cvt_pk_bf16_f32 v178, v180, v181\n"
  "v_cvt_pk_bf16_f32 v179, v182, v183\n"
  "v_cvt_pk_bf16_f32 v180, v184, v185\n"
  "v_cvt_pk_bf16_f32 v181, v186, v187\n"
  "v_cvt_pk_bf16_f32 v182, v188, v189\n"
  "v_cvt_pk_bf16_f32 v183, v190, v191\n"
  "v_mfma_f32_32x32x16_bf16 v[112:127], v[48:51], v[48:51], 0\n"
  "ds_write_b128 v251, v[176:179]\n"
  "v_cndmask_b32_e64 v182, v182, 0, s[4:5]\n"
  "v_cndmask_b32_e64 v183, v183, 0, s[4:5]\n"
  "ds_write_b128 v251, v[180:183] offset:1024\n"
  "v_mfma_f32_32x32x16_bf16 v[128:143], v[48:51], v[48:51], 0\n"
  "v_mfma_f32_32x32x16_bf16 v[144:159], v[48:51], v[48:51], 0\n"
  "s_branch LE%=\n"
  "LT02%=:\n"
  "s_waitcnt vmcnt(8) lgkmcnt(0)\n"
  "s_barrier\n"
  "ds_read_b128 v[192:195], v237 offset:32768\n"
  "ds_read_b64_tr_b16 v[196:197], v242 offset:40960\n"
  "ds_read_b64_tr_b16 v[198:199], v242 offset:41216\n"
  "ds_read_b128 v[200:203], v238 offset:32768\n"
  "ds_read_b64_tr_b16 v[204:205], v242 offset:41984\n"
  "ds_read_b64_tr_b16 v[206:207], v242 offset:42240\n"
  "ds_read_b128 v[208:211], v239 offset:32768\n"
  "ds_read_b64_tr_b16 v[212:213], v242 offset:43008\n"
  "ds_read_b64_tr_b16 v[214:215], v242 offset:43264\n"
  "ds_read_b128 v[224:227], v240 offset:32768\n"
  "ds_read_b64_tr_b16 v[228:229], v242 offset:44032\n"
  "ds_read_b64_tr_b16 v[230:231], v242 offset:44288\n"
  "v_mbcnt_lo_u32_b32 v254, -1, 0\n"
  "v_mbcnt_hi_u32_b32 v254, -1, v254\n"
  "v_bfe_u32 v255, v254, 4, 1\n"
  "v_mul_u32_u24 v232, s63, v255\n"
  "v_lshrrev_b32 v255, 5, v254\n"
  "v_lshl_add_u32 v232, v255, 14, v232\n"
  "v_bfe_u32 v255, v254, 2, 2\n"
  "v_lshl_add_u32 v232, v255, 7, v232\n"
  "v_add_u32 v232, s62, v232\n"
  "s_waitcnt vmcnt(5) lgkmcnt(0)\n"
  "s_barrier\n"
  "v_mfma_f32_32x32x16_bf16 v[160:175], v[0:3], v[192:195], 0\n"
  "ds_read_b128 v[216:219], v237 offset:45056\n"
  "ds_read_b64_tr_b16 v[220:221], v242 offset:53248\n"
  "ds_read_b64_tr_b16 v[222:223], v242 offset:53504\n"
  "v_mfma_f32_32x32x16_bf16 v[176:191], v[196:199], v[192:195], 0\n"
  "ds_read_b128 v[80:83], v238 offset:45056\n"
  "ds_read_b64_tr_b16 v[84:85], v242 offset:54272\n"
  "ds_read_b64_tr_b16 v[86:87], v242 offset:54528\n"
  "v_mfma_f32_32x32x16_bf16 v[160:175], v[4:7], v[200:203], v[160:175]\n"
  "ds_read_b128 v[88:91], v239 offset:45056\n"
  "ds_read_b64_tr_b16 v[92:93], v242 offset:55296\n"
  "ds_read_b64_tr_b16 v[94:95], v242 offset:55552\n"
  "v_mfma_f32_32x32x16_bf16 v[176:191], v[204:207], v[200:203], v[176:191]\n"
  "ds_read_b128 v[96:99], v240 offset:45056\n"
  "ds_read_b64_tr_b16 v[100:101], v242 offset:56320\n"
  "ds_read_b64_tr_b16 v[102:103], v242 offset:56576\n"
  "v_mfma_f32_32x32x16_bf16 v[160:175], v[8:11], v[208:211], v[160:175]\n"
  "v_mfma_f32_32x32x16_bf16 v[176:191], v[212:215], v[208:211], v[176:191]\n"
  "v_mfma_f32_32x32x16_bf16 v[160:175], v[12:15], v[224:227], v[160:175]\n"
  "v_mfma_f32_32x32x16_bf16 v[176:191], v[228:231], v[224:227], v[176:191]\n"
  "s_waitcnt vmcnt(0) lgkmcnt(0)\n"
  "s_barrier\n"
  "v_mfma_f32_32x32x16_bf16 v[160:175], v[16:19], v[216:219], v[160:175]\n"
  "ds_read_b128 v[104:107], v237 offset:57344\n"
  "ds_read_b64_tr_b16 v[108:109], v250 offset:32768\n"
  "ds_read_b64_tr_b16 v[110:111], v250 offset:33024\n"
  "v_mfma_f32_32x32x16_bf16 v[176:191], v[220:223], v[216:219], v[176:191]\n"
  "ds_read_b128 v[112:115], v238 offset:57344\n"
  "ds_read_b64_tr_b16 v[116:117], v250 offset:33792\n"
  "ds_read_b64_tr_b16 v[118:119], v250 offset:34048\n"
  "v_mfma_f32_32x32x16_bf16 v[160:175], v[20:23], v[80:83], v[160:175]\n"
  "ds_read_b128 v[120:123], v239 offset:57344\n"
  "ds_read_b64_tr_b16 v[124:125], v250 offset:34816\n"
  "ds_read_b64_tr_b16 v[126:127], v250 offset:35072\n"
  "v_mfma_f32_32x32x16_bf16 v[176:191], v[84:87], v[80:83], v[176:191]\n"
  "ds_read_b128 v[128:131], v240 offset:57344\n"
  "ds_read_b64_tr_b16 v[132:133], v250 offset:35840\n"
  "ds_read_b64_tr_b16 v[134:135], v250 offset:36096\n"
  "v_mfma_f32_32x32x16_bf16 v[160:175], v[24:27], v[88:91], v[160:175]\n"
  "v_mov_b32 v8, 0x3f803f80\n"
  "v_mov_b32 v9, 0x3f803f80\n"
  "v_mov_b32 v10, 0x3f803f80\n"
  "v_mov_b32 v11, 0x3f803f80\n"
  "v_mfma_f32_32x32x16_bf16 v[176:191], v[92:95], v[88:91], v[176:191]\n"
  "v_mov_b32 v12, 0x3f803f80\n"
  "v_mov_b32 v13, 0x3f803f80\n"
  "v_mov_b32 v14, 0x3f803f80\n"
  "v_mov_b32 v15, 0x3f803f80\n"
  "v_mfma_f32_32x32x16_bf16 v[160:175], v[28:31], v[96:99], v[160:175]\n"
  "v_mfma_f32_32x32x16_bf16 v[176:191], v[100:103], v[96:99], v[176:191]\n"
  "s_waitcnt lgkmcnt(0)\n"
  "s_barrier\n"
  "v_mfma_f32_32x32x16_bf16 v[160:175], v[32:35], v[104:107], v[160:175]\n"
  "ds_read_b128 v[136:139], v245 offset:36864\n"
  "ds_read_b64_tr_b16 v[140:141], v250 offset:45056\n"
  "ds_read_b64_tr_b16 v[142:143], v250 offset:45312\n"
  "v_mfma_f32_32x32x16_bf16 v[176:191], v[108:111], v[104:107], v[176:191]\n"
  "ds_read_b128 v[144:147], v246 offset:36864\n"
  "ds_read_b64_tr_b16 v[148:149], v250 offset:46080\n"
  "ds_read_b64_tr_b16 v[150:151], v250 offset:46336\n"
  "v_mfma_f32_32x32x16_bf16 v[160:175], v[36:39], v[112:115], v[160:175]\n"
  "ds_read_b128 v[152:155], v247 offset:36864\n"
  "ds_read_b64_tr_b16 v[156:157], v250 offset:47104\n"
  "ds_read_b64_tr_b16 v[158:159], v250 offset:47360\n"
  "v_mfma_f32_32x32x16_bf16 v[176:191], v[116:119], v[112:115], v[176:191]\n"
  "ds_read_b128 v[192:195], v248 offset:36864\n"
  "ds_read_b64_tr_b16 v[196:197], v250 offset:48128\n"
  "ds_read_b64_tr_b16 v[198:199], v250 offset:48384\n"
  "v_mfma_f32_32x32x16_bf16 v[160:175], v[40:43], v[120:123], v[160:175]\n"
  "v_mov_b32 v24, 0x3f803f80\n"
  "v_mov_b32 v25, 0x3f803f80\n"
  "v_mov_b32 v26, 0x3f803f80\n"
  "v_mov_b32 v27, 0x3f803f80\n"
  "v_mfma_f32_32x32x16_bf16 v[176:191], v[124:127], v[120:123], v[176:191]\n"
  "v_mov_b32 v28, 0x3f803f80\n"
  "v_mov_b32 v29, 0x3f803f80\n"
  "v_mov_b32 v30, 0x3f803f80\n"
  "v_mov_b32 v31, 0x3f803f80\n"
  "v_mfma_f32_32x32x16_bf16 v[160:175], v[44:47], v[128:131], v[160:175]\n"
  "v_mfma_f32_32x32x16_bf16 v[176:191], v[132:135], v[128:131], v[176:191]\n"
  "s_waitcnt lgkmcnt(0)\n"
  "s_barrier\n"
  "v_mfma_f32_32x32x16_bf16 v[160:175], v[48:51], v[136:139], v[160:175]\n"
  "ds_read_b128 v[200:203], v237 offset:32768\n"
  "ds_read_b64_tr_b16 v[204:205], v242 offset:40960\n"
  "ds_read_b64_tr_b16 v[206:207], v242 offset:41216\n"
  "v_mfma_f32_32x32x16_bf16 v[176:191], v[140:143], v[136:139], v[176:191]\n"
  "ds_read_b128 v[208:211], v238 offset:32768\n"
  "ds_read_b64_tr_b16 v[212:213], v242 offset:41984\n"
  "ds_read_b64_tr_b16 v[214:215], v242 offset:42240\n"
  "v_mfma_f32_32x32x16_bf16 v[160:175], v[52:55], v[144:147], v[160:175]\n"
  "ds_read_b128 v[224:227], v239 offset:32768\n"
  "ds_read_b64_tr_b16 v[228:229], v242 offset:43008\n"
  "ds_read_b64_tr_b16 v[230:231], v242 offset:43264\n"
  "v_mfma_f32_32x32x16_bf16 v[176:191], v[148:151], v[144:147], v[176:191]\n"
  "ds_read_b128 v[216:219], v240 offset:32768\n"
  "ds_read_b64_tr_b16 v[220:221], v242 offset:44032\n"
  "ds_read_b64_tr_b16 v[222:223], v242 offset:44288\n"
  "v_mfma_f32_32x32x16_bf16 v[160:175], v[56:59], v[152:155], v[160:175]\n"
  "v_mbcnt_lo_u32_b32 v236, -1, 0\n"
  "v_mbcnt_hi_u32_b32 v236, -1, v236\n"
  "v_and_b32 v235, 31, v236\n"
  "v_lshrrev_b32 v236, 5, v236\n"
  "v_lshl_add_u32 v233, v235, 4, s58\n"
  "v_mad_i32_i24 v233, v236, s59, v233\n"
  "v_lshl_add_u32 v234, s59, 1, v233\n"
  "v_mad_i32_i24 v234, v236, s60, v234\n"
  "v_mfma_f32_32x32x16_bf16 v[176:191], v[156:159], v[152:155], v[176:191]\n"
  "v_lshl_add_u32 v235, v235, 4, s61\n"
  "v_mul_u32_u24 v236, 0x1c00, v236\n"
  "v_add_u32 v235, v235, v236\n"
  "v_add_u32 v236, 0x3800, v235\n"
  "s_mul_i32 s52, s54, 917504\n"
  "s_add_u32 s52, s52, 1048576\n"
  "s_add_u32 s6, s44, s52\n"
  "s_addc_u32 s7, s45, 0\n"
  "v_mfma_f32_32x32x16_bf16 v[160:175], v[60:63], v[192:195], v[160:175]\n"
  "s_mov_b32 s70, 0x0fffffff\n"
  "s_mov_b32 s71, 0x0fffffff\n"
  "v_mov_b32 v40, 0x3f803f80\n"
  "v_mov_b32 v41, 0x3f803f80\n"
  "v_mov_b32 v42, 0x3f803f80\n"
  "v_mov_b32 v43, 0x3f803f80\n"
  "v_mfma_f32_32x32x16_bf16 v[176:191], v[196:199], v[192:195], v[176:191]\n"
  "v_mov_b32 v44, 0x3f803f80\n"
  "v_mov_b32 v45, 0x3f803f80\n"
  "v_mov_b32 v46, 0x3f803f80\n"
  "v_mov_b32 v47, 0x3f803f80\n"
  "s_waitcnt lgkmcnt(0)\n"
  "s_barrier\n"
  "v_mfma_f32_32x32x16_bf16 v[160:175], v[64:67], v[200:203], v[160:175]\n"
  "ds_read_b128 v[80:83], v237 offset:45056\n"
  "ds_read_b64_tr_b16 v[84:85], v242 offset:53248\n"
  "ds_read_b64_tr_b16 v[86:87], v242 offset:53504\n"
  "ds_read_b64_tr_b16 v[88:89], v241 offset:0\n"
  "ds_read_b64_tr_b16 v[90:91], v241 offset:512\n"
  "v_mfma_f32_32x32x16_bf16 v[176:191], v[204:207], v[200:203], v[176:191]\n"
  "ds_read_b128 v[92:95], v238 offset:45056\n"
  "ds_read_b64_tr_b16 v[96:97], v242 offset:54272\n"
  "ds_read_b64_tr_b16 v[98:99], v242 offset:54528\n"
  "ds_read_b64_tr_b16 v[100:101], v241 offset:2048\n"
  "ds_read_b64_tr_b16 v[102:103], v241 offset:2560\n"
  "v_mfma_f32_32x32x16_bf16 v[160:175], v[68:71], v[208:211], v[160:175]\n"
  "ds_read_b128 v[104:107], v239 offset:45056\n"
  "ds_read_b64_tr_b16 v[108:109], v242 offset:55296\n"
  "ds_read_b64_tr_b16 v[110:111], v242 offset:55552\n"
  "ds_read_b64_tr_b16 v[112:113], v241 offset:4096\n"
  "ds_read_b64_tr_b16 v[114:115], v241 offset:4608\n"
  "v_mfma_f32_32x32x16_bf16 v[176:191], v[212:215], v[208:211], v[176:191]\n"
  "ds_read_b128 v[116:119], v240 offset:45056\n"
  "ds_read_b64_tr_b16 v[120:121], v242 offset:56320\n"
  "ds_read_b64_tr_b16 v[122:123], v242 offset:56576\n"
  "ds_read_b64_tr_b16 v[124:125], v241 offset:6144\n"
  "ds_read_b64_tr_b16 v[126:127], v241 offset:6656\n"
  "v_mfma_f32_32x32x16_bf16 v[160:175], v[72:75], v[224:227], v[160:175]\n"
  "buffer_load_dwordx2 v[254:255], v232, s[44:47], 0 offen sc1\n"
  "v_mov_b32 v56, 0x3f803f80\n"
  "v_mov_b32 v57, 0x3f803f80\n"
  "v_mov_b32 v58, 0x3f803f80\n"
  "v_mov_b32 v59, 0x3f803f80\n"
  "v_mfma_f32_32x32x16_bf16 v[176:191], v[228:231], v[224:227], v[176:191]\n"
  "v_mov_b32 v60, 0x3f803f80\n"
  "v_mov_b32 v61, 0x3f803f80\n"
  "v_mov_b32 v62, 0x3f803f80\n"
  "v_mov_b32 v63, 0x3f803f80\n"
  "s_add_u32 s22, s54, 1\n"
  "s_and_b32 s22, s22, 3\n"
  "s_mul_i32 s55, s22, 12288\n"
  "s_add_u32 s23, s54, 2\n"
  "s_and_b32 s23, s23, 3\n"
  "s_mul_i32 s60, s23, 12288\n"
  "v_mfma_f32_32x32x16_bf16 v[160:175], v[76:79], v[216:219], v[160:175]\n"
  "s_add_u32 s60, s60, 4096\n"
  "s_add_u32 s21, s54, 3\n"
  "s_and_b32 s21, s21, 3\n"
  "s_mul_i32 s61, s21, 12288\n"
  "s_add_u32 s61, s61, 8192\n"
  "s_mul_i32 s63, s54, 12288\n"
  "s_mul_i32 s28, s54, 7168\n"
  "s_add_u32 s64, s64, s28\n"
  "s_lshl_b32 s28, s24, 6\n"
  "s_add_u32 s28, s28, s20\n"
  "s_lshl_b32 s28, s28, 7\n"
  "s_add_u32 s62, s28, 69632\n"
  "v_mfma_f32_32x32x16_bf16 v[176:191], v[220:223], v[216:219], v[176:191]\n"
  "s_mov_b32 s67, 0x8000\n"
  "s_mov_b32 s29, 0x3e8b97f6\n"
  "s_mov_b32 s4, 0\n"
  "s_mov_b32 s5, -1\n"
  "s_mov_b32 s28, 0x8000\n"
  "v_mov_b32 v48, 0\n"
  "v_mov_b32 v49, 0\n"
  "v_mov_b32 v50, 0\n"
  "v_mov_b32 v51, 0\n"
  "s_waitcnt lgkmcnt(0)\n"
  "s_barrier\n"
  "v_mfma_f32_32x32x16_bf16 v[160:175], v[88:91], v[80:83], v[160:175]\n"
  "ds_read_b128 v[128:131], v237 offset:57344\n"
  "ds_read_b64_tr_b16 v[132:133], v250 offset:32768\n"
  "ds_read_b64_tr_b16 v[134:135], v250 offset:33024\n"
  "ds_read_b64_tr_b16 v[136:137], v241 offset:16384\n"
  "ds_read_b64_tr_b16 v[138:139], v241 offset:16896\n"
  "v_mfma_f32_32x32x16_bf16 v[176:191], v[84:87], v[80:83], v[176:191]\n"
  "ds_read_b128 v[140:143], v238 offset:57344\n"
  "ds_read_b64_tr_b16 v[144:145], v250 offset:33792\n"
  "ds_read_b64_tr_b16 v[146:147], v250 offset:34048\n"
  "ds_read_b64_tr_b16 v[148:149], v241 offset:18432\n"
  "ds_read_b64_tr_b16 v[150:151], v241 offset:18944\n"
  "v_mfma_f32_32x32x16_bf16 v[160:175], v[100:103], v[92:95], v[160:175]\n"
  "ds_read_b128 v[152:155], v239 offset:57344\n"
  "ds_read_b64_tr_b16 v[156:157], v250 offset:34816\n"
  "ds_read_b64_tr_b16 v[158:159], v250 offset:35072\n"
  "ds_read_b64_tr_b16 v[192:193], v241 offset:20480\n"
  "ds_read_b64_tr_b16 v[194:195], v241 offset:20992\n"
  "v_mfma_f32_32x32x16_bf16 v[176:191], v[96:99], v[92:95], v[176:191]\n"
  "ds_read_b128 v[196:199], v240 offset:57344\n"
  "ds_read_b64_tr_b16 v[64:65], v250 offset:35840\n"
  "ds_read_b64_tr_b16 v[66:67], v250 offset:36096\n"
  "ds_read_b64_tr_b16 v[200:201], v241 offset:22528\n"
  "ds_read_b64_tr_b16 v[202:203], v241 offset:23040\n"
  "v_mfma_f32_32x32x16_bf16 v[160:175], v[112:115], v[104:107], v[160:175]\n"
  "v_and_b32 v238, 0x3f0, v251\n"
  "v_lshl_add_u32 v249, s22, 11, v238\n"
  "v_lshl_add_u32 v250, s23, 11, v238\n"
  "v_lshl_add_u32 v241, s21, 11, v238\n"
  "v_add_u32 v245, s55, v238\n"
  "v_mfma_f32_32x32x16_bf16 v[176:191], v[108:111], v[104:107], v[176:191]\n"
  "v_add_u32 v246, s60, v238\n"
  "v_add_u32 v247, s61, v238\n"
  "v_add_u32 v248, s63, v238\n"
  "v_mov_b32 v237, 0xdddddddd\n"
  "v_lshrrev_b32 v240, 7, v238\n"
  "v_mfma_f32_32x32x16_bf16 v[160:175], v[124:127], v[116:119], v[160:175]\n"
  "v_lshrrev_b32 v242, 2, v238\n"
  "v_and_b32 v242, 64, v242\n"
  "v_and_b32 v239, 0x70, v238\n"
  "v_mul_u32_u24 v240, 0x380, v240\n"
  "v_xad_u32 v239, v239, v242, v240\n"
  "v_mfma_f32_32x32x16_bf16 v[176:191], v[120:123], v[116:119], v[176:191]\n"
  "v_add_u32 v239, s64, v239\n"
  "v_bfe_u32 v240, v238, 4, 3\n"
  "v_and_b32 v242, 4, v240\n"
  "v_add_lshl_u32 v240, v240, v242, 11\n"
  "s_waitcnt vmcnt(0)\n"
  "v_xor_b32 v254, v254, v252\n"
  "v_xor_b32 v255, v255, v253\n"
  "v_or_b32 v254, v254, v255\n"
  "v_cmp_ne_u32 vcc, 0, v254\n"
  "s_cbranch_vccz LDa010%=\n"
  "s_mov_b32 s69, 0x40000\n"
  "LPa010%=:\n"
  "s_sleep 1\n"
  "buffer_load_dwordx2 v[254:255], v232, s[44:47], 0 offen sc1\n"
  "s_waitcnt vmcnt(0)\n"
  "v_xor_b32 v254, v254, v252\n"
  "v_xor_b32 v255, v255, v253\n"
  "v_or_b32 v254, v254, v255\n"
  "v_cmp_ne_u32 vcc, 0, v254\n"
  "s_cbranch_vccz LDa010%=\n"
  "s_sub_u32 s69, s69, 1\n"
  "s_cmp_lg_u32 s69, 0\n"
  "s_cbranch_scc1 LPa010%=\n"
  "LDa010%=:\n"
  "s_add_u32 s30, s6, 0\n"
  "s_addc_u32 s31, s7, 0\n"
  "global_load_dwordx4 v[0:3], v233, s[30:31] sc1\n"
  "global_load_dwordx4 v[4:7], v234, s[30:31] sc1\n"
  "s_waitcnt lgkmcnt(14)\n"
  "v_mfma_f32_32x32x16_bf16 v[176:191], v[132:135], v[128:131], v[176:191]\n"
  "s_add_u32 s30, s6, 229376\n"
  "s_addc_u32 s31, s7, 0\n"
  "global_load_dwordx4 v[16:19], v233, s[30:31] sc1\n"
  "global_load_dwordx4 v[20:23], v234, s[30:31] sc1\n"
  "s_waitcnt lgkmcnt(10)\n"
  "v_mfma_f32_32x32x16_bf16 v[176:191], v[144:147], v[140:143], v[176:191]\n"
  "s_add_u32 s30, s6, 0\n"
  "s_addc_u32 s31, s7, 0\n"
  "s_mov_b64 exec, s[70:71]\n"
  "global_load_dwordx4 v[8:11], v235, s[30:31] sc1\n"
  "global_load_dwordx4 v[12:15], v236, s[30:31] sc1\n"
  "s_mov_b64 exec, -1\n"
  "s_waitcnt lgkmcnt(5)\n"
  "v_mfma_f32_32x32x16_bf16 v[176:191], v[156:159], v[152:155], v[176:191]\n"
  "s_add_u32 s30, s6, 229376\n"
  "s_addc_u32 s31, s7, 0\n"
  "s_mov_b64 exec, s[70:71]\n"
  "global_load_dwordx4 v[24:27], v235, s[30:31] sc1\n"
  "global_load_dwordx4 v[28:31], v236, s[30:31] sc1\n"
  "s_mov_b64 exec, -1\n"
  "s_waitcnt lgkmcnt(0)\n"
  "v_mfma_f32_32x32x16_bf16 v[176:191], v[64:67], v[196:199], v[176:191]\n"
  "v_mfma_f32_32x32x16_bf16 v[160:175], v[136:139], v[128:131], v[160:175]\n"
  "v_mfma_f32_32x32x16_bf16 v[160:175], v[148:151], v[140:143], v[160:175]\n"
  "s_nop 9\n"
  "v_mul_f32 v176, s29, v176\n"
  "v_mul_f32 v177, s29, v177\n"
  "v_mul_f32 v178, s29, v178\n"
  "v_mul_f32 v179, s29, v179\n"
  "v_mul_f32 v180, s29, v180\n"
  "v_mul_f32 v181, s29, v181\n"
  "v_mfma_f32_32x32x16_bf16 v[160:175], v[192:195], v[152:155], v[160:175]\n"
  "v_mul_f32 v182, s29, v182\n"
  "v_mul_f32 v183, s29, v183\n"
  "v_mul_f32 v184, s29, v184\n"
  "v_mul_f32 v185, s29, v185\n"
  "v_mul_f32 v186, s29, v186\n"
  "v_mul_f32 v187, s29, v187\n"
  "v_mfma_f32_32x32x16_bf16 v[160:175], v[200:203], v[196:199], v[160:175]\n"
  "v_mul_f32 v188, s29, v188\n"
  "v_mul_f32 v189, s29, v189\n"
  "v_mul_f32 v190, s29, v190\n"
  "v_mul_f32 v191, s29, v191\n"
  "v_cvt_pk_bf16_f32 v176, v176, v177\n"
  "v_cvt_pk_bf16_f32 v177, v178, v179\n"
  "v_mfma_f32_32x32x16_bf16 v[96:111], v[48:51], v[48:51], 0\n"
  "v_cvt_pk_bf16_f32 v178, v180, v181\n"
  "v_cvt_pk_bf16_f32 v179, v182, v183\n"
  "v_cvt_pk_bf16_f32 v180, v184, v185\n"
  "v_cvt_pk_bf16_f32 v181, v186, v187\n"
  "v_cvt_pk_bf16_f32 v182, v188, v189\n"
  "v_cvt_pk_bf16_f32 v183, v190, v191\n"
  "v_mfma_f32_32x32x16_bf16 v[112:127], v[48:51], v[48:51], 0\n"
  "ds_write_b128 v251, v[176:179]\n"
  "v_cndmask_b32_e64 v182, v182, 0, s[4:5]\n"
  "v_cndmask_b32_e64 v183, v183, 0, s[4:5]\n"
  "ds_write_b128 v251, v[180:183] offset:1024\n"
  "v_mfma_f32_32x32x16_bf16 v[128:143], v[48:51], v[48:51], 0\n"
  "v_mfma_f32_32x32x16_bf16 v[144:159], v[48:51], v[48:51], 0\n"
  "s_branch LE%=\n"
  "LT01%=:\n"
  "v_subrev_u32 v233, 43008, v236\n"
  "s_waitcnt vmcnt(6) lgkmcnt(0)\n"
  "s_barrier\n"
  "ds_read_b128 v[192:195], v237 offset:32768\n"
  "ds_read_b64_tr_b16 v[196:197], v242 offset:40960\n"
  "ds_read_b64_tr_b16 v[198:199], v242 offset:41216\n"
  "ds_read_b128 v[200:203], v238 offset:32768\n"
  "ds_read_b64_tr_b16 v[204:205], v242 offset:41984\n"
  "ds_read_b64_tr_b16 v[206:207], v242 offset:42240\n"
  "ds_read_b128 v[208:211], v239 offset:32768\n"
  "ds_read_b64_tr_b16 v[212:213], v242 offset:43008\n"
  "ds_read_b64_tr_b16 v[214:215], v242 offset:43264\n"
  "ds_read_b128 v[224:227], v240 offset:32768\n"
  "ds_read_b64_tr_b16 v[228:229], v242 offset:44032\n"
  "ds_read_b64_tr_b16 v[230:231], v242 offset:44288\n"
  "s_add_u32 m0, s53, 67584\n"
  "s_add_u32 s66, s52, 43008\n"
  "buffer_load_dwordx4 v234, s[36:39], s66 offen lds\n"
  "s_add_u32 m0, s53, 68608\n"
  "s_nop 0\n"
  "buffer_load_dwordx4 v235, s[36:39], s66 offen lds\n"
  "s_add_u32 m0, s53, 69632\n"
  "s_add_u32 s66, s52, 57344\n"
  "buffer_load_dwordx4 v234, s[36:39], s66 offen lds\n"
  "s_add_u32 m0, s53, 70656\n"
  "s_nop 0\n"
  "buffer_load_dwordx4 v235, s[36:39], s66 offen lds\n"
  "s_add_u32 m0, s68, 76800\n"
  "s_mul_i32 s65, s52, 1344\n"
  "buffer_load_dwordx4 v233, s[40:43], s65 offen lds\n"
  "s_add_u32 m0, s68, 77824\n"
  "s_mul_i32 s65, s52, 1344\n"
  "buffer_load_dwordx4 v236, s[40:43], s65 offen lds\n"
  "s_add_u32 s52, s52, 128\n"
  "s_cmp_eq_u32 s52, 896\n"
  "s_cselect_b32 s52, 0, s52\n"
  "v_mbcnt_lo_u32_b32 v254, -1, 0\n"
  "v_mbcnt_hi_u32_b32 v254, -1, v254\n"
  "v_bfe_u32 v255, v254, 4, 1\n"
  "v_mul_u32_u24 v232, s63, v255\n"
  "v_lshrrev_b32 v255, 5, v254\n"
  "v_lshl_add_u32 v232, v255, 14, v232\n"
  "v_bfe_u32 v255, v254, 2, 2\n"
  "v_lshl_add_u32 v232, v255, 7, v232\n"
  "v_add_u32 v232, s62, v232\n"
  "s_waitcnt vmcnt(9) lgkmcnt(0)\n"
  "s_barrier\n"
  "v_mfma_f32_32x32x16_bf16 v[160:175], v[0:3], v[192:195], 0\n"
  "v_mfma_f32_32x32x16_bf16 v[176:191], v[196:199], v[80:83], 0\n"
  "v_mfma_f32_32x32x16_bf16 v[160:175], v[4:7], v[200:203], v[160:175]\n"
  "ds_read_b128 v[192:195], v237 offset:45056\n"
  "ds_read_b64_tr_b16 v[196:197], v242 offset:53248\n"
  "ds_read_b64_tr_b16 v[198:199], v242 offset:53504\n"
  "v_mfma_f32_32x32x16_bf16 v[176:191], v[204:207], v[84:87], v[176:191]\n"
  "v_mfma_f32_32x32x16_bf16 v[160:175], v[8:11], v[208:211], v[160:175]\n"
  "ds_read_b128 v[80:83], v238 offset:45056\n"
  "ds_read_b64_tr_b16 v[200:201], v242 offset:54272\n"
  "ds_read_b64_tr_b16 v[202:203], v242 offset:54528\n"
  "v_mfma_f32_32x32x16_bf16 v[176:191], v[212:215], v[88:91], v[176:191]\n"
  "ds_read_b128 v[204:207], v239 offset:45056\n"
  "ds_read_b64_tr_b16 v[84:85], v242 offset:55296\n"
  "ds_read_b64_tr_b16 v[86:87], v242 offset:55552\n"
  "v_mfma_f32_32x32x16_bf16 v[160:175], v[12:15], v[224:227], v[160:175]\n"
  "ds_read_b128 v[208:211], v240 offset:45056\n"
  "ds_read_b64_tr_b16 v[212:213], v242 offset:56320\n"
  "ds_read_b64_tr_b16 v[214:215], v242 offset:56576\n"
  "v_mfma_f32_32x32x16_bf16 v[176:191], v[228:231], v[92:95], v[176:191]\n"
  "s_add_u32 m0, s53, 30720\n"
  "s_add_u32 s66, s52, 43008\n"
  "buffer_load_dwordx4 v234, s[36:39], s66 offen lds\n"
  "s_add_u32 m0, s53, 31744\n"
  "s_nop 0\n"
  "buffer_load_dwordx4 v235, s[36:39], s66 offen lds\n"
  "s_add_u32 m0, s53, 32768\n"
  "s_add_u32 s66, s52, 57344\n"
  "buffer_load_dwordx4 v234, s[36:39], s66 offen lds\n"
  "s_add_u32 m0, s53, 33792\n"
  "s_nop 0\n"
  "buffer_load_dwordx4 v235, s[36:39], s66 offen lds\n"
  "s_add_u32 m0, s68, 39936\n"
  "s_mul_i32 s65, s52, 1344\n"
  "buffer_load_dwordx4 v233, s[40:43], s65 offen lds\n"
  "s_add_u32 m0, s68, 40960\n"
  "s_mul_i32 s65, s52, 1344\n"
  "buffer_load_dwordx4 v236, s[40:43], s65 offen lds\n"
  "s_add_u32 s52, s52, 128\n"
  "s_cmp_eq_u32 s52, 896\n"
  "s_cselect_b32 s52, 0, s52\n"
  "buffer_store_dwordx4 v[216:219], v243, s[44:47], s56 offen sc1\n"
  "buffer_store_dwordx4 v[220:223], v243, s[44:47], s67 offen sc1\n"
  "s_waitcnt vmcnt(14) lgkmcnt(0)\n"
  "s_barrier\n"
  "v_mfma_f32_32x32x16_bf16 v[160:175], v[16:19], v[192:195], v[160:175]\n"
  "ds_read_b128 v[88:91], v237 offset:57344\n"
  "ds_read_b64_tr_b16 v[224:225], v250 offset:32768\n"
  "ds_read_b64_tr_b16 v[226:227], v250 offset:33024\n"
  "v_mfma_f32_32x32x16_bf16 v[176:191], v[196:199], v[96:99], v[176:191]\n"
  "ds_read_b128 v[228:231], v238 offset:57344\n"
  "ds_read_b64_tr_b16 v[92:93], v250 offset:33792\n"
  "ds_read_b64_tr_b16 v[94:95], v250 offset:34048\n"
  "v_mfma_f32_32x32x16_bf16 v[160:175], v[20:23], v[80:83], v[160:175]\n"
  "ds_read_b128 v[192:195], v239 offset:57344\n"
  "ds_read_b64_tr_b16 v[196:197], v250 offset:34816\n"
  "ds_read_b64_tr_b16 v[198:199], v250 offset:35072\n"
  "v_mfma_f32_32x32x16_bf16 v[176:191], v[200:203], v[100:103], v[176:191]\n"
  "v_mfma_f32_32x32x16_bf16 v[160:175], v[24:27], v[204:207], v[160:175]\n"
  "ds_read_b128 v[96:99], v240 offset:57344\n"
  "ds_read_b64_tr_b16 v[80:81], v250 offset:35840\n"
  "ds_read_b64_tr_b16 v[82:83], v250 offset:36096\n"
  "v_mfma_f32_32x32x16_bf16 v[176:191], v[84:87], v[104:107], v[176:191]\n"
  "s_add_u32 m0, s53, 43008\n"
  "s_add_u32 s66, s52, 43008\n"
  "buffer_load_dwordx4 v234, s[36:39], s66 offen lds\n"
  "s_add_u32 m0, s53, 44032\n"
  "s_nop 0\n"
  "buffer_load_dwordx4 v235, s[36:39], s66 offen lds\n"
  "s_add_u32 m0, s53, 45056\n"
  "s_add_u32 s66, s52, 57344\n"
  "buffer_load_dwordx4 v234, s[36:39], s66 offen lds\n"
  "v_mfma_f32_32x32x16_bf16 v[160:175], v[28:31], v[208:211], v[160:175]\n"
  "s_add_u32 m0, s53, 46080\n"
  "s_nop 0\n"
  "buffer_load_dwordx4 v235, s[36:39], s66 offen lds\n"
  "s_add_u32 m0, s68, 52224\n"
  "s_mul_i32 s65, s52, 1344\n"
  "buffer_load_dwordx4 v233, s[40:43], s65 offen lds\n"
  "s_add_u32 m0, s68, 53248\n"
  "s_mul_i32 s65, s52, 1344\n"
  "buffer_load_dwordx4 v236, s[40:43], s65 offen lds\n"
  "s_add_u32 s52, s52, 128\n"
  "s_cmp_eq_u32 s52, 896\n"
  "s_cselect_b32 s52, 0, s52\n"
  "v_mfma_f32_32x32x16_bf16 v[176:191], v[212:215], v[108:111], v[176:191]\n"
  "v_mov_b32 v8, 0x3f803f80\n"
  "v_mov_b32 v9, 0x3f803f80\n"
  "v_mov_b32 v10, 0x3f803f80\n"
  "v_mov_b32 v11, 0x3f803f80\n"
  "v_mov_b32 v12, 0x3f803f80\n"
  "v_mov_b32 v13, 0x3f803f80\n"
  "v_mov_b32 v14, 0x3f803f80\n"
  "v_mov_b32 v15, 0x3f803f80\n"
  "s_waitcnt vmcnt(14) lgkmcnt(0)\n"
  "s_barrier\n"
  "v_mfma_f32_32x32x16_bf16 v[160:175], v[32:35], v[88:91], v[160:175]\n"
  "ds_read_b128 v[200:203], v245 offset:36864\n"
  "ds_read_b64_tr_b16 v[100:101], v250 offset:45056\n"
  "ds_read_b64_tr_b16 v[102:103], v250 offset:45312\n"
  "v_mfma_f32_32x32x16_bf16 v[176:191], v[224:227], v[112:115], v[176:191]\n"
  "ds_read_b128 v[204:207], v246 offset:36864\n"
  "ds_read_b64_tr_b16 v[84:85], v250 offset:46080\n"
  "ds_read_b64_tr_b16 v[86:87], v250 offset:46336\n"
  "v_mfma_f32_32x32x16_bf16 v[160:175], v[36:39], v[228:231], v[160:175]\n"
  "ds_read_b128 v[104:107], v247 offset:36864\n"
  "ds_read_b64_tr_b16 v[208:209], v250 offset:47104\n"
  "ds_read_b64_tr_b16 v[210:211], v250 offset:47360\n"
  "v_mfma_f32_32x32x16_bf16 v[176:191], v[92:95], v[116:119], v[176:191]\n"
  "ds_read_b128 v[212:215], v248 offset:36864\n"
  "ds_read_b64_tr_b16 v[108:109], v250 offset:48128\n"
  "ds_read_b64_tr_b16 v[110:111], v250 offset:48384\n"
  "v_mfma_f32_32x32x16_bf16 v[160:175], v[40:43], v[192:195], v[160:175]\n"
  "s_add_u32 m0, s53, 55296\n"
  "s_add_u32 s66, s52, 43008\n"
  "buffer_load_dwordx4 v234, s[36:39], s66 offen lds\n"
  "s_add_u32 m0, s53, 56320\n"
  "s_nop 0\n"
  "buffer_load_dwordx4 v235, s[36:39], s66 offen lds\n"
  "v_mfma_f32_32x32x16_bf16 v[176:191], v[196:199], v[120:123], v[176:191]\n"
  "s_add_u32 m0, s53, 57344\n"
  "s_add_u32 s66, s52, 57344\n"
  "buffer_load_dwordx4 v234, s[36:39], s66 offen lds\n"
  "s_add_u32 m0, s53, 58368\n"
  "s_nop 0\n"
  "buffer_load_dwordx4 v235, s[36:39], s66 offen lds\n"
  "v_mfma_f32_32x32x16_bf16 v[160:175], v[44:47], v[96:99], v[160:175]\n"
  "s_add_u32 m0, s68, 64512\n"
  "s_mul_i32 s65, s52, 1344\n"
  "buffer_load_dwordx4 v233, s[40:43], s65 offen lds\n"
  "s_add_u32 m0, s68, 65536\n"
  "s_mul_i32 s65, s52, 1344\n"
  "buffer_load_dwordx4 v236, s[40:43], s65 offen lds\n"
  "s_add_u32 s52, s52, 128\n"
  "s_cmp_eq_u32 s52, 896\n"
  "s_cselect_b32 s52, 0, s52\n"
  "v_mfma_f32_32x32x16_bf16 v[176:191], v[80:83], v[124:127], v[176:191]\n"
  "v_mov_b32 v24, 0x3f803f80\n"
  "v_mov_b32 v25, 0x3f803f80\n"
  "v_mov_b32 v26, 0x3f803f80\n"
  "v_mov_b32 v27, 0x3f803f80\n"
  "v_mov_b32 v28, 0x3f803f80\n"
  "v_mov_b32 v29, 0x3f803f80\n"
  "v_mov_b32 v30, 0x3f803f80\n"
  "v_mov_b32 v31, 0x3f803f80\n"
  "s_waitcnt vmcnt(14) lgkmcnt(0)\n"
  "s_barrier\n"
  "v_mfma_f32_32x32x16_bf16 v[160:175], v[48:51], v[200:203], v[160:175]\n"
  "ds_read_b128 v[88:91], v237 offset:32768\n"
  "ds_read_b64_tr_b16 v[224:225], v242 offset:40960\n"
  "ds_read_b64_tr_b16 v[226:227], v242 offset:41216\n"
  "v_mfma_f32_32x32x16_bf16 v[176:191], v[100:103], v[128:131], v[176:191]\n"
  "ds_read_b128 v[112:115], v238 offset:32768\n"
  "ds_read_b64_tr_b16 v[228:229], v242 offset:41984\n"
  "ds_read_b64_tr_b16 v[230:231], v242 offset:42240\n"
  "v_mfma_f32_32x32x16_bf16 v[160:175], v[52:55], v[204:207], v[160:175]\n"
  "ds_read_b128 v[92:95], v239 offset:32768\n"
  "ds_read_b64_tr_b16 v[116:117], v242 offset:43008\n"
  "ds_read_b64_tr_b16 v[118:119], v242 offset:43264\n"
  "v_mfma_f32_32x32x16_bf16 v[176:191], v[84:87], v[132:135], v[176:191]\n"
  "ds_read_b128 v[192:195], v240 offset:32768\n"
  "ds_read_b64_tr_b16 v[196:197], v242 offset:44032\n"
  "ds_read_b64_tr_b16 v[198:199], v242 offset:44288\n"
  "v_mfma_f32_32x32x16_bf16 v[160:175], v[56:59], v[104:107], v[160:175]\n"
  "v_mbcnt_lo_u32_b32 v236, -1, 0\n"
  "v_mbcnt_hi_u32_b32 v236, -1, v236\n"
  "v_and_b32 v235, 31, v236\n"
  "v_lshrrev_b32 v236, 5, v236\n"
  "v_lshl_add_u32 v233, v235, 4, s58\n"
  "v_mad_i32_i24 v233, v236, s59, v233\n"
  "v_lshl_add_u32 v234, s59, 1, v233\n"
  "v_mad_i32_i24 v234, v236, s60, v234\n"
  "v_mfma_f32_32x32x16_bf16 v[176:191], v[208:211], v[136:139], v[176:191]\n"
  "v_lshl_add_u32 v235, v235, 4, s61\n"
  "v_mul_u32_u24 v236, 0x1c00, v236\n"
  "v_add_u32 v235, v235, v236\n"
  "v_add_u32 v236, 0x3800, v235\n"
  "s_mul_i32 s52, s54, 917504\n"
  "s_add_u32 s52, s52, 1048576\n"
  "s_add_u32 s6, s44, s52\n"
  "s_addc_u32 s7, s45, 0\n"
  "v_mfma_f32_32x32x16_bf16 v[160:175], v[60:63], v[212:215], v[160:175]\n"
  "s_mov_b32 s70, 0x0fffffff\n"
  "s_mov_b32 s71, 0x0fffffff\n"
  "v_mov_b32 v40, 0x3f803f80\n"
  "v_mov_b32 v41, 0x3f803f80\n"
  "v_mov_b32 v42, 0x3f803f80\n"
  "v_mov_b32 v43, 0x3f803f80\n"
  "v_mfma_f32_32x32x16_bf16 v[176:191], v[108:111], v[140:143], v[176:191]\n"
  "v_mov_b32 v44, 0x3f803f80\n"
  "v_mov_b32 v45, 0x3f803f80\n"
  "v_mov_b32 v46, 0x3f803f80\n"
  "v_mov_b32 v47, 0x3f803f80\n"
  "s_waitcnt vmcnt(6) lgkmcnt(0)\n"
  "s_barrier\n"
  "v_mfma_f32_32x32x16_bf16 v[160:175], v[64:67], v[88:91], v[160:175]\n"
  "ds_read_b128 v[120:123], v237 offset:45056\n"
  "ds_read_b64_tr_b16 v[96:97], v242 offset:53248\n"
  "ds_read_b64_tr_b16 v[98:99], v242 offset:53504\n"
  "ds_read_b64_tr_b16 v[80:81], v241 offset:0\n"
  "ds_read_b64_tr_b16 v[82:83], v241 offset:512\n"
  "ds_read_b128 v[124:127], v237 offset:8192\n"
  "v_mfma_f32_32x32x16_bf16 v[176:191], v[224:227], v[144:147], v[176:191]\n"
  "ds_read_b128 v[200:203], v238 offset:45056\n"
  "ds_read_b64_tr_b16 v[100:101], v242 offset:54272\n"
  "ds_read_b64_tr_b16 v[102:103], v242 offset:54528\n"
  "ds_read_b64_tr_b16 v[128:129], v241 offset:2048\n"
  "ds_read_b64_tr_b16 v[130:131], v241 offset:2560\n"
  "ds_read_b128 v[204:207], v238 offset:8192\n"
  "v_mfma_f32_32x32x16_bf16 v[160:175], v[68:71], v[112:115], v[160:175]\n"
  "ds_read_b128 v[84:87], v239 offset:45056\n"
  "ds_read_b64_tr_b16 v[132:133], v242 offset:55296\n"
  "ds_read_b64_tr_b16 v[134:135], v242 offset:55552\n"
  "ds_read_b64_tr_b16 v[104:105], v241 offset:4096\n"
  "ds_read_b64_tr_b16 v[106:107], v241 offset:4608\n"
  "ds_read_b128 v[208:211], v239 offset:8192\n"
  "v_mfma_f32_32x32x16_bf16 v[176:191], v[228:231], v[148:151], v[176:191]\n"
  "ds_read_b128 v[136:139], v240 offset:45056\n"
  "ds_read_b64_tr_b16 v[212:213], v242 offset:56320\n"
  "ds_read_b64_tr_b16 v[214:215], v242 offset:56576\n"
  "ds_read_b64_tr_b16 v[108:109], v241 offset:6144\n"
  "ds_read_b64_tr_b16 v[110:111], v241 offset:6656\n"
  "ds_read_b128 v[140:143], v240 offset:8192\n"
  "v_mfma_f32_32x32x16_bf16 v[160:175], v[72:75], v[92:95], v[160:175]\n"
  "buffer_load_dwordx2 v[254:255], v232, s[44:47], 0 offen sc1\n"
  "v_mov_b32 v56, 0x3f803f80\n"
  "v_mov_b32 v57, 0x3f803f80\n"
  "v_mov_b32 v58, 0x3f803f80\n"
  "v_mov_b32 v59, 0x3f803f80\n"
  "v_mfma_f32_32x32x16_bf16 v[176:191], v[116:119], v[152:155], v[176:191]\n"
  "v_mov_b32 v60, 0x3f803f80\n"
  "v_mov_b32 v61, 0x3f803f80\n"
  "v_mov_b32 v62, 0x3f803f80\n"
  "v_mov_b32 v63, 0x3f803f80\n"
  "s_add_u32 s22, s54, 1\n"
  "s_and_b32 s22, s22, 3\n"
  "s_mul_i32 s55, s22, 12288\n"
  "s_add_u32 s23, s54, 2\n"
  "s_and_b32 s23, s23, 3\n"
  "s_mul_i32 s60, s23, 12288\n"
  "v_mfma_f32_32x32x16_bf16 v[160:175], v[76:79], v[192:195], v[160:175]\n"
  "s_add_u32 s60, s60, 4096\n"
  "s_add_u32 s21, s54, 3\n"
  "s_and_b32 s21, s21, 3\n"
  "s_mul_i32 s61, s21, 12288\n"
  "s_add_u32 s61, s61, 8192\n"
  "s_mul_i32 s63, s54, 12288\n"
  "s_mul_i32 s28, s54, 7168\n"
  "s_add_u32 s64, s64, s28\n"
  "s_lshl_b32 s28, s24, 6\n"
  "s_add_u32 s28, s28, s20\n"
  "s_lshl_b32 s28, s28, 7\n"
  "s_add_u32 s62, s28, 69632\n"
  "v_mfma_f32_32x32x16_bf16 v[176:191], v[196:199], v[156:159], v[176:191]\n"
  "s_mov_b32 s67, 0x8000\n"
  "s_mov_b32 s29, 0x3e8b97f6\n"
  "s_mov_b32 s4, 0\n"
  "s_mov_b32 s5, -1\n"
  "s_mov_b32 s28, 0x8000\n"
  "v_mov_b32 v48, 0\n"
  "v_mov_b32 v49, 0\n"
  "v_mov_b32 v50, 0\n"
  "v_mov_b32 v51, 0\n"
  "s_waitcnt vmcnt(1) lgkmcnt(0)\n"
  "s_barrier\n"
  "v_mfma_f32_32x32x16_bf16 v[160:175], v[80:83], v[120:123], v[160:175]\n"
  "ds_read_b128 v[64:67], v237 offset:57344\n"
  "ds_read_b64_tr_b16 v[88:89], v250 offset:32768\n"
  "ds_read_b64_tr_b16 v[90:91], v250 offset:33024\n"
  "ds_read_b64_tr_b16 v[224:225], v241 offset:16384\n"
  "ds_read_b64_tr_b16 v[226:227], v241 offset:16896\n"
  "ds_read_b128 v[144:147], v237 offset:24576\n"
  "v_mfma_f32_32x32x16_bf16 v[176:191], v[96:99], v[124:127], v[176:191]\n"
  "ds_read_b128 v[68:71], v238 offset:57344\n"
  "ds_read_b64_tr_b16 v[112:113], v250 offset:33792\n"
  "ds_read_b64_tr_b16 v[114:115], v250 offset:34048\n"
  "ds_read_b64_tr_b16 v[228:229], v241 offset:18432\n"
  "ds_read_b64_tr_b16 v[230:231], v241 offset:18944\n"
  "ds_read_b128 v[148:151], v238 offset:24576\n"
  "v_mfma_f32_32x32x16_bf16 v[160:175], v[128:131], v[200:203], v[160:175]\n"
  "ds_read_b128 v[72:75], v239 offset:57344\n"
  "ds_read_b64_tr_b16 v[92:93], v250 offset:34816\n"
  "ds_read_b64_tr_b16 v[94:95], v250 offset:35072\n"
  "ds_read_b64_tr_b16 v[116:117], v241 offset:20480\n"
  "ds_read_b64_tr_b16 v[118:119], v241 offset:20992\n"
  "ds_read_b128 v[152:155], v239 offset:24576\n"
  "v_mfma_f32_32x32x16_bf16 v[176:191], v[100:103], v[204:207], v[176:191]\n"
  "ds_read_b128 v[76:79], v240 offset:57344\n"
  "ds_read_b64_tr_b16 v[192:193], v250 offset:35840\n"
  "ds_read_b64_tr_b16 v[194:195], v250 offset:36096\n"
  "ds_read_b64_tr_b16 v[196:197], v241 offset:22528\n"
  "ds_read_b64_tr_b16 v[198:199], v241 offset:23040\n"
  "ds_read_b128 v[156:159], v240 offset:24576\n"
  "v_mfma_f32_32x32x16_bf16 v[160:175], v[104:107], v[84:87], v[160:175]\n"
  "v_and_b32 v238, 0x3f0, v251\n"
  "v_lshl_add_u32 v249, s22, 11, v238\n"
  "v_lshl_add_u32 v250, s23, 11, v238\n"
  "v_lshl_add_u32 v241, s21, 11, v238\n"
  "v_add_u32 v245, s55, v238\n"
  "v_mfma_f32_32x32x16_bf16 v[176:191], v[132:135], v[208:211], v[176:191]\n"
  "v_add_u32 v246, s60, v238\n"
  "v_add_u32 v247, s61, v238\n"
  "v_add_u32 v248, s63, v238\n"
  "v_mov_b32 v237, 0xdddddddd\n"
  "v_lshrrev_b32 v240, 7, v238\n"
  "v_mfma_f32_32x32x16_bf16 v[160:175], v[108:111], v[136:139], v[160:175]\n"
  "v_lshrrev_b32 v242, 2, v238\n"
  "v_and_b32 v242, 64, v242\n"
  "v_and_b32 v239, 0x70, v238\n"
  "v_mul_u32_u24 v240, 0x380, v240\n"
  "v_xad_u32 v239, v239, v242, v240\n"
  "v_mfma_f32_32x32x16_bf16 v[176:191], v[212:215], v[140:143], v[176:191]\n"
  "v_add_u32 v239, s64, v239\n"
  "v_bfe_u32 v240, v238, 4, 3\n"
  "v_and_b32 v242, 4, v240\n"
  "v_add_lshl_u32 v240, v240, v242, 11\n"
  "s_waitcnt vmcnt(0)\n"
  "v_xor_b32 v254, v254, v252\n"
  "v_xor_b32 v255, v255, v253\n"
  "v_or_b32 v254, v254, v255\n"
  "v_cmp_ne_u32 vcc, 0, v254\n"
  "s_cbranch_vccz LDa001%=\n"
  "s_mov_b32 s69, 0x40000\n"
  "LPa001%=:\n"
  "s_sleep 1\n"
  "buffer_load_dwordx2 v[254:255], v232, s[44:47], 0 offen sc1\n"
  "s_waitcnt vmcnt(0)\n"
  "v_xor_b32 v254, v254, v252\n"
  "v_xor_b32 v255, v255, v253\n"
  "v_or_b32 v254, v254, v255\n"
  "v_cmp_ne_u32 vcc, 0, v254\n"
  "s_cbranch_vccz LDa001%=\n"
  "s_sub_u32 s69, s69, 1\n"
  "s_cmp_lg_u32 s69, 0\n"
  "s_cbranch_scc1 LPa001%=\n"
  "LDa001%=:\n"
  "s_add_u32 s30, s6, 0\n"
  "s_addc_u32 s31, s7, 0\n"
  "global_load_dwordx4 v[0:3], v233, s[30:31] sc1\n"
  "global_load_dwordx4 v[4:7], v234, s[30:31] sc1\n"
  "s_waitcnt lgkmcnt(14)\n"
  "v_mfma_f32_32x32x16_bf16 v[176:191], v[88:91], v[144:147], v[176:191]\n"
  "s_add_u32 s30, s6, 229376\n"
  "s_addc_u32 s31, s7, 0\n"
  "global_load_dwordx4 v[16:19], v233, s[30:31] sc1\n"
  "global_load_dwordx4 v[20:23], v234, s[30:31] sc1\n"
  "s_waitcnt lgkmcnt(12)\n"
  "v_mfma_f32_32x32x16_bf16 v[176:191], v[112:115], v[148:151], v[176:191]\n"
  "s_add_u32 s30, s6, 0\n"
  "s_addc_u32 s31, s7, 0\n"
  "s_mov_b64 exec, s[70:71]\n"
  "global_load_dwordx4 v[8:11], v235, s[30:31] sc1\n"
  "global_load_dwordx4 v[12:15], v236, s[30:31] sc1\n"
  "s_mov_b64 exec, -1\n"
  "s_waitcnt lgkmcnt(6)\n"
  "v_mfma_f32_32x32x16_bf16 v[176:191], v[92:95], v[152:155], v[176:191]\n"
  "s_add_u32 s30, s6, 229376\n"
  "s_addc_u32 s31, s7, 0\n"
  "s_mov_b64 exec, s[70:71]\n"
  "global_load_dwordx4 v[24:27], v235, s[30:31] sc1\n"
  "global_load_dwordx4 v[28:31], v236, s[30:31] sc1\n"
  "s_mov_b64 exec, -1\n"
  "s_waitcnt lgkmcnt(0)\n"
  "v_mfma_f32_32x32x16_bf16 v[176:191], v[192:195], v[156:159], v[176:191]\n"
  "v_mfma_f32_32x32x16_bf16 v[160:175], v[224:227], v[64:67], v[160:175]\n"
  "v_mfma_f32_32x32x16_bf16 v[160:175], v[228:231], v[68:71], v[160:175]\n"
  "s_nop 9\n"
  "v_mul_f32 v176, s29, v176\n"
  "v_mul_f32 v177, s29, v177\n"
  "v_mul_f32 v178, s29, v178\n"
  "v_mul_f32 v179, s29, v179\n"
  "v_mul_f32 v180, s29, v180\n"
  "v_mul_f32 v181, s29, v181\n"
  "v_mfma_f32_32x32x16_bf16 v[160:175], v[116:119], v[72:75], v[160:175]\n"
  "v_mul_f32 v182, s29, v182\n"
  "v_mul_f32 v183, s29, v183\n"
  "v_mul_f32 v184, s29, v184\n"
  "v_mul_f32 v185, s29, v185\n"
  "v_mul_f32 v186, s29, v186\n"
  "v_mul_f32 v187, s29, v187\n"
  "v_mfma_f32_32x32x16_bf16 v[160:175], v[196:199], v[76:79], v[160:175]\n"
  "v_mul_f32 v188, s29, v188\n"
  "v_mul_f32 v189, s29, v189\n"
  "v_mul_f32 v190, s29, v190\n"
  "v_mul_f32 v191, s29, v191\n"
  "v_cvt_pk_bf16_f32 v176, v176, v177\n"
  "v_cvt_pk_bf16_f32 v177, v178, v179\n"
  "v_mfma_f32_32x32x16_bf16 v[96:111], v[48:51], v[48:51], 0\n"
  "v_cvt_pk_bf16_f32 v178, v180, v181\n"
  "v_cvt_pk_bf16_f32 v179, v182, v183\n"
  "v_cvt_pk_bf16_f32 v180, v184, v185\n"
  "v_cvt_pk_bf16_f32 v181, v186, v187\n"
  "v_cvt_pk_bf16_f32 v182, v188, v189\n"
  "v_cvt_pk_bf16_f32 v183, v190, v191\n"
  "v_mfma_f32_32x32x16_bf16 v[112:127], v[48:51], v[48:51], 0\n"
  "ds_write_b128 v251, v[176:179]\n"
  "v_cndmask_b32_e64 v182, v182, 0, s[4:5]\n"
  "v_cndmask_b32_e64 v183, v183, 0, s[4:5]\n"
  "ds_write_b128 v251, v[180:183] offset:1024\n"
  "v_mfma_f32_32x32x16_bf16 v[128:143], v[48:51], v[48:51], 0\n"
  "v_mfma_f32_32x32x16_bf16 v[144:159], v[48:51], v[48:51], 0\n"
  "s_branch LE%=\n"
  "LT03%=:\n"
  "v_subrev_u32 v233, 43008, v236\n"
  "s_waitcnt vmcnt(6) lgkmcnt(0)\n"
  "s_barrier\n"
  "ds_read_b128 v[192:195], v237 offset:32768\n"
  "ds_read_b64_tr_b16 v[196:197], v242 offset:40960\n"
  "ds_read_b64_tr_b16 v[198:199], v242 offset:41216\n"
  "ds_read_b128 v[200:203], v238 offset:32768\n"
  "ds_read_b64_tr_b16 v[204:205], v242 offset:41984\n"
  "ds_read_b64_tr_b16 v[206:207], v242 offset:42240\n"
  "ds_read_b128 v[208:211], v239 offset:32768\n"
  "ds_read_b64_tr_b16 v[212:213], v242 offset:43008\n"
  "ds_read_b64_tr_b16 v[214:215], v242 offset:43264\n"
  "ds_read_b128 v[224:227], v240 offset:32768\n"
  "ds_read_b64_tr_b16 v[228:229], v242 offset:44032\n"
  "ds_read_b64_tr_b16 v[230:231], v242 offset:44288\n"
  "s_add_u32 m0, s53, 67584\n"
  "s_add_u32 s66, s52, 43008\n"
  "buffer_load_dwordx4 v234, s[36:39], s66 offen lds\n"
  "s_add_u32 m0, s53, 68608\n"
  "s_nop 0\n"
  "buffer_load_dwordx4 v235, s[36:39], s66 offen lds\n"
  "s_add_u32 m0, s53, 69632\n"
  "s_add_u32 s66, s52, 57344\n"
  "buffer_load_dwordx4 v234, s[36:39], s66 offen lds\n"
  "s_add_u32 m0, s53, 70656\n"
  "s_nop 0\n"
  "buffer_load_dwordx4 v235, s[36:39], s66 offen lds\n"
  "s_add_u32 m0, s68, 76800\n"
  "s_mul_i32 s65, s52, 1344\n"
  "buffer_load_dwordx4 v233, s[40:43], s65 offen lds\n"
  "s_add_u32 m0, s68, 77824\n"
  "s_mul_i32 s65, s52, 1344\n"
  "buffer_load_dwordx4 v236, s[40:43], s65 offen lds\n"
  "s_add_u32 s52, s52, 128\n"
  "s_cmp_eq_u32 s52, 896\n"
  "s_cselect_b32 s52, 0, s52\n"
  "v_mbcnt_lo_u32_b32 v254, -1, 0\n"
  "v_mbcnt_hi_u32_b32 v254, -1, v254\n"
  "v_bfe_u32 v255, v254, 4, 1\n"
  "v_mul_u32_u24 v232, s63, v255\n"
  "v_lshrrev_b32 v255, 5, v254\n"
  "v_lshl_add_u32 v232, v255, 14, v232\n"
  "v_bfe_u32 v255, v254, 2, 2\n"
  "v_lshl_add_u32 v232, v255, 7, v232\n"
  "v_add_u32 v232, s62, v232\n"
  "s_waitcnt vmcnt(9) lgkmcnt(0)\n"
  "s_barrier\n"
  "v_mfma_f32_32x32x16_bf16 v[160:175], v[0:3], v[192:195], 0\n"
  "ds_read_b128 v[80:83], v237 offset:45056\n"
  "ds_read_b64_tr_b16 v[84:85], v242 offset:53248\n"
  "ds_read_b64_tr_b16 v[86:87], v242 offset:53504\n"
  "v_mfma_f32_32x32x16_bf16 v[176:191], v[196:199], v[192:195], 0\n"
  "ds_read_b128 v[88:91], v238 offset:45056\n"
  "ds_read_b64_tr_b16 v[92:93], v242 offset:54272\n"
  "ds_read_b64_tr_b16 v[94:95], v242 offset:54528\n"
  "v_mfma_f32_32x32x16_bf16 v[160:175], v[4:7], v[200:203], v[160:175]\n"
  "ds_read_b128 v[96:99], v239 offset:45056\n"
  "ds_read_b64_tr_b16 v[100:101], v242 offset:55296\n"
  "ds_read_b64_tr_b16 v[102:103], v242 offset:55552\n"
  "v_mfma_f32_32x32x16_bf16 v[176:191], v[204:207], v[200:203], v[176:191]\n"
  "ds_read_b128 v[104:107], v240 offset:45056\n"
  "ds_read_b64_tr_b16 v[108:109], v242 offset:56320\n"
  "ds_read_b64_tr_b16 v[110:111], v242 offset:56576\n"
  "v_mfma_f32_32x32x16_bf16 v[160:175], v[8:11], v[208:211], v[160:175]\n"
  "s_add_u32 m0, s53, 30720\n"
  "s_add_u32 s66, s52, 43008\n"
  "buffer_load_dwordx4 v234, s[36:39], s66 offen lds\n"
  "s_add_u32 m0, s53, 31744\n"
  "s_nop 0\n"
  "buffer_load_dwordx4 v235, s[36:39], s66 offen lds\n"
  "v_mfma_f32_32x32x16_bf16 v[176:191], v[212:215], v[208:211], v[176:191]\n"
  "s_add_u32 m0, s53, 32768\n"
  "s_add_u32 s66, s52, 57344\n"
  "buffer_load_dwordx4 v234, s[36:39], s66 offen lds\n"
  "s_add_u32 m0, s53, 33792\n"
  "s_nop 0\n"
  "buffer_load_dwordx4 v235, s[36:39], s66 offen lds\n"
  "v_mfma_f32_32x32x16_bf16 v[160:175], v[12:15], v[224:227], v[160:175]\n"
  "s_add_u32 m0, s68, 39936\n"
  "s_mul_i32 s65, s52, 1344\n"
  "buffer_load_dwordx4 v233, s[40:43], s65 offen lds\n"
  "s_add_u32 m0, s68, 40960\n"
  "s_mul_i32 s65, s52, 1344\n"
  "buffer_load_dwordx4 v236, s[40:43], s65 offen lds\n"
  "s_add_u32 s52, s52, 128\n"
  "s_cmp_eq_u32 s52, 896\n"
  "s_cselect_b32 s52, 0, s52\n"
  "v_mfma_f32_32x32x16_bf16 v[176:191], v[228:231], v[224:227], v[176:191]\n"
  "buffer_store_dwordx4 v[216:219], v243, s[44:47], s56 offen sc1\n"
  "buffer_store_dwordx4 v[220:223], v243, s[44:47], s67 offen sc1\n"
  "s_waitcnt vmcnt(14) lgkmcnt(0)\n"
  "s_barrier\n"
  "v_mfma_f32_32x32x16_bf16 v[160:175], v[16:19], v[80:83], v[160:175]\n"
  "ds_read_b128 v[112:115], v237 offset:57344\n"
  "ds_read_b64_tr_b16 v[116:117], v250 offset:32768\n"
  "ds_read_b64_tr_b16 v[118:119], v250 offset:33024\n"
  "v_mfma_f32_32x32x16_bf16 v[176:191], v[84:87], v[80:83], v[176:191]\n"
  "ds_read_b128 v[120:123], v238 offset:57344\n"
  "ds_read_b64_tr_b16 v[124:125], v250 offset:33792\n"
  "ds_read_b64_tr_b16 v[126:127], v250 offset:34048\n"
  "v_mfma_f32_32x32x16_bf16 v[160:175], v[20:23], v[88:91], v[160:175]\n"
  "ds_read_b128 v[128:131], v239 offset:57344\n"
  "ds_read_b64_tr_b16 v[132:133], v250 offset:34816\n"
  "ds_read_b64_tr_b16 v[134:135], v250 offset:35072\n"
  "v_mfma_f32_32x32x16_bf16 v[176:191], v[92:95], v[88:91], v[176:191]\n"
  "ds_read_b128 v[136:139], v240 offset:57344\n"
  "ds_read_b64_tr_b16 v[140:141], v250 offset:35840\n"
  "ds_read_b64_tr_b16 v[142:143], v250 offset:36096\n"
  "v_mfma_f32_32x32x16_bf16 v[160:175], v[24:27], v[96:99], v[160:175]\n"
  "s_add_u32 m0, s53, 43008\n"
  "s_add_u32 s66, s52, 43008\n"
  "buffer_load_dwordx4 v234, s[36:39], s66 offen lds\n"
  "s_add_u32 m0, s53, 44032\n"
  "s_nop 0\n"
  "buffer_load_dwordx4 v235, s[36:39], s66 offen lds\n"
  "v_mfma_f32_32x32x16_bf16 v[176:191], v[100:103], v[96:99], v[176:191]\n"
  "s_add_u32 m0, s53, 45056\n"
  "s_add_u32 s66, s52, 57344\n"
  "buffer_load_dwordx4 v234, s[36:39], s66 offen lds\n"
  "s_add_u32 m0, s53, 46080\n"
  "s_nop 0\n"
  "buffer_load_dwordx4 v235, s[36:39], s66 offen lds\n"
  "v_mfma_f32_32x32x16_bf16 v[160:175], v[28:31], v[104:107], v[160:175]\n"
  "s_add_u32 m0, s68, 52224\n"
  "s_mul_i32 s65, s52, 1344\n"
  "buffer_load_dwordx4 v233, s[40:43], s65 offen lds\n"
  "s_add_u32 m0, s68, 53248\n"
  "s_mul_i32 s65, s52, 1344\n"
  "buffer_load_dwordx4 v236, s[40:43], s65 offen lds\n"
  "s_add_u32 s52, s52, 128\n"
  "s_cmp_eq_u32 s52, 896\n"
  "s_cselect_b32 s52, 0, s52\n"
  "v_mfma_f32_32x32x16_bf16 v[176:191], v[108:111], v[104:107], v[176:191]\n"
  "v_mov_b32 v8, 0x3f803f80\n"
  "v_mov_b32 v9, 0x3f803f80\n"
  "v_mov_b32 v10, 0x3f803f80\n"
  "v_mov_b32 v11, 0x3f803f80\n"
  "v_mov_b32 v12, 0x3f803f80\n"
  "v_mov_b32 v13, 0x3f803f80\n"
  "v_mov_b32 v14, 0x3f803f80\n"
  "v_mov_b32 v15, 0x3f803f80\n"
  "s_waitcnt vmcnt(14) lgkmcnt(0)\n"
  "s_barrier\n"
  "v_mfma_f32_32x32x16_bf16 v[160:175], v[32:35], v[112:115], v[160:175]\n"
  "ds_read_b128 v[144:147], v245 offset:36864\n"
  "ds_read_b64_tr_b16 v[148:149], v250 offset:45056\n"
  "ds_read_b64_tr_b16 v[150:151], v250 offset:45312\n"
  "v_mfma_f32_32x32x16_bf16 v[176:191], v[116:119], v[112:115], v[176:191]\n"
  "ds_read_b128 v[152:155], v246 offset:36864\n"
  "ds_read_b64_tr_b16 v[156:157], v250 offset:46080\n"
  "ds_read_b64_tr_b16 v[158:159], v250 offset:46336\n"
  "v_mfma_f32_32x32x16_bf16 v[160:175], v[36:39], v[120:123], v[160:175]\n"
  "ds_read_b128 v[192:195], v247 offset:36864\n"
  "ds_read_b64_tr_b16 v[196:197], v250 offset:47104\n"
  "ds_read_b64_tr_b16 v[198:199], v250 offset:47360\n"
  "v_mfma_f32_32x32x16_bf16 v[176:191], v[124:127], v[120:123], v[176:191]\n"
  "ds_read_b128 v[200:203], v248 offset:36864\n"
  "ds_read_b64_tr_b16 v[204:205], v250 offset:48128\n"
  "ds_read_b64_tr_b16 v[206:207], v250 offset:48384\n"
  "v_mfma_f32_32x32x16_bf16 v[160:175], v[40:43], v[128:131], v[160:175]\n"
  "s_add_u32 m0, s53, 55296\n"
  "s_add_u32 s66, s52, 43008\n"
  "buffer_load_dwordx4 v234, s[36:39], s66 offen lds\n"
  "s_add_u32 m0, s53, 56320\n"
  "s_nop 0\n"
  "buffer_load_dwordx4 v235, s[36:39], s66 offen lds\n"
  "v_mfma_f32_32x32x16_bf16 v[176:191], v[132:135], v[128:131], v[176:191]\n"
  "s_add_u32 m0, s53, 57344\n"
  "s_add_u32 s66, s52, 57344\n"
  "buffer_load_dwordx4 v234, s[36:39], s66 offen lds\n"
  "s_add_u32 m0, s53, 58368\n"
  "s_nop 0\n"
  "buffer_load_dwordx4 v235, s[36:39], s66 offen lds\n"
  "v_mfma_f32_32x32x16_bf16 v[160:175], v[44:47], v[136:139], v[160:175]\n"
  "s_add_u32 m0, s68, 64512\n"
  "s_mul_i32 s65, s52, 1344\n"
  "buffer_load_dwordx4 v233, s[40:43], s65 offen lds\n"
  "s_add_u32 m0, s68, 65536\n"
  "s_mul_i32 s65, s52, 1344\n"
  "buffer_load_dwordx4 v236, s[40:43], s65 offen lds\n"
  "s_add_u32 s52, s52, 128\n"
  "s_cmp_eq_u32 s52, 896\n"
  "s_cselect_b32 s52, 0, s52\n"
  "v_mfma_f32_32x32x16_bf16 v[176:191], v[140:143], v[136:139], v[176:191]\n"
  "v_mov_b32 v24, 0x3f803f80\n"
  "v_mov_b32 v25, 0x3f803f80\n"
  "v_mov_b32 v26, 0x3f803f80\n"
  "v_mov_b32 v27, 0x3f803f80\n"
  "v_mov_b32 v28, 0x3f803f80\n"
  "v_mov_b32 v29, 0x3f803f80\n"
  "v_mov_b32 v30, 0x3f803f80\n"
  "v_mov_b32 v31, 0x3f803f80\n"
  "s_waitcnt vmcnt(14) lgkmcnt(0)\n"
  "s_barrier\n"
  "v_mfma_f32_32x32x16_bf16 v[160:175], v[48:51], v[144:147], v[160:175]\n"
  "ds_read_b128 v[208:211], v237 offset:32768\n"
  "ds_read_b64_tr_b16 v[212:213], v242 offset:40960\n"
  "ds_read_b64_tr_b16 v[214:215], v242 offset:41216\n"
  "v_mfma_f32_32x32x16_bf16 v[176:191], v[148:151], v[144:147], v[176:191]\n"
  "ds_read_b128 v[224:227], v238 offset:32768\n"
  "ds_read_b64_tr_b16 v[228:229], v242 offset:41984\n"
  "ds_read_b64_tr_b16 v[230:231], v242 offset:42240\n"
  "v_mfma_f32_32x32x16_bf16 v[160:175], v[52:55], v[152:155], v[160:175]\n"
  "ds_read_b128 v[80:83], v239 offset:32768\n"
  "ds_read_b64_tr_b16 v[84:85], v242 offset:43008\n"
  "ds_read_b64_tr_b16 v[86:87], v242 offset:43264\n"
  "v_mfma_f32_32x32x16_bf16 v[176:191], v[156:159], v[152:155], v[176:191]\n"
  "ds_read_b128 v[88:91], v240 offset:32768\n"
  "ds_read_b64_tr_b16 v[92:93], v242 offset:44032\n"
  "ds_read_b64_tr_b16 v[94:95], v242 offset:44288\n"
  "v_mfma_f32_32x32x16_bf16 v[160:175], v[56:59], v[192:195], v[160:175]\n"
  "v_mbcnt_lo_u32_b32 v236, -1, 0\n"
  "v_mbcnt_hi_u32_b32 v236, -1, v236\n"
  "v_and_b32 v235, 31, v236\n"
  "v_lshrrev_b32 v236, 5, v236\n"
  "v_lshl_add_u32 v233, v235, 4, s58\n"
  "v_mad_i32_i24 v233, v236, s59, v233\n"
  "v_lshl_add_u32 v234, s59, 1, v233\n"
  "v_mad_i32_i24 v234, v236, s60, v234\n"
  "v_mfma_f32_32x32x16_bf16 v[176:191], v[196:199], v[192:195], v[176:191]\n"
  "v_lshl_add_u32 v235, v235, 4, s61\n"
  "v_mul_u32_u24 v236, 0x1c00, v236\n"
  "v_add_u32 v235, v235, v236\n"
  "v_add_u32 v236, 0x3800, v235\n"
  "s_mul_i32 s52, s54, 917504\n"
  "s_add_u32 s52, s52, 1048576\n"
  "s_add_u32 s6, s44, s52\n"
  "s_addc_u32 s7, s45, 0\n"
  "v_mfma_f32_32x32x16_bf16 v[160:175], v[60:63], v[200:203], v[160:175]\n"
  "s_mov_b32 s70, 0x0fffffff\n"
  "s_mov_b32 s71, 0x0fffffff\n"
  "v_mov_b32 v40, 0x3f803f80\n"
  "v_mov_b32 v41, 0x3f803f80\n"
  "v_mov_b32 v42, 0x3f803f80\n"
  "v_mov_b32 v43, 0x3f803f80\n"
  "v_mfma_f32_32x32x16_bf16 v[176:191], v[204:207], v[200:203], v[176:191]\n"
  "v_mov_b32 v44, 0x3f803f80\n"
  "v_mov_b32 v45, 0x3f803f80\n"
  "v_mov_b32 v46, 0x3f803f80\n"
  "v_mov_b32 v47, 0x3f803f80\n"
  "s_waitcnt vmcnt(6) lgkmcnt(0)\n"
  "s_barrier\n"
  "v_mfma_f32_32x32x16_bf16 v[160:175], v[64:67], v[208:211], v[160:175]\n"
  "ds_read_b128 v[96:99], v237 offset:45056\n"
  "ds_read_b64_tr_b16 v[100:101], v242 offset:53248\n"
  "ds_read_b64_tr_b16 v[102:103], v242 offset:53504\n"
  "ds_read_b64_tr_b16 v[104:105], v241 offset:0\n"
  "ds_read_b64_tr_b16 v[106:107], v241 offset:512\n"
  "v_mfma_f32_32x32x16_bf16 v[176:191], v[212:215], v[208:211], v[176:191]\n"
  "ds_read_b128 v[108:111], v238 offset:45056\n"
  "ds_read_b64_tr_b16 v[112:113], v242 offset:54272\n"
  "ds_read_b64_tr_b16 v[114:115], v242 offset:54528\n"
  "ds_read_b64_tr_b16 v[116:117], v241 offset:2048\n"
  "ds_read_b64_tr_b16 v[118:119], v241 offset:2560\n"
  "v_mfma_f32_32x32x16_bf16 v[160:175], v[68:71], v[224:227], v[160:175]\n"
  "ds_read_b128 v[120:123], v239 offset:45056\n"
  "ds_read_b64_tr_b16 v[124:125], v242 offset:55296\n"
  "ds_read_b64_tr_b16 v[126:127], v242 offset:55552\n"
  "ds_read_b64_tr_b16 v[128:129], v241 offset:4096\n"
  "ds_read_b64_tr_b16 v[130:131], v241 offset:4608\n"
  "v_mfma_f32_32x32x16_bf16 v[176:191], v[228:231], v[224:227], v[176:191]\n"
  "ds_read_b128 v[132:135], v240 offset:45056\n"
  "ds_read_b64_tr_b16 v[136:137], v242 offset:56320\n"
  "ds_read_b64_tr_b16 v[138:139], v242 offset:56576\n"
  "ds_read_b64_tr_b16 v[140:141], v241 offset:6144\n"
  "ds_read_b64_tr_b16 v[142:143], v241 offset:6656\n"
  "v_mfma_f32_32x32x16_bf16 v[160:175], v[72:75], v[80:83], v[160:175]\n"
  "buffer_load_dwordx2 v[254:255], v232, s[44:47], 0 offen sc1\n"
  "v_mov_b32 v56, 0x3f803f80\n"
  "v_mov_b32 v57, 0x3f803f80\n"
  "v_mov_b32 v58, 0x3f803f80\n"
  "v_mov_b32 v59, 0x3f803f80\n"
  "v_mfma_f32_32x32x16_bf16 v[176:191], v[84:87], v[80:83], v[176:191]\n"
  "v_mov_b32 v60, 0x3f803f80\n"
  "v_mov_b32 v61, 0x3f803f80\n"
  "v_mov_b32 v62, 0x3f803f80\n"
  "v_mov_b32 v63, 0x3f803f80\n"
  "s_add_u32 s22, s54, 1\n"
  "s_and_b32 s22, s22, 3\n"
  "s_mul_i32 s55, s22, 12288\n"
  "s_add_u32 s23, s54, 2\n"
  "s_and_b32 s23, s23, 3\n"
  "s_mul_i32 s60, s23, 12288\n"
  "v_mfma_f32_32x32x16_bf16 v[160:175], v[76:79], v[88:91], v[160:175]\n"
  "s_add_u32 s60, s60, 4096\n"
  "s_add_u32 s21, s54, 3\n"
  "s_and_b32 s21, s21, 3\n"
  "s_mul_i32 s61, s21, 12288\n"
  "s_add_u32 s61, s61, 8192\n"
  "s_mul_i32 s63, s54, 12288\n"
  "s_mul_i32 s28, s54, 7168\n"
  "s_add_u32 s64, s64, s28\n"
  "s_lshl_b32 s28, s24, 6\n"
  "s_add_u32 s28, s28, s20\n"
  "s_lshl_b32 s28, s28, 7\n"
  "s_add_u32 s62, s28, 69632\n"
  "v_mfma_f32_32x32x16_bf16 v[176:191], v[92:95], v[88:91], v[176:191]\n"
  "s_mov_b32 s67, 0x8000\n"
  "s_mov_b32 s29, 0x3e8b97f6\n"
  "s_mov_b32 s4, 0\n"
  "s_mov_b32 s5, -1\n"
  "s_mov_b32 s28, 0x8000\n"
  "v_mov_b32 v48, 0\n"
  "v_mov_b32 v49, 0\n"
  "v_mov_b32 v50, 0\n"
  "v_mov_b32 v51, 0\n"
  "s_waitcnt vmcnt(1) lgkmcnt(0)\n"
  "s_barrier\n"
  "v_mfma_f32_32x32x16_bf16 v[160:175], v[104:107], v[96:99], v[160:175]\n"
  "ds_read_b128 v[144:147], v237 offset:57344\n"
  "ds_read_b64_tr_b16 v[148:149], v250 offset:32768\n"
  "ds_read_b64_tr_b16 v[150:151], v250 offset:33024\n"
  "ds_read_b64_tr_b16 v[152:153], v241 offset:16384\n"
  "ds_read_b64_tr_b16 v[154:155], v241 offset:16896\n"
  "v_mfma_f32_32x32x16_bf16 v[176:191], v[100:103], v[96:99], v[176:191]\n"
  "ds_read_b128 v[156:159], v238 offset:57344\n"
  "ds_read_b64_tr_b16 v[192:193], v250 offset:33792\n"
  "ds_read_b64_tr_b16 v[194:195], v250 offset:34048\n"
  "ds_read_b64_tr_b16 v[196:197], v241 offset:18432\n"
  "ds_read_b64_tr_b16 v[198:199], v241 offset:18944\n"
  "v_mfma_f32_32x32x16_bf16 v[160:175], v[116:119], v[108:111], v[160:175]\n"
  "ds_read_b128 v[200:203], v239 offset:57344\n"
  "ds_read_b64_tr_b16 v[204:205], v250 offset:34816\n"
  "ds_read_b64_tr_b16 v[206:207], v250 offset:35072\n"
  "ds_read_b64_tr_b16 v[64:65], v241 offset:20480\n"
  "ds_read_b64_tr_b16 v[66:67], v241 offset:20992\n"
  "v_mfma_f32_32x32x16_bf16 v[176:191], v[112:115], v[108:111], v[176:191]\n"
  "ds_read_b128 v[208:211], v240 offset:57344\n"
  "ds_read_b64_tr_b16 v[212:213], v250 offset:35840\n"
  "ds_read_b64_tr_b16 v[214:215], v250 offset:36096\n"
  "ds_read_b64_tr_b16 v[68:69], v241 offset:22528\n"
  "ds_read_b64_tr_b16 v[70:71], v241 offset:23040\n"
  "v_mfma_f32_32x32x16_bf16 v[160:175], v[128:131], v[120:123], v[160:175]\n"
  "v_and_b32 v238, 0x3f0, v251\n"
  "v_lshl_add_u32 v249, s22, 11, v238\n"
  "v_lshl_add_u32 v250, s23, 11, v238\n"
  "v_lshl_add_u32 v241, s21, 11, v238\n"
  "v_add_u32 v245, s55, v238\n"
  "v_mfma_f32_32x32x16_bf16 v[176:191], v[124:127], v[120:123], v[176:191]\n"
  "v_add_u32 v246, s60, v238\n"
  "v_add_u32 v247, s61, v238\n"
  "v_add_u32 v248, s63, v238\n"
  "v_mov_b32 v237, 0xdddddddd\n"
  "v_lshrrev_b32 v240, 7, v238\n"
  "v_mfma_f32_32x32x16_bf16 v[160:175], v[140:143], v[132:135], v[160:175]\n"
  "v_lshrrev_b32 v242, 2, v238\n"
  "v_and_b32 v242, 64, v242\n"
  "v_and_b32 v239, 0x70, v238\n"
  "v_mul_u32_u24 v240, 0x380, v240\n"
  "v_xad_u32 v239, v239, v242, v240\n"
  "v_mfma_f32_32x32x16_bf16 v[176:191], v[136:139], v[132:135], v[176:191]\n"
  "v_add_u32 v239, s64, v239\n"
  "v_bfe_u32 v240, v238, 4, 3\n"
  "v_and_b32 v242, 4, v240\n"
  "v_add_lshl_u32 v240, v240, v242, 11\n"
  "s_waitcnt vmcnt(0)\n"
  "v_xor_b32 v254, v254, v252\n"
  "v_xor_b32 v255, v255, v253\n"
  "v_or_b32 v254, v254, v255\n"
  "v_cmp_ne_u32 vcc, 0, v254\n"
  "s_cbranch_vccz LDa011%=\n"
  "s_mov_b32 s69, 0x40000\n"
  "LPa011%=:\n"
  "s_sleep 1\n"
  "buffer_load_dwordx2 v[254:255], v232, s[44:47], 0 offen sc1\n"
  "s_waitcnt vmcnt(0)\n"
  "v_xor_b32 v254, v254, v252\n"
  "v_xor_b32 v255, v255, v253\n"
  "v_or_b32 v254, v254, v255\n"
  "v_cmp_ne_u32 vcc, 0, v254\n"
  "s_cbranch_vccz LDa011%=\n"
  "s_sub_u32 s69, s69, 1\n"
  "s_cmp_lg_u32 s69, 0\n"
  "s_cbranch_scc1 LPa011%=\n"
  "LDa011%=:\n"
  "s_add_u32 s30, s6, 0\n"
  "s_addc_u32 s31, s7, 0\n"
  "global_load_dwordx4 v[0:3], v233, s[30:31] sc1\n"
  "global_load_dwordx4 v[4:7], v234, s[30:31] sc1\n"
  "s_waitcnt lgkmcnt(14)\n"
  "v_mfma_f32_32x32x16_bf16 v[176:191], v[148:151], v[144:147], v[176:191]\n"
  "s_add_u32 s30, s6, 229376\n"
  "s_addc_u32 s31, s7, 0\n"
  "global_load_dwordx4 v[16:19], v233, s[30:31] sc1\n"
  "global_load_dwordx4 v[20:23], v234, s[30:31] sc1\n"
  "s_waitcnt lgkmcnt(10)\n"
  "v_mfma_f32_32x32x16_bf16 v[176:191], v[192:195], v[156:159], v[176:191]\n"
  "s_add_u32 s30, s6, 0\n"
  "s_addc_u32 s31, s7, 0\n"
  "s_mov_b64 exec, s[70:71]\n"
  "global_load_dwordx4 v[8:11], v235, s[30:31] sc1\n"
  "global_load_dwordx4 v[12:15], v236, s[30:31] sc1\n"
  "s_mov_b64 exec, -1\n"
  "s_waitcnt lgkmcnt(5)\n"
  "v_mfma_f32_32x32x16_bf16 v[176:191], v[204:207], v[200:203], v[176:191]\n"
  "s_add_u32 s30, s6, 229376\n"
  "s_addc_u32 s31, s7, 0\n"
  "s_mov_b64 exec, s[70:71]\n"
  "global_load_dwordx4 v[24:27], v235, s[30:31] sc1\n"
  "global_load_dwordx4 v[28:31], v236, s[30:31] sc1\n"
  "s_mov_b64 exec, -1\n"
  "s_waitcnt lgkmcnt(0)\n"
  "v_mfma_f32_32x32x16_bf16 v[176:191], v[212:215], v[208:211], v[176:191]\n"
  "v_mfma_f32_32x32x16_bf16 v[160:175], v[152:155], v[144:147], v[160:175]\n"
  "v_mfma_f32_32x32x16_bf16 v[160:175], v[196:199], v[156:159], v[160:175]\n"
  "s_nop 9\n"
  "v_mul_f32 v176, s29, v176\n"
  "v_mul_f32 v177, s29, v177\n"
  "v_mul_f32 v178, s29, v178\n"
  "v_mul_f32 v179, s29, v179\n"
  "v_mul_f32 v180, s29, v180\n"
  "v_mul_f32 v181, s29, v181\n"
  "v_mfma_f32_32x32x16_bf16 v[160:175], v[64:67], v[200:203], v[160:175]\n"
  "v_mul_f32 v182, s29, v182\n"
  "v_mul_f32 v183, s29, v183\n"
  "v_mul_f32 v184, s29, v184\n"
  "v_mul_f32 v185, s29, v185\n"
  "v_mul_f32 v186, s29, v186\n"
  "v_mul_f32 v187, s29, v187\n"
  "v_mfma_f32_32x32x16_bf16 v[160:175], v[68:71], v[208:211], v[160:175]\n"
  "v_mul_f32 v188, s29, v188\n"
  "v_mul_f32 v189, s29, v189\n"
  "v_mul_f32 v190, s29, v190\n"
  "v_mul_f32 v191, s29, v191\n"
  "v_cvt_pk_bf16_f32 v176, v176, v177\n"
  "v_cvt_pk_bf16_f32 v177, v178, v179\n"
  "v_mfma_f32_32x32x16_bf16 v[96:111], v[48:51], v[48:51], 0\n"
  "v_cvt_pk_bf16_f32 v178, v180, v181\n"
  "v_cvt_pk_bf16_f32 v179, v182, v183\n"
  "v_cvt_pk_bf16_f32 v180, v184, v185\n"
  "v_cvt_pk_bf16_f32 v181, v186, v187\n"
  "v_cvt_pk_bf16_f32 v182, v188, v189\n"
  "v_cvt_pk_bf16_f32 v183, v190, v191\n"
  "v_mfma_f32_32x32x16_bf16 v[112:127], v[48:51], v[48:51], 0\n"
  "ds_write_b128 v251, v[176:179]\n"
  "v_cndmask_b32_e64 v182, v182, 0, s[4:5]\n"
  "v_cndmask_b32_e64 v183, v183, 0, s[4:5]\n"
  "ds_write_b128 v251, v[180:183] offset:1024\n"
  "v_mfma_f32_32x32x16_bf16 v[128:143], v[48:51], v[48:51], 0\n"
  "v_mfma_f32_32x32x16_bf16 v[144:159], v[48:51], v[48:51], 0\n"
  "s_branch LE%=\n"
  "LV%=:\n"
  "s_waitcnt vmcnt(12) lgkmcnt(0)\n"
  "s_barrier\n"
  "ds_read_b128 v[80:83], v237 offset:8192\n"
  "ds_read_b64_tr_b16 v[0:1], v241 offset:0\n"
  "ds_read_b64_tr_b16 v[2:3], v241 offset:512\n"
  "ds_read_b128 v[84:87], v238 offset:8192\n"
  "ds_read_b64_tr_b16 v[4:5], v241 offset:2048\n"
  "ds_read_b64_tr_b16 v[6:7], v241 offset:2560\n"
  "ds_read_b128 v[88:91], v239 offset:8192\n"
  "ds_read_b64_tr_b16 v[8:9], v241 offset:4096\n"
  "ds_read_b64_tr_b16 v[10:11], v241 offset:4608\n"
  "ds_read_b128 v[92:95], v240 offset:8192\n"
  "ds_read_b64_tr_b16 v[12:13], v241 offset:6144\n"
  "ds_read_b64_tr_b16 v[14:15], v241 offset:6656\n"
  "s_add_u32 m0, s53, 65536\n"
  "s_mul_i32 s65, s52, 1344\n"
  "buffer_load_dwordx4 v232, s[40:43], s65 offen lds\n"
  "s_add_u32 m0, s53, 66560\n"
  "s_nop 0\n"
  "buffer_load_dwordx4 v233, s[40:43], s65 offen lds\n"
  "s_add_u32 m0, s53, 73728\n"
  "s_nop 0\n"
  "buffer_load_dwordx4 v234, s[36:39], s52 offen lds\n"
  "s_add_u32 m0, s53, 74752\n"
  "s_nop 0\n"
  "buffer_load_dwordx4 v235, s[36:39], s52 offen lds\n"
  "s_add_u32 s52, s52, 128\n"
  "s_cmp_eq_u32 s52, 896\n"
  "s_cselect_b32 s52, 0, s52\n"
  "s_waitcnt vmcnt(12) lgkmcnt(0)\n"
  "s_barrier\n"
  "ds_read_b128 v[96:99], v237 offset:24576\n"
  "ds_read_b64_tr_b16 v[16:17], v241 offset:16384\n"
  "ds_read_b64_tr_b16 v[18:19], v241 offset:16896\n"
  "ds_read_b128 v[100:103], v238 offset:24576\n"
  "ds_read_b64_tr_b16 v[20:21], v241 offset:18432\n"
  "ds_read_b64_tr_b16 v[22:23], v241 offset:18944\n"
  "ds_read_b128 v[104:107], v239 offset:24576\n"
  "ds_read_b64_tr_b16 v[24:25], v241 offset:20480\n"
  "ds_read_b64_tr_b16 v[26:27], v241 offset:20992\n"
  "v_mfma_f32_32x32x16_bf16 v[160:175], v[80:83], v[0:3], 0\n"
  "ds_read_b128 v[108:111], v240 offset:24576\n"
  "ds_read_b64_tr_b16 v[28:29], v241 offset:22528\n"
  "ds_read_b64_tr_b16 v[30:31], v241 offset:23040\n"
  "v_mfma_f32_32x32x16_bf16 v[160:175], v[84:87], v[4:7], v[160:175]\n"
  "s_add_u32 m0, s53, 0\n"
  "s_mul_i32 s65, s52, 1344\n"
  "buffer_load_dwordx4 v232, s[40:43], s65 offen lds\n"
  "v_mfma_f32_32x32x16_bf16 v[160:175], v[88:91], v[8:11], v[160:175]\n"
  "s_add_u32 m0, s53, 1024\n"
  "s_nop 0\n"
  "buffer_load_dwordx4 v233, s[40:43], s65 offen lds\n"
  "v_mfma_f32_32x32x16_bf16 v[160:175], v[92:95], v[12:15], v[160:175]\n"
  "s_add_u32 m0, s53, 8192\n"
  "s_nop 0\n"
  "buffer_load_dwordx4 v234, s[36:39], s52 offen lds\n"
  "s_add_u32 m0, s53, 9216\n"
  "s_nop 0\n"
  "buffer_load_dwordx4 v235, s[36:39], s52 offen lds\n"
  "s_add_u32 s52, s52, 128\n"
  "s_cmp_eq_u32 s52, 896\n"
  "s_cselect_b32 s52, 0, s52\n"
  "s_waitcnt vmcnt(12) lgkmcnt(0)\n"
  "s_barrier\n"
  "ds_read_b128 v[112:115], v237 offset:40960\n"
  "ds_read_b64_tr_b16 v[32:33], v241 offset:32768\n"
  "ds_read_b64_tr_b16 v[34:35], v241 offset:33280\n"
  "ds_read_b128 v[116:119], v238 offset:40960\n"
  "ds_read_b64_tr_b16 v[36:37], v241 offset:34816\n"
  "ds_read_b64_tr_b16 v[38:39], v241 offset:35328\n"
  "ds_read_b128 v[120:123], v239 offset:40960\n"
  "ds_read_b64_tr_b16 v[40:41], v241 offset:36864\n"
  "ds_read_b64_tr_b16 v[42:43], v241 offset:37376\n"
  "v_mfma_f32_32x32x16_bf16 v[160:175], v[96:99], v[16:19], v[160:175]\n"
  "ds_read_b128 v[124:127], v240 offset:40960\n"
  "ds_read_b64_tr_b16 v[44:45], v241 offset:38912\n"
  "ds_read_b64_tr_b16 v[46:47], v241 offset:39424\n"
  "v_mfma_f32_32x32x16_bf16 v[160:175], v[100:103], v[20:23], v[160:175]\n"
  "s_add_u32 m0, s53, 16384\n"
  "s_mul_i32 s65, s52, 1344\n"
  "buffer_load_dwordx4 v232, s[40:43], s65 offen lds\n"
  "v_mfma_f32_32x32x16_bf16 v[160:175], v[104:107], v[24:27], v[160:175]\n"
  "s_add_u32 m0, s53, 17408\n"
  "s_nop 0\n"
  "buffer_load_dwordx4 v233, s[40:43], s65 offen lds\n"
  "v_mfma_f32_32x32x16_bf16 v[160:175], v[108:111], v[28:31], v[160:175]\n"
  "s_add_u32 m0, s53, 24576\n"
  "s_nop 0\n"
  "buffer_load_dwordx4 v234, s[36:39], s52 offen lds\n"
  "s_add_u32 m0, s53, 25600\n"
  "s_nop 0\n"
  "buffer_load_dwordx4 v235, s[36:39], s52 offen lds\n"
  "s_add_u32 s52, s52, 128\n"
  "s_cmp_eq_u32 s52, 896\n"
  "s_cselect_b32 s52, 0, s52\n"
  "s_waitcnt vmcnt(12) lgkmcnt(0)\n"
  "s_barrier\n"
  "ds_read_b128 v[128:131], v237 offset:57344\n"
  "ds_read_b64_tr_b16 v[48:49], v241 offset:49152\n"
  "ds_read_b64_tr_b16 v[50:51], v241 offset:49664\n"
  "ds_read_b128 v[132:135], v238 offset:57344\n"
  "ds_read_b64_tr_b16 v[52:53], v241 offset:51200\n"
  "ds_read_b64_tr_b16 v[54:55], v241 offset:51712\n"
  "ds_read_b128 v[136:139], v239 offset:57344\n"
  "ds_read_b64_tr_b16 v[56:57], v241 offset:53248\n"
  "ds_read_b64_tr_b16 v[58:59], v241 offset:53760\n"
  "v_mfma_f32_32x32x16_bf16 v[160:175], v[112:115], v[32:35], v[160:175]\n"
  "ds_read_b128 v[140:143], v240 offset:57344\n"
  "ds_read_b64_tr_b16 v[60:61], v241 offset:55296\n"
  "ds_read_b64_tr_b16 v[62:63], v241 offset:55808\n"
  "v_mfma_f32_32x32x16_bf16 v[160:175], v[116:119], v[36:39], v[160:175]\n"
  "s_add_u32 m0, s53, 32768\n"
  "s_add_u32 s66, s52, 57344\n"
  "buffer_load_dwordx4 v234, s[36:39], s66 offen lds\n"
  "v_mfma_f32_32x32x16_bf16 v[160:175], v[120:123], v[40:43], v[160:175]\n"
  "s_add_u32 m0, s53, 33792\n"
  "s_nop 0\n"
  "buffer_load_dwordx4 v235, s[36:39], s66 offen lds\n"
  "v_mfma_f32_32x32x16_bf16 v[160:175], v[124:127], v[44:47], v[160:175]\n"
  "s_add_u32 m0, s68, 40960\n"
  "s_mul_i32 s65, s52, 1344\n"
  "buffer_load_dwordx4 v236, s[40:43], s65 offen lds\n"
  "s_add_u32 s52, s52, 128\n"
  "s_cmp_eq_u32 s52, 896\n"
  "s_cselect_b32 s52, 0, s52\n"
  "s_waitcnt vmcnt(11) lgkmcnt(0)\n"
  "s_barrier\n"
  "s_add_u32 s26, s26, 1\n"
  "s_addc_u32 s27, s27, 0\n"
  "v_mov_b32 v252, s26\n"
  "v_mov_b32 v253, s27\n"
  "ds_read_b128 v[144:147], v245 offset:40960\n"
  "ds_read_b64_tr_b16 v[64:65], v249 offset:32768\n"
  "ds_read_b64_tr_b16 v[66:67], v249 offset:33280\n"
  "ds_read_b128 v[148:151], v246 offset:40960\n"
  "ds_read_b64_tr_b16 v[68:69], v249 offset:34816\n"
  "ds_read_b64_tr_b16 v[70:71], v249 offset:35328\n"
  "ds_read_b128 v[152:155], v247 offset:40960\n"
  "ds_read_b64_tr_b16 v[72:73], v249 offset:36864\n"
  "ds_read_b64_tr_b16 v[74:75], v249 offset:37376\n"
  "v_mfma_f32_32x32x16_bf16 v[160:175], v[128:131], v[48:51], v[160:175]\n"
  "ds_read_b128 v[156:159], v248 offset:40960\n"
  "ds_read_b64_tr_b16 v[76:77], v249 offset:38912\n"
  "ds_read_b64_tr_b16 v[78:79], v249 offset:39424\n"
  "v_mfma_f32_32x32x16_bf16 v[160:175], v[132:135], v[52:55], v[160:175]\n"
  "s_add_u32 m0, s53, 45056\n"
  "s_add_u32 s66, s52, 57344\n"
  "buffer_load_dwordx4 v234, s[36:39], s66 offen lds\n"
  "v_mfma_f32_32x32x16_bf16 v[160:175], v[136:139], v[56:59], v[160:175]\n"
  "s_add_u32 m0, s53, 46080\n"
  "s_nop 0\n"
  "buffer_load_dwordx4 v235, s[36:39], s66 offen lds\n"
  "v_mfma_f32_32x32x16_bf16 v[160:175], v[140:143], v[60:63], v[160:175]\n"
  "s_add_u32 m0, s68, 53248\n"
  "s_mul_i32 s65, s52, 1344\n"
  "buffer_load_dwordx4 v236, s[40:43], s65 offen lds\n"
  "s_add_u32 s52, s52, 128\n"
  "s_cmp_eq_u32 s52, 896\n"
  "s_cselect_b32 s52, 0, s52\n"
  "s_waitcnt vmcnt(10) lgkmcnt(0)\n"
  "s_barrier\n"
  "ds_read_b128 v[196:199], v237 offset:8192\n"
  "ds_read_b64_tr_b16 v[192:193], v241 offset:0\n"
  "ds_read_b64_tr_b16 v[194:195], v241 offset:512\n"
  "ds_read_b128 v[204:207], v238 offset:8192\n"
  "ds_read_b64_tr_b16 v[200:201], v241 offset:2048\n"
  "ds_read_b64_tr_b16 v[202:203], v241 offset:2560\n"
  "ds_read_b128 v[212:215], v239 offset:8192\n"
  "ds_read_b64_tr_b16 v[208:209], v241 offset:4096\n"
  "ds_read_b64_tr_b16 v[210:211], v241 offset:4608\n"
  "v_mfma_f32_32x32x16_bf16 v[160:175], v[144:147], v[64:67], v[160:175]\n"
  "ds_read_b128 v[220:223], v240 offset:8192\n"
  "ds_read_b64_tr_b16 v[216:217], v241 offset:6144\n"
  "ds_read_b64_tr_b16 v[218:219], v241 offset:6656\n"
  "v_mfma_f32_32x32x16_bf16 v[160:175], v[148:151], v[68:71], v[160:175]\n"
  "s_add_u32 m0, s53, 57344\n"
  "s_add_u32 s66, s52, 57344\n"
  "buffer_load_dwordx4 v234, s[36:39], s66 offen lds\n"
  "v_mfma_f32_32x32x16_bf16 v[160:175], v[152:155], v[72:75], v[160:175]\n"
  "s_add_u32 m0, s53, 58368\n"
  "s_nop 0\n"
  "buffer_load_dwordx4 v235, s[36:39], s66 offen lds\n"
  "v_mfma_f32_32x32x16_bf16 v[160:175], v[156:159], v[76:79], v[160:175]\n"
  "s_add_u32 m0, s68, 65536\n"
  "s_mul_i32 s65, s52, 1344\n"
  "buffer_load_dwordx4 v236, s[40:43], s65 offen lds\n"
  "s_add_u32 s52, s52, 128\n"
  "s_cmp_eq_u32 s52, 896\n"
  "s_cselect_b32 s52, 0, s52\n"
  "s_waitcnt lgkmcnt(9)\n"
  "v_mfma_f32_32x32x16_bf16 v[160:175], v[196:199], v[192:195], v[160:175]\n"
  "s_waitcnt lgkmcnt(6)\n"
  "v_mfma_f32_32x32x16_bf16 v[160:175], v[204:207], v[200:203], v[160:175]\n"
  "s_waitcnt lgkmcnt(3)\n"
  "v_mfma_f32_32x32x16_bf16 v[160:175], v[212:215], v[208:211], v[160:175]\n"
  "s_waitcnt lgkmcnt(0)\n"
  "v_mfma_f32_32x32x16_bf16 v[160:175], v[220:223], v[216:219], v[160:175]\n"
  "s_waitcnt vmcnt(9) lgkmcnt(0)\n"
  "s_barrier\n"
  "ds_read_b128 v[228:231], v237 offset:24576\n"
  "ds_read_b64_tr_b16 v[224:225], v241 offset:16384\n"
  "ds_read_b64_tr_b16 v[226:227], v241 offset:16896\n"
  "ds_read_b128 v[196:199], v238 offset:24576\n"
  "ds_read_b64_tr_b16 v[192:193], v241 offset:18432\n"
  "ds_read_b64_tr_b16 v[194:195], v241 offset:18944\n"
  "ds_read_b128 v[204:207], v239 offset:24576\n"
  "ds_read_b64_tr_b16 v[200:201], v241 offset:20480\n"
  "ds_read_b64_tr_b16 v[202:203], v241 offset:20992\n"
  "ds_read_b128 v[212:215], v240 offset:24576\n"
  "ds_read_b64_tr_b16 v[208:209], v241 offset:22528\n"
  "ds_read_b64_tr_b16 v[210:211], v241 offset:23040\n"
  "s_waitcnt lgkmcnt(9)\n"
  "v_mfma_f32_32x32x16_bf16 v[160:175], v[228:231], v[224:227], v[160:175]\n"
  "s_waitcnt lgkmcnt(6)\n"
  "v_mfma_f32_32x32x16_bf16 v[160:175], v[196:199], v[192:195], v[160:175]\n"
  "s_waitcnt lgkmcnt(3)\n"
  "v_mfma_f32_32x32x16_bf16 v[160:175], v[204:207], v[200:203], v[160:175]\n"
  "s_waitcnt lgkmcnt(0)\n"
  "v_mfma_f32_32x32x16_bf16 v[160:175], v[212:215], v[208:211], v[160:175]\n"
  "s_add_u32 s67, s56, s57\n"
  "s_bitcmp1_b32 s54, 0\n"
  "s_nop 9\n"
  "v_cvt_pk_bf16_f32 v216, v160, v161\n"
  "v_cvt_pk_bf16_f32 v217, v162, v163\n"
  "v_cvt_pk_bf16_f32 v218, v164, v165\n"
  "v_cvt_pk_bf16_f32 v219, v166, v167\n"
  "v_cvt_pk_bf16_f32 v220, v168, v169\n"
  "v_cvt_pk_bf16_f32 v221, v170, v171\n"
  "v_cvt_pk_bf16_f32 v222, v172, v173\n"
  "v_cvt_pk_bf16_f32 v223, v174, v175\n"
  "s_cbranch_scc1 LS1%=\n"
  "buffer_store_dwordx4 v[216:219], v243, s[44:47], s56 offen sc1\n"
  "buffer_store_dwordx4 v[220:223], v243, s[44:47], s67 offen sc1\n"
  "LS1%=:\n"
  "s_cmp_eq_u32 s54, 2\n"
  "s_cbranch_scc1 LT12%=\n"
  "s_cmp_eq_u32 s54, 1\n"
  "s_cbranch_scc1 LT11%=\n"
  "s_cmp_eq_u32 s54, 3\n"
  "s_cbranch_scc1 LT13%=\n"
  "s_waitcnt vmcnt(8) lgkmcnt(0)\n"
  "s_barrier\n"
  "ds_read_b128 v[192:195], v237 offset:32768\n"
  "ds_read_b64_tr_b16 v[196:197], v242 offset:40960\n"
  "ds_read_b64_tr_b16 v[198:199], v242 offset:41216\n"
  "ds_read_b128 v[200:203], v238 offset:32768\n"
  "ds_read_b64_tr_b16 v[204:205], v242 offset:41984\n"
  "ds_read_b64_tr_b16 v[206:207], v242 offset:42240\n"
  "ds_read_b128 v[208:211], v239 offset:32768\n"
  "ds_read_b64_tr_b16 v[212:213], v242 offset:43008\n"
  "ds_read_b64_tr_b16 v[214:215], v242 offset:43264\n"
  "ds_read_b128 v[224:227], v240 offset:32768\n"
  "ds_read_b64_tr_b16 v[228:229], v242 offset:44032\n"
  "ds_read_b64_tr_b16 v[230:231], v242 offset:44288\n"
  "v_mbcnt_lo_u32_b32 v254, -1, 0\n"
  "v_mbcnt_hi_u32_b32 v254, -1, v254\n"
  "v_bfe_u32 v255, v254, 4, 1\n"
  "v_mul_u32_u24 v232, s63, v255\n"
  "v_lshrrev_b32 v255, 5, v254\n"
  "v_lshl_add_u32 v232, v255, 14, v232\n"
  "v_bfe_u32 v255, v254, 2, 2\n"
  "v_lshl_add_u32 v232, v255, 7, v232\n"
  "v_add_u32 v232, s62, v232\n"
  "s_waitcnt vmcnt(5) lgkmcnt(0)\n"
  "s_barrier\n"
  "v_mfma_f32_32x32x16_bf16 v[160:175], v[192:195], v[0:3], 0\n"
  "ds_read_b128 v[216:219], v237 offset:45056\n"
  "ds_read_b64_tr_b16 v[220:221], v242 offset:53248\n"
  "ds_read_b64_tr_b16 v[222:223], v242 offset:53504\n"
  "v_mfma_f32_32x32x16_bf16 v[176:191], v[196:199], v[80:83], 0\n"
  "v_mfma_f32_32x32x16_bf16 v[160:175], v[200:203], v[4:7], v[160:175]\n"
  "ds_read_b128 v[192:195], v238 offset:45056\n"
  "ds_read_b64_tr_b16 v[196:197], v242 offset:54272\n"
  "ds_read_b64_tr_b16 v[198:199], v242 offset:54528\n"
  "v_mfma_f32_32x32x16_bf16 v[176:191], v[204:207], v[84:87], v[176:191]\n"
  "v_mfma_f32_32x32x16_bf16 v[160:175], v[208:211], v[8:11], v[160:175]\n"
  "ds_read_b128 v[80:83], v239 offset:45056\n"
  "ds_read_b64_tr_b16 v[200:201], v242 offset:55296\n"
  "ds_read_b64_tr_b16 v[202:203], v242 offset:55552\n"
  "v_mfma_f32_32x32x16_bf16 v[176:191], v[212:215], v[88:91], v[176:191]\n"
  "ds_read_b128 v[204:207], v240 offset:45056\n"
  "ds_read_b64_tr_b16 v[84:85], v242 offset:56320\n"
  "ds_read_b64_tr_b16 v[86:87], v242 offset:56576\n"
  "v_mfma_f32_32x32x16_bf16 v[160:175], v[224:227], v[12:15], v[160:175]\n"
  "v_mfma_f32_32x32x16_bf16 v[176:191], v[228:231], v[92:95], v[176:191]\n"
  "s_waitcnt vmcnt(0) lgkmcnt(0)\n"
  "s_barrier\n"
  "buffer_store_dwordx2 v[252:253], v244, s[44:47], 0 offen sc1\n"
  "v_mfma_f32_32x32x16_bf16 v[160:175], v[216:219], v[16:19], v[160:175]\n"
  "ds_read_b128 v[208:211], v237 offset:57344\n"
  "ds_read_b64_tr_b16 v[212:213], v250 offset:32768\n"
  "ds_read_b64_tr_b16 v[214:215], v250 offset:33024\n"
  "v_mfma_f32_32x32x16_bf16 v[176:191], v[220:223], v[96:99], v[176:191]\n"
  "ds_read_b128 v[88:91], v238 offset:57344\n"
  "ds_read_b64_tr_b16 v[224:225], v250 offset:33792\n"
  "ds_read_b64_tr_b16 v[226:227], v250 offset:34048\n"
  "v_mfma_f32_32x32x16_bf16 v[160:175], v[192:195], v[20:23], v[160:175]\n"
  "ds_read_b128 v[228:231], v239 offset:57344\n"
  "ds_read_b64_tr_b16 v[92:93], v250 offset:34816\n"
  "ds_read_b64_tr_b16 v[94:95], v250 offset:35072\n"
  "v_mfma_f32_32x32x16_bf16 v[176:191], v[196:199], v[100:103], v[176:191]\n"
  "ds_read_b128 v[216:219], v240 offset:57344\n"
  "ds_read_b64_tr_b16 v[220:221], v250 offset:35840\n"
  "ds_read_b64_tr_b16 v[222:223], v250 offset:36096\n"
  "v_mfma_f32_32x32x16_bf16 v[160:175], v[80:83], v[24:27], v[160:175]\n"
  "v_mov_b32 v8, 0x3f803f80\n"
  "v_mov_b32 v9, 0x3f803f80\n"
  "v_mov_b32 v10, 0x3f803f80\n"
  "v_mov_b32 v11, 0x3f803f80\n"
  "v_mfma_f32_32x32x16_bf16 v[176:191], v[200:203], v[104:107], v[176:191]\n"
  "v_mov_b32 v12, 0x3f803f80\n"
  "v_mov_b32 v13, 0x3f803f80\n"
  "v_mov_b32 v14, 0x3f803f80\n"
  "v_mov_b32 v15, 0x3f803f80\n"
  "v_mfma_f32_32x32x16_bf16 v[160:175], v[204:207], v[28:31], v[160:175]\n"
  "v_mfma_f32_32x32x16_bf16 v[176:191], v[84:87], v[108:111], v[176:191]\n"
  "s_waitcnt lgkmcnt(0)\n"
  "s_barrier\n"
  "v_mfma_f32_32x32x16_bf16 v[160:175], v[208:211], v[32:35], v[160:175]\n"
  "ds_read_b128 v[96:99], v245 offset:36864\n"
  "ds_read_b64_tr_b16 v[192:193], v250 offset:45056\n"
  "ds_read_b64_tr_b16 v[194:195], v250 offset:45312\n"
  "v_mfma_f32_32x32x16_bf16 v[176:191], v[212:215], v[112:115], v[176:191]\n"
  "ds_read_b128 v[196:199], v246 offset:36864\n"
  "ds_read_b64_tr_b16 v[100:101], v250 offset:46080\n"
  "ds_read_b64_tr_b16 v[102:103], v250 offset:46336\n"
  "v_mfma_f32_32x32x16_bf16 v[160:175], v[88:91], v[36:39], v[160:175]\n"
  "ds_read_b128 v[80:83], v247 offset:36864\n"
  "ds_read_b64_tr_b16 v[200:201], v250 offset:47104\n"
  "ds_read_b64_tr_b16 v[202:203], v250 offset:47360\n"
  "v_mfma_f32_32x32x16_bf16 v[176:191], v[224:227], v[116:119], v[176:191]\n"
  "ds_read_b128 v[104:107], v248 offset:36864\n"
  "ds_read_b64_tr_b16 v[204:205], v250 offset:48128\n"
  "ds_read_b64_tr_b16 v[206:207], v250 offset:48384\n"
  "v_mfma_f32_32x32x16_bf16 v[160:175], v[228:231], v[40:43], v[160:175]\n"
  "v_mov_b32 v24, 0x3f803f80\n"
  "v_mov_b32 v25, 0x3f803f80\n"
  "v_mov_b32 v26, 0x3f803f80\n"
  "v_mov_b32 v27, 0x3f803f80\n"
  "v_mfma_f32_32x32x16_bf16 v[176:191], v[92:95], v[120:123], v[176:191]\n"
  "v_mov_b32 v28, 0x3f803f80\n"
  "v_mov_b32 v29, 0x3f803f80\n"
  "v_mov_b32 v30, 0x3f803f80\n"
  "v_mov_b32 v31, 0x3f803f80\n"
  "v_mfma_f32_32x32x16_bf16 v[160:175], v[216:219], v[44:47], v[160:175]\n"
  "v_mfma_f32_32x32x16_bf16 v[176:191], v[220:223], v[124:127], v[176:191]\n"
  "s_waitcnt lgkmcnt(0)\n"
  "s_barrier\n"
  "v_mfma_f32_32x32x16_bf16 v[160:175], v[96:99], v[48:51], v[160:175]\n"
  "ds_read_b128 v[84:87], v237 offset:32768\n"
  "ds_read_b64_tr_b16 v[108:109], v242 offset:40960\n"
  "ds_read_b64_tr_b16 v[110:111], v242 offset:41216\n"
  "v_mfma_f32_32x32x16_bf16 v[176:191], v[192:195], v[128:131], v[176:191]\n"
  "ds_read_b128 v[208:211], v238 offset:32768\n"
  "ds_read_b64_tr_b16 v[212:213], v242 offset:41984\n"
  "ds_read_b64_tr_b16 v[214:215], v242 offset:42240\n"
  "v_mfma_f32_32x32x16_bf16 v[160:175], v[196:199], v[52:55], v[160:175]\n"
  "ds_read_b128 v[112:115], v239 offset:32768\n"
  "ds_read_b64_tr_b16 v[88:89], v242 offset:43008\n"
  "ds_read_b64_tr_b16 v[90:91], v242 offset:43264\n"
  "v_mfma_f32_32x32x16_bf16 v[176:191], v[100:103], v[132:135], v[176:191]\n"
  "ds_read_b128 v[224:227], v240 offset:32768\n"
  "ds_read_b64_tr_b16 v[116:117], v242 offset:44032\n"
  "ds_read_b64_tr_b16 v[118:119], v242 offset:44288\n"
  "v_mfma_f32_32x32x16_bf16 v[160:175], v[80:83], v[56:59], v[160:175]\n"
  "v_mbcnt_lo_u32_b32 v236, -1, 0\n"
  "v_mbcnt_hi_u32_b32 v236, -1, v236\n"
  "v_and_b32 v235, 31, v236\n"
  "v_lshrrev_b32 v236, 5, v236\n"
  "v_lshl_add_u32 v233, v235, 4, s58\n"
  "v_mad_i32_i24 v233, v236, s59, v233\n"
  "v_lshl_add_u32 v234, s59, 1, v233\n"
  "v_mad_i32_i24 v234, v236, s60, v234\n"
  "v_mfma_f32_32x32x16_bf16 v[176:191], v[200:203], v[136:139], v[176:191]\n"
  "v_lshl_add_u32 v235, v235, 4, s61\n"
  "v_mul_u32_u24 v236, 0x1c00, v236\n"
  "v_add_u32 v235, v235, v236\n"
  "v_add_u32 v236, 0x3800, v235\n"
  "s_mul_i32 s52, s54, 917504\n"
  "s_add_u32 s52, s52, 1048576\n"
  "s_add_u32 s6, s44, s52\n"
  "s_addc_u32 s7, s45, 0\n"
  "v_mfma_f32_32x32x16_bf16 v[160:175], v[104:107], v[60:63], v[160:175]\n"
  "s_mov_b32 s70, 0x0fffffff\n"
  "s_mov_b32 s71, 0x0fffffff\n"
  "v_mov_b32 v40, 0x3f803f80\n"
  "v_mov_b32 v41, 0x3f803f80\n"
  "v_mov_b32 v42, 0x3f803f80\n"
  "v_mov_b32 v43, 0x3f803f80\n"
  "v_mfma_f32_32x32x16_bf16 v[176:191], v[204:207], v[140:143], v[176:191]\n"
  "v_mov_b32 v44, 0x3f803f80\n"
  "v_mov_b32 v45, 0x3f803f80\n"
  "v_mov_b32 v46, 0x3f803f80\n"
  "v_mov_b32 v47, 0x3f803f80\n"
  "s_waitcnt lgkmcnt(0)\n"
  "s_barrier\n"
  "s_mov_b32 s28, 0x8000\n"
  "buffer_store_dwordx2 v[252:253], v244, s[44:47], s28 offen sc1\n"
  "v_mfma_f32_32x32x16_bf16 v[160:175], v[84:87], v[64:67], v[160:175]\n"
  "ds_read_b128 v[228:231], v237 offset:45056\n"
  "ds_read_b64_tr_b16 v[92:93], v242 offset:53248\n"
  "ds_read_b64_tr_b16 v[94:95], v242 offset:53504\n"
  "ds_read_b64_tr_b16 v[120:121], v241 offset:0\n"
  "ds_read_b64_tr_b16 v[122:123], v241 offset:512\n"
  "ds_read_b128 v[216:219], v237 offset:8192\n"
  "v_mfma_f32_32x32x16_bf16 v[176:191], v[108:111], v[144:147], v[176:191]\n"
  "ds_read_b128 v[220:223], v238 offset:45056\n"
  "ds_read_b64_tr_b16 v[124:125], v242 offset:54272\n"
  "ds_read_b64_tr_b16 v[126:127], v242 offset:54528\n"
  "ds_read_b64_tr_b16 v[96:97], v241 offset:2048\n"
  "ds_read_b64_tr_b16 v[98:99], v241 offset:2560\n"
  "ds_read_b128 v[192:195], v238 offset:8192\n"
  "v_mfma_f32_32x32x16_bf16 v[160:175], v[208:211], v[68:71], v[160:175]\n"
  "ds_read_b128 v[128:131], v239 offset:45056\n"
  "ds_read_b64_tr_b16 v[196:197], v242 offset:55296\n"
  "ds_read_b64_tr_b16 v[198:199], v242 offset:55552\n"
  "ds_read_b64_tr_b16 v[100:101], v241 offset:4096\n"
  "ds_read_b64_tr_b16 v[102:103], v241 offset:4608\n"
  "ds_read_b128 v[132:135], v239 offset:8192\n"
  "v_mfma_f32_32x32x16_bf16 v[176:191], v[212:215], v[148:151], v[176:191]\n"
  "ds_read_b128 v[80:83], v240 offset:45056\n"
  "ds_read_b64_tr_b16 v[200:201], v242 offset:56320\n"
  "ds_read_b64_tr_b16 v[202:203], v242 offset:56576\n"
  "ds_read_b64_tr_b16 v[136:137], v241 offset:6144\n"
  "ds_read_b64_tr_b16 v[138:139], v241 offset:6656\n"
  "ds_read_b128 v[104:107], v240 offset:8192\n"
  "v_mfma_f32_32x32x16_bf16 v[160:175], v[112:115], v[72:75], v[160:175]\n"
  "buffer_load_dwordx2 v[254:255], v232, s[44:47], 0 offen sc1\n"
  "v_mov_b32 v56, 0x3f803f80\n"
  "v_mov_b32 v57, 0x3f803f80\n"
  "v_mov_b32 v58, 0x3f803f80\n"
  "v_mov_b32 v59, 0x3f803f80\n"
  "v_mfma_f32_32x32x16_bf16 v[176:191], v[88:91], v[152:155], v[176:191]\n"
  "v_mov_b32 v60, 0x3f803f80\n"
  "v_mov_b32 v61, 0x3f803f80\n"
  "v_mov_b32 v62, 0x3f803f80\n"
  "v_mov_b32 v63, 0x3f803f80\n"
  "s_add_u32 s22, s54, 1\n"
  "s_and_b32 s22, s22, 3\n"
  "s_mul_i32 s55, s22, 12288\n"
  "s_add_u32 s23, s54, 2\n"
  "s_and_b32 s23, s23, 3\n"
  "s_mul_i32 s60, s23, 12288\n"
  "v_mfma_f32_32x32x16_bf16 v[160:175], v[224:227], v[76:79], v[160:175]\n"
  "s_add_u32 s60, s60, 4096\n"
  "s_add_u32 s21, s54, 3\n"
  "s_and_b32 s21, s21, 3\n"
  "s_mul_i32 s61, s21, 12288\n"
  "s_add_u32 s61, s61, 8192\n"
  "s_mul_i32 s63, s54, 12288\n"
  "s_mul_i32 s28, s54, 7168\n"
  "s_add_u32 s64, s64, s28\n"
  "s_lshl_b32 s28, s24, 6\n"
  "s_add_u32 s28, s28, s20\n"
  "s_lshl_b32 s28, s28, 7\n"
  "s_add_u32 s62, s28, 69632\n"
  "v_mfma_f32_32x32x16_bf16 v[176:191], v[116:119], v[156:159], v[176:191]\n"
  "s_mov_b32 s67, 0x8000\n"
  "s_mov_b32 s29, 0x3e8b97f6\n"
  "s_mov_b32 s4, 0\n"
  "s_mov_b32 s5, -1\n"
  "s_mov_b32 s28, 0x8000\n"
  "v_mov_b32 v48, 0\n"
  "v_mov_b32 v49, 0\n"
  "v_mov_b32 v50, 0\n"
  "v_mov_b32 v51, 0\n"
  "s_waitcnt lgkmcnt(0)\n"
  "s_barrier\n"
  "v_mfma_f32_32x32x16_bf16 v[160:175], v[228:231], v[120:123], v[160:175]\n"
  "ds_read_b128 v[204:207], v237 offset:57344\n"
  "ds_read_b64_tr_b16 v[140:141], v250 offset:32768\n"
  "ds_read_b64_tr_b16 v[142:143], v250 offset:33024\n"
  "ds_read_b64_tr_b16 v[64:65], v241 offset:16384\n"
  "ds_read_b64_tr_b16 v[66:67], v241 offset:16896\n"
  "ds_read_b128 v[84:87], v237 offset:24576\n"
  "v_mfma_f32_32x32x16_bf16 v[176:191], v[92:95], v[216:219], v[176:191]\n"
  "ds_read_b128 v[108:111], v238 offset:57344\n"
  "ds_read_b64_tr_b16 v[144:145], v250 offset:33792\n"
  "ds_read_b64_tr_b16 v[146:147], v250 offset:34048\n"
  "ds_read_b64_tr_b16 v[68:69], v241 offset:18432\n"
  "ds_read_b64_tr_b16 v[70:71], v241 offset:18944\n"
  "ds_read_b128 v[208:211], v238 offset:24576\n"
  "v_mfma_f32_32x32x16_bf16 v[160:175], v[220:223], v[96:99], v[160:175]\n"
  "ds_read_b128 v[212:215], v239 offset:57344\n"
  "ds_read_b64_tr_b16 v[148:149], v250 offset:34816\n"
  "ds_read_b64_tr_b16 v[150:151], v250 offset:35072\n"
  "ds_read_b64_tr_b16 v[72:73], v241 offset:20480\n"
  "ds_read_b64_tr_b16 v[74:75], v241 offset:20992\n"
  "ds_read_b128 v[112:115], v239 offset:24576\n"
  "v_mfma_f32_32x32x16_bf16 v[176:191], v[124:127], v[192:195], v[176:191]\n"
  "ds_read_b128 v[88:91], v240 offset:57344\n"
  "ds_read_b64_tr_b16 v[152:153], v250 offset:35840\n"
  "ds_read_b64_tr_b16 v[154:155], v250 offset:36096\n"
  "ds_read_b64_tr_b16 v[76:77], v241 offset:22528\n"
  "ds_read_b64_tr_b16 v[78:79], v241 offset:23040\n"
  "ds_read_b128 v[224:227], v240 offset:24576\n"
  "v_mfma_f32_32x32x16_bf16 v[160:175], v[128:131], v[100:103], v[160:175]\n"
  "v_and_b32 v238, 0x3f0, v251\n"
  "v_lshl_add_u32 v249, s22, 11, v238\n"
  "v_lshl_add_u32 v250, s23, 11, v238\n"
  "v_lshl_add_u32 v241, s21, 11, v238\n"
  "v_add_u32 v245, s55, v238\n"
  "v_mfma_f32_32x32x16_bf16 v[176:191], v[196:199], v[132:135], v[176:191]\n"
  "v_add_u32 v246, s60, v238\n"
  "v_add_u32 v247, s61, v238\n"
  "v_add_u32 v248, s63, v238\n"
  "v_mov_b32 v237, 0xdddddddd\n"
  "v_lshrrev_b32 v240, 7, v238\n"
  "v_mfma_f32_32x32x16_bf16 v[160:175], v[80:83], v[136:139], v[160:175]\n"
  "v_lshrrev_b32 v242, 2, v238\n"
  "v_and_b32 v242, 64, v242\n"
  "v_and_b32 v239, 0x70, v238\n"
  "v_mul_u32_u24 v240, 0x380, v240\n"
  "v_xad_u32 v239, v239, v242, v240\n"
  "v_mfma_f32_32x32x16_bf16 v[176:191], v[200:203], v[104:107], v[176:191]\n"
  "v_add_u32 v239, s64, v239\n"
  "v_bfe_u32 v240, v238, 4, 3\n"
  "v_and_b32 v242, 4, v240\n"
  "v_add_lshl_u32 v240, v240, v242, 11\n"
  "s_waitcnt vmcnt(0)\n"
  "v_xor_b32 v254, v254, v252\n"
  "v_xor_b32 v255, v255, v253\n"
  "v_or_b32 v254, v254, v255\n"
  "v_cmp_ne_u32 vcc, 0, v254\n"
  "s_cbranch_vccz LDa100%=\n"
  "s_mov_b32 s69, 0x40000\n"
  "LPa100%=:\n"
  "s_sleep 1\n"
  "buffer_load_dwordx2 v[254:255], v232, s[44:47], 0 offen sc1\n"
  "s_waitcnt vmcnt(0)\n"
  "v_xor_b32 v254, v254, v252\n"
  "v_xor_b32 v255, v255, v253\n"
  "v_or_b32 v254, v254, v255\n"
  "v_cmp_ne_u32 vcc, 0, v254\n"
  "s_cbranch_vccz LDa100%=\n"
  "s_sub_u32 s69, s69, 1\n"
  "s_cmp_lg_u32 s69, 0\n"
  "s_cbranch_scc1 LPa100%=\n"
  "LDa100%=:\n"
  "s_add_u32 s30, s6, 0\n"
  "s_addc_u32 s31, s7, 0\n"
  "global_load_dwordx4 v[0:3], v233, s[30:31] sc1\n"
  "global_load_dwordx4 v[4:7], v234, s[30:31] sc1\n"
  "s_waitcnt lgkmcnt(14)\n"
  "v_mfma_f32_32x32x16_bf16 v[176:191], v[140:143], v[84:87], v[176:191]\n"
  "s_add_u32 s30, s6, 229376\n"
  "s_addc_u32 s31, s7, 0\n"
  "global_load_dwordx4 v[16:19], v233, s[30:31] sc1\n"
  "global_load_dwordx4 v[20:23], v234, s[30:31] sc1\n"
  "s_waitcnt lgkmcnt(12)\n"
  "v_mfma_f32_32x32x16_bf16 v[176:191], v[144:147], v[208:211], v[176:191]\n"
  "s_add_u32 s30, s6, 0\n"
  "s_addc_u32 s31, s7, 0\n"
  "s_mov_b64 exec, s[70:71]\n"
  "global_load_dwordx4 v[8:11], v235, s[30:31] sc1\n"
  "global_load_dwordx4 v[12:15], v236, s[30:31] sc1\n"
  "s_mov_b64 exec, -1\n"
  "s_waitcnt lgkmcnt(6)\n"
  "v_mfma_f32_32x32x16_bf16 v[176:191], v[148:151], v[112:115], v[176:191]\n"
  "s_add_u32 s30, s6, 229376\n"
  "s_addc_u32 s31, s7, 0\n"
  "s_mov_b64 exec, s[70:71]\n"
  "global_load_dwordx4 v[24:27], v235, s[30:31] sc1\n"
  "global_load_dwordx4 v[28:31], v236, s[30:31] sc1\n"
  "s_mov_b64 exec, -1\n"
  "s_waitcnt lgkmcnt(0)\n"
  "v_mfma_f32_32x32x16_bf16 v[176:191], v[152:155], v[224:227], v[176:191]\n"
  "v_mfma_f32_32x32x16_bf16 v[160:175], v[204:207], v[64:67], v[160:175]\n"
  "v_mfma_f32_32x32x16_bf16 v[160:175], v[108:111], v[68:71], v[160:175]\n"
  "s_nop 9\n"
  "v_mul_f32 v176, s29, v176\n"
  "v_mul_f32 v177, s29, v177\n"
  "v_mul_f32 v178, s29, v178\n"
  "v_mul_f32 v179, s29, v179\n"
  "v_mul_f32 v180, s29, v180\n"
  "v_mul_f32 v181, s29, v181\n"
  "v_mfma_f32_32x32x16_bf16 v[160:175], v[212:215], v[72:75], v[160:175]\n"
  "v_mul_f32 v182, s29, v182\n"
  "v_mul_f32 v183, s29, v183\n"
  "v_mul_f32 v184, s29, v184\n"
  "v_mul_f32 v185, s29, v185\n"
  "v_mul_f32 v186, s29, v186\n"
  "v_mul_f32 v187, s29, v187\n"
  "v_mfma_f32_32x32x16_bf16 v[160:175], v[88:91], v[76:79], v[160:175]\n"
  "v_mul_f32 v188, s29, v188\n"
  "v_mul_f32 v189, s29, v189\n"
  "v_mul_f32 v190, s29, v190\n"
  "v_mul_f32 v191, s29, v191\n"
  "v_cvt_pk_bf16_f32 v176, v176, v177\n"
  "v_cvt_pk_bf16_f32 v177, v178, v179\n"
  "v_mfma_f32_32x32x16_bf16 v[96:111], v[48:51], v[48:51], 0\n"
  "v_cvt_pk_bf16_f32 v178, v180, v181\n"
  "v_cvt_pk_bf16_f32 v179, v182, v183\n"
  "v_cvt_pk_bf16_f32 v180, v184, v185\n"
  "v_cvt_pk_bf16_f32 v181, v186, v187\n"
  "v_cvt_pk_bf16_f32 v182, v188, v189\n"
  "v_cvt_pk_bf16_f32 v183, v190, v191\n"
  "v_mfma_f32_32x32x16_bf16 v[112:127], v[48:51], v[48:51], 0\n"
  "ds_write_b128 v251, v[176:179]\n"
  "v_cndmask_b32_e64 v182, v182, 0, s[4:5]\n"
  "v_cndmask_b32_e64 v183, v183, 0, s[4:5]\n"
  "ds_write_b128 v251, v[180:183] offset:1024\n"
  "v_mfma_f32_32x32x16_bf16 v[128:143], v[48:51], v[48:51], 0\n"
  "v_mfma_f32_32x32x16_bf16 v[144:159], v[48:51], v[48:51], 0\n"
  "s_branch LE%=\n"
  "LT12%=:\n"
  "s_waitcnt vmcnt(8) lgkmcnt(0)\n"
  "s_barrier\n"
  "ds_read_b128 v[192:195], v237 offset:32768\n"
  "ds_read_b64_tr_b16 v[196:197], v242 offset:40960\n"
  "ds_read_b64_tr_b16 v[198:199], v242 offset:41216\n"
  "ds_read_b128 v[200:203], v238 offset:32768\n"
  "ds_read_b64_tr_b16 v[204:205], v242 offset:41984\n"
  "ds_read_b64_tr_b16 v[206:207], v242 offset:42240\n"
  "ds_read_b128 v[208:211], v239 offset:32768\n"
  "ds_read_b64_tr_b16 v[212:213], v242 offset:43008\n"
  "ds_read_b64_tr_b16 v[214:215], v242 offset:43264\n"
  "ds_read_b128 v[224:227], v240 offset:32768\n"
  "ds_read_b64_tr_b16 v[228:229], v242 offset:44032\n"
  "ds_read_b64_tr_b16 v[230:231], v242 offset:44288\n"
  "v_mbcnt_lo_u32_b32 v254, -1, 0\n"
  "v_mbcnt_hi_u32_b32 v254, -1, v254\n"
  "v_bfe_u32 v255, v254, 4, 1\n"
  "v_mul_u32_u24 v232, s63, v255\n"
  "v_lshrrev_b32 v255, 5, v254\n"
  "v_lshl_add_u32 v232, v255, 14, v232\n"
  "v_bfe_u32 v255, v254, 2, 2\n"
  "v_lshl_add_u32 v232, v255, 7, v232\n"
  "v_add_u32 v232, s62, v232\n"
  "s_waitcnt vmcnt(5) lgkmcnt(0)\n"
  "s_barrier\n"
  "v_mfma_f32_32x32x16_bf16 v[160:175], v[192:195], v[0:3], 0\n"
  "ds_read_b128 v[216:219], v237 offset:45056\n"
  "ds_read_b64_tr_b16 v[220:221], v242 offset:53248\n"
  "ds_read_b64_tr_b16 v[222:223], v242 offset:53504\n"
  "v_mfma_f32_32x32x16_bf16 v[176:191], v[196:199], v[192:195], 0\n"
  "ds_read_b128 v[80:83], v238 offset:45056\n"
  "ds_read_b64_tr_b16 v[84:85], v242 offset:54272\n"
  "ds_read_b64_tr_b16 v[86:87], v242 offset:54528\n"
  "v_mfma_f32_32x32x16_bf16 v[160:175], v[200:203], v[4:7], v[160:175]\n"
  "ds_read_b128 v[88:91], v239 offset:45056\n"
  "ds_read_b64_tr_b16 v[92:93], v242 offset:55296\n"
  "ds_read_b64_tr_b16 v[94:95], v242 offset:55552\n"
  "v_mfma_f32_32x32x16_bf16 v[176:191], v[204:207], v[200:203], v[176:191]\n"
  "ds_read_b128 v[96:99], v240 offset:45056\n"
  "ds_read_b64_tr_b16 v[100:101], v242 offset:56320\n"
  "ds_read_b64_tr_b16 v[102:103], v242 offset:56576\n"
  "v_mfma_f32_32x32x16_bf16 v[160:175], v[208:211], v[8:11], v[160:175]\n"
  "v_mfma_f32_32x32x16_bf16 v[176:191], v[212:215], v[208:211], v[176:191]\n"
  "v_mfma_f32_32x32x16_bf16 v[160:175], v[224:227], v[12:15], v[160:175]\n"
  "v_mfma_f32_32x32x16_bf16 v[176:191], v[228:231], v[224:227], v[176:191]\n"
  "s_waitcnt vmcnt(0) lgkmcnt(0)\n"
  "s_barrier\n"
  "v_mfma_f32_32x32x16_bf16 v[160:175], v[216:219], v[16:19], v[160:175]\n"
  "ds_read_b128 v[104:107], v237 offset:57344\n"
  "ds_read_b64_tr_b16 v[108:109], v250 offset:32768\n"
  "ds_read_b64_tr_b16 v[110:111], v250 offset:33024\n"
  "v_mfma_f32_32x32x16_bf16 v[176:191], v[220:223], v[216:219], v[176:191]\n"
  "ds_read_b128 v[112:115], v238 offset:57344\n"
  "ds_read_b64_tr_b16 v[116:117], v250 offset:33792\n"
  "ds_read_b64_tr_b16 v[118:119], v250 offset:34048\n"
  "v_mfma_f32_32x32x16_bf16 v[160:175], v[80:83], v[20:23], v[160:175]\n"
  "ds_read_b128 v[120:123], v239 offset:57344\n"
  "ds_read_b64_tr_b16 v[124:125], v250 offset:34816\n"
  "ds_read_b64_tr_b16 v[126:127], v250 offset:35072\n"
  "v_mfma_f32_32x32x16_bf16 v[176:191], v[84:87], v[80:83], v[176:191]\n"
  "ds_read_b128 v[128:131], v240 offset:57344\n"
  "ds_read_b64_tr_b16 v[132:133], v250 offset:35840\n"
  "ds_read_b64_tr_b16 v[134:135], v250 offset:36096\n"
  "v_mfma_f32_32x32x16_bf16 v[160:175], v[88:91], v[24:27], v[160:175]\n"
  "v_mov_b32 v8, 0x3f803f80\n"
  "v_mov_b32 v9, 0x3f803f80\n"
  "v_mov_b32 v10, 0x3f803f80\n"
  "v_mov_b32 v11, 0x3f803f80\n"
  "v_mfma_f32_32x32x16_bf16 v[176:191], v[92:95], v[88:91], v[176:191]\n"
  "v_mov_b32 v12, 0x3f803f80\n"
  "v_mov_b32 v13, 0x3f803f80\n"
  "v_mov_b32 v14, 0x3f803f80\n"
  "v_mov_b32 v15, 0x3f803f80\n"
  "v_mfma_f32_32x32x16_bf16 v[160:175], v[96:99], v[28:31], v[160:175]\n"
  "v_mfma_f32_32x32x16_bf16 v[176:191], v[100:103], v[96:99], v[176:191]\n"
  "s_waitcnt lgkmcnt(0)\n"
  "s_barrier\n"
  "v_mfma_f32_32x32x16_bf16 v[160:175], v[104:107], v[32:35], v[160:175]\n"
  "ds_read_b128 v[136:139], v245 offset:36864\n"
  "ds_read_b64_tr_b16 v[140:141], v250 offset:45056\n"
  "ds_read_b64_tr_b16 v[142:143], v250 offset:45312\n"
  "v_mfma_f32_32x32x16_bf16 v[176:191], v[108:111], v[104:107], v[176:191]\n"
  "ds_read_b128 v[144:147], v246 offset:36864\n"
  "ds_read_b64_tr_b16 v[148:149], v250 offset:46080\n"
  "ds_read_b64_tr_b16 v[150:151], v250 offset:46336\n"
  "v_mfma_f32_32x32x16_bf16 v[160:175], v[112:115], v[36:39], v[160:175]\n"
  "ds_read_b128 v[152:155], v247 offset:36864\n"
  "ds_read_b64_tr_b16 v[156:157], v250 offset:47104\n"
  "ds_read_b64_tr_b16 v[158:159], v250 offset:47360\n"
  "v_mfma_f32_32x32x16_bf16 v[176:191], v[116:119], v[112:115], v[176:191]\n"
  "ds_read_b128 v[192:195], v248 offset:36864\n"
  "ds_read_b64_tr_b16 v[196:197], v250 offset:48128\n"
  "ds_read_b64_tr_b16 v[198:199], v250 offset:48384\n"
  "v_mfma_f32_32x32x16_bf16 v[160:175], v[120:123], v[40:43], v[160:175]\n"
  "v_mov_b32 v24, 0x3f803f80\n"
  "v_mov_b32 v25, 0x3f803f80\n"
  "v_mov_b32 v26, 0x3f803f80\n"
  "v_mov_b32 v27, 0x3f803f80\n"
  "v_mfma_f32_32x32x16_bf16 v[176:191], v[124:127], v[120:123], v[176:191]\n"
  "v_mov_b32 v28, 0x3f803f80\n"
  "v_mov_b32 v29, 0x3f803f80\n"
  "v_mov_b32 v30, 0x3f803f80\n"
  "v_mov_b32 v31, 0x3f803f80\n"
  "v_mfma_f32_32x32x16_bf16 v[160:175], v[128:131], v[44:47], v[160:175]\n"
  "v_mfma_f32_32x32x16_bf16 v[176:191], v[132:135], v[128:131], v[176:191]\n"
  "s_waitcnt lgkmcnt(0)\n"
  "s_barrier\n"
  "v_mfma_f32_32x32x16_bf16 v[160:175], v[136:139], v[48:51], v[160:175]\n"
  "ds_read_b128 v[200:203], v237 offset:32768\n"
  "ds_read_b64_tr_b16 v[204:205], v242 offset:40960\n"
  "ds_read_b64_tr_b16 v[206:207], v242 offset:41216\n"
  "v_mfma_f32_32x32x16_bf16 v[176:191], v[140:143], v[136:139], v[176:191]\n"
  "ds_read_b128 v[208:211], v238 offset:32768\n"
  "ds_read_b64_tr_b16 v[212:213], v242 offset:41984\n"
  "ds_read_b64_tr_b16 v[214:215], v242 offset:42240\n"
  "v_mfma_f32_32x32x16_bf16 v[160:175], v[144:147], v[52:55], v[160:175]\n"
  "ds_read_b128 v[224:227], v239 offset:32768\n"
  "ds_read_b64_tr_b16 v[228:229], v242 offset:43008\n"
  "ds_read_b64_tr_b16 v[230:231], v242 offset:43264\n"
  "v_mfma_f32_32x32x16_bf16 v[176:191], v[148:151], v[144:147], v[176:191]\n"
  "ds_read_b128 v[216:219], v240 offset:32768\n"
  "ds_read_b64_tr_b16 v[220:221], v242 offset:44032\n"
  "ds_read_b64_tr_b16 v[222:223], v242 offset:44288\n"
  "v_mfma_f32_32x32x16_bf16 v[160:175], v[152:155], v[56:59], v[160:175]\n"
  "v_mbcnt_lo_u32_b32 v236, -1, 0\n"
  "v_mbcnt_hi_u32_b32 v236, -1, v236\n"
  "v_and_b32 v235, 31, v236\n"
  "v_lshrrev_b32 v236, 5, v236\n"
  "v_lshl_add_u32 v233, v235, 4, s58\n"
  "v_mad_i32_i24 v233, v236, s59, v233\n"
  "v_lshl_add_u32 v234, s59, 1, v233\n"
  "v_mad_i32_i24 v234, v236, s60, v234\n"
  "v_mfma_f32_32x32x16_bf16 v[176:191], v[156:159], v[152:155], v[176:191]\n"
  "v_lshl_add_u32 v235, v235, 4, s61\n"
  "v_mul_u32_u24 v236, 0x1c00, v236\n"
  "v_add_u32 v235, v235, v236\n"
  "v_add_u32 v236, 0x3800, v235\n"
  "s_mul_i32 s52, s54, 917504\n"
  "s_add_u32 s52, s52, 1048576\n"
  "s_add_u32 s6, s44, s52\n"
  "s_addc_u32 s7, s45, 0\n"
  "v_mfma_f32_32x32x16_bf16 v[160:175], v[192:195], v[60:63], v[160:175]\n"
  "s_mov_b32 s70, 0x0fffffff\n"
  "s_mov_b32 s71, 0x0fffffff\n"
  "v_mov_b32 v40, 0x3f803f80\n"
  "v_mov_b32 v41, 0x3f803f80\n"
  "v_mov_b32 v42, 0x3f803f80\n"
  "v_mov_b32 v43, 0x3f803f80\n"
  "v_mfma_f32_32x32x16_bf16 v[176:191], v[196:199], v[192:195], v[176:191]\n"
  "v_mov_b32 v44, 0x3f803f80\n"
  "v_mov_b32 v45, 0x3f803f80\n"
  "v_mov_b32 v46, 0x3f803f80\n"
  "v_mov_b32 v47, 0x3f803f80\n"
  "s_waitcnt lgkmcnt(0)\n"
  "s_barrier\n"
  "v_mfma_f32_32x32x16_bf16 v[160:175], v[200:203], v[64:67], v[160:175]\n"
  "ds_read_b128 v[80:83], v237 offset:45056\n"
  "ds_read_b64_tr_b16 v[84:85], v242 offset:53248\n"
  "ds_read_b64_tr_b16 v[86:87], v242 offset:53504\n"
  "ds_read_b64_tr_b16 v[88:89], v241 offset:0\n"
  "ds_read_b64_tr_b16 v[90:91], v241 offset:512\n"
  "v_mfma_f32_32x32x16_bf16 v[176:191], v[204:207], v[200:203], v[176:191]\n"
  "ds_read_b128 v[92:95], v238 offset:45056\n"
  "ds_read_b64_tr_b16 v[96:97], v242 offset:54272\n"
  "ds_read_b64_tr_b16 v[98:99], v242 offset:54528\n"
  "ds_read_b64_tr_b16 v[100:101], v241 offset:2048\n"
  "ds_read_b64_tr_b16 v[102:103], v241 offset:2560\n"
  "v_mfma_f32_32x32x16_bf16 v[160:175], v[208:211], v[68:71], v[160:175]\n"
  "ds_read_b128 v[104:107], v239 offset:45056\n"
  "ds_read_b64_tr_b16 v[108:109], v242 offset:55296\n"
  "ds_read_b64_tr_b16 v[110:111], v242 offset:55552\n"
  "ds_read_b64_tr_b16 v[112:113], v241 offset:4096\n"
  "ds_read_b64_tr_b16 v[114:115], v241 offset:4608\n"
  "v_mfma_f32_32x32x16_bf16 v[176:191], v[212:215], v[208:211], v[176:191]\n"
  "ds_read_b128 v[116:119], v240 offset:45056\n"
  "ds_read_b64_tr_b16 v[120:121], v242 offset:56320\n"
  "ds_read_b64_tr_b16 v[122:123], v242 offset:56576\n"
  "ds_read_b64_tr_b16 v[124:125], v241 offset:6144\n"
  "ds_read_b64_tr_b16 v[126:127], v241 offset:6656\n"
  "v_mfma_f32_32x32x16_bf16 v[160:175], v[224:227], v[72:75], v[160:175]\n"
  "buffer_load_dwordx2 v[254:255], v232, s[44:47], 0 offen sc1\n"
  "v_mov_b32 v56, 0x3f803f80\n"
  "v_mov_b32 v57, 0x3f803f80\n"
  "v_mov_b32 v58, 0x3f803f80\n"
  "v_mov_b32 v59, 0x3f803f80\n"
  "v_mfma_f32_32x32x16_bf16 v[176:191], v[228:231], v[224:227], v[176:191]\n"
  "v_mov_b32 v60, 0x3f803f80\n"
  "v_mov_b32 v61, 0x3f803f80\n"
  "v_mov_b32 v62, 0x3f803f80\n"
  "v_mov_b32 v63, 0x3f803f80\n"
  "s_add_u32 s22, s54, 1\n"
  "s_and_b32 s22, s22, 3\n"
  "s_mul_i32 s55, s22, 12288\n"
  "s_add_u32 s23, s54, 2\n"
  "s_and_b32 s23, s23, 3\n"
  "s_mul_i32 s60, s23, 12288\n"
  "v_mfma_f32_32x32x16_bf16 v[160:175], v[216:219], v[76:79], v[160:175]\n"
  "s_add_u32 s60, s60, 4096\n"
  "s_add_u32 s21, s54, 3\n"
  "s_and_b32 s21, s21, 3\n"
  "s_mul_i32 s61, s21, 12288\n"
  "s_add_u32 s61, s61, 8192\n"
  "s_mul_i32 s63, s54, 12288\n"
  "s_mul_i32 s28, s54, 7168\n"
  "s_add_u32 s64, s64, s28\n"
  "s_lshl_b32 s28, s24, 6\n"
  "s_add_u32 s28, s28, s20\n"
  "s_lshl_b32 s28, s28, 7\n"
  "s_add_u32 s62, s28, 69632\n"
  "v_mfma_f32_32x32x16_bf16 v[176:191], v[220:223], v[216:219], v[176:191]\n"
  "s_mov_b32 s67, 0x8000\n"
  "s_mov_b32 s29, 0x3e8b97f6\n"
  "s_mov_b32 s4, 0\n"
  "s_mov_b32 s5, -1\n"
  "s_mov_b32 s28, 0x8000\n"
  "v_mov_b32 v48, 0\n"
  "v_mov_b32 v49, 0\n"
  "v_mov_b32 v50, 0\n"
  "v_mov_b32 v51, 0\n"
  "s_waitcnt lgkmcnt(0)\n"
  "s_barrier\n"
  "v_mfma_f32_32x32x16_bf16 v[160:175], v[80:83], v[88:91], v[160:175]\n"
  "ds_read_b128 v[128:131], v237 offset:57344\n"
  "ds_read_b64_tr_b16 v[132:133], v250 offset:32768\n"
  "ds_read_b64_tr_b16 v[134:135], v250 offset:33024\n"
  "ds_read_b64_tr_b16 v[136:137], v241 offset:16384\n"
  "ds_read_b64_tr_b16 v[138:139], v241 offset:16896\n"
  "v_mfma_f32_32x32x16_bf16 v[176:191], v[84:87], v[80:83], v[176:191]\n"
  "ds_read_b128 v[140:143], v238 offset:57344\n"
  "ds_read_b64_tr_b16 v[144:145], v250 offset:33792\n"
  "ds_read_b64_tr_b16 v[146:147], v250 offset:34048\n"
  "ds_read_b64_tr_b16 v[148:149], v241 offset:18432\n"
  "ds_read_b64_tr_b16 v[150:151], v241 offset:18944\n"
  "v_mfma_f32_32x32x16_bf16 v[160:175], v[92:95], v[100:103], v[160:175]\n"
  "ds_read_b128 v[152:155], v239 offset:57344\n"
  "ds_read_b64_tr_b16 v[156:157], v250 offset:34816\n"
  "ds_read_b64_tr_b16 v[158:159], v250 offset:35072\n"
  "ds_read_b64_tr_b16 v[192:193], v241 offset:20480\n"
  "ds_read_b64_tr_b16 v[194:195], v241 offset:20992\n"
  "v_mfma_f32_32x32x16_bf16 v[176:191], v[96:99], v[92:95], v[176:191]\n"
  "ds_read_b128 v[196:199], v240 offset:57344\n"
  "ds_read_b64_tr_b16 v[64:65], v250 offset:35840\n"
  "ds_read_b64_tr_b16 v[66:67], v250 offset:36096\n"
  "ds_read_b64_tr_b16 v[200:201], v241 offset:22528\n"
  "ds_read_b64_tr_b16 v[202:203], v241 offset:23040\n"
  "v_mfma_f32_32x32x16_bf16 v[160:175], v[104:107], v[112:115], v[160:175]\n"
  "v_and_b32 v238, 0x3f0, v251\n"
  "v_lshl_add_u32 v249, s22, 11, v238\n"
  "v_lshl_add_u32 v250, s23, 11, v238\n"
  "v_lshl_add_u32 v241, s21, 11, v238\n"
  "v_add_u32 v245, s55, v238\n"
  "v_mfma_f32_32x32x16_bf16 v[176:191], v[108:111], v[104:107], v[176:191]\n"
  "v_add_u32 v246, s60, v238\n"
  "v_add_u32 v247, s61, v238\n"
  "v_add_u32 v248, s63, v238\n"
  "v_mov_b32 v237, 0xdddddddd\n"
  "v_lshrrev_b32 v240, 7, v238\n"
  "v_mfma_f32_32x32x16_bf16 v[160:175], v[116:119], v[124:127], v[160:175]\n"
  "v_lshrrev_b32 v242, 2, v238\n"
  "v_and_b32 v242, 64, v242\n"
  "v_and_b32 v239, 0x70, v238\n"
  "v_mul_u32_u24 v240, 0x380, v240\n"
  "v_xad_u32 v239, v239, v242, v240\n"
  "v_mfma_f32_32x32x16_bf16 v[176:191], v[120:123], v[116:119], v[176:191]\n"
  "v_add_u32 v239, s64, v239\n"
  "v_bfe_u32 v240, v238, 4, 3\n"
  "v_and_b32 v242, 4, v240\n"
  "v_add_lshl_u32 v240, v240, v242, 11\n"
  "s_waitcnt vmcnt(0)\n"
  "v_xor_b32 v254, v254, v252\n"
  "v_xor_b32 v255, v255, v253\n"
  "v_or_b32 v254, v254, v255\n"
  "v_cmp_ne_u32 vcc, 0, v254\n"
  "s_cbranch_vccz LDa110%=\n"
  "s_mov_b32 s69, 0x40000\n"
  "LPa110%=:\n"
  "s_sleep 1\n"
  "buffer_load_dwordx2 v[254:255], v232, s[44:47], 0 offen sc1\n"
  "s_waitcnt vmcnt(0)\n"
  "v_xor_b32 v254, v254, v252\n"
  "v_xor_b32 v255, v255, v253\n"
  "v_or_b32 v254, v254, v255\n"
  "v_cmp_ne_u32 vcc, 0, v254\n"
  "s_cbranch_vccz LDa110%=\n"
  "s_sub_u32 s69, s69, 1\n"
  "s_cmp_lg_u32 s69, 0\n"
  "s_cbranch_scc1 LPa110%=\n"
  "LDa110%=:\n"
  "s_add_u32 s30, s6, 0\n"
  "s_addc_u32 s31, s7, 0\n"
  "global_load_dwordx4 v[0:3], v233, s[30:31] sc1\n"
  "global_load_dwordx4 v[4:7], v234, s[30:31] sc1\n"
  "s_waitcnt lgkmcnt(14)\n"
  "v_mfma_f32_32x32x16_bf16 v[176:191], v[132:135], v[128:131], v[176:191]\n"
  "s_add_u32 s30, s6, 229376\n"
  "s_addc_u32 s31, s7, 0\n"
  "global_load_dwordx4 v[16:19], v233, s[30:31] sc1\n"
  "global_load_dwordx4 v[20:23], v234, s[30:31] sc1\n"
  "s_waitcnt lgkmcnt(10)\n"
  "v_mfma_f32_32x32x16_bf16 v[176:191], v[144:147], v[140:143], v[176:191]\n"
  "s_add_u32 s30, s6, 0\n"
  "s_addc_u32 s31, s7, 0\n"
  "s_mov_b64 exec, s[70:71]\n"
  "global_load_dwordx4 v[8:11], v235, s[30:31] sc1\n"
  "global_load_dwordx4 v[12:15], v236, s[30:31] sc1\n"
  "s_mov_b64 exec, -1\n"
  "s_waitcnt lgkmcnt(5)\n"
  "v_mfma_f32_32x32x16_bf16 v[176:191], v[156:159], v[152:155], v[176:191]\n"
  "s_add_u32 s30, s6, 229376\n"
  "s_addc_u32 s31, s7, 0\n"
  "s_mov_b64 exec, s[70:71]\n"
  "global_load_dwordx4 v[24:27], v235, s[30:31] sc1\n"
  "global_load_dwordx4 v[28:31], v236, s[30:31] sc1\n"
  "s_mov_b64 exec, -1\n"
  "s_waitcnt lgkmcnt(0)\n"
  "v_mfma_f32_32x32x16_bf16 v[176:191], v[64:67], v[196:199], v[176:191]\n"
  "v_mfma_f32_32x32x16_bf16 v[160:175], v[128:131], v[136:139], v[160:175]\n"
  "v_mfma_f32_32x32x16_bf16 v[160:175], v[140:143], v[148:151], v[160:175]\n"
  "s_nop 9\n"
  "v_mul_f32 v176, s29, v176\n"
  "v_mul_f32 v177, s29, v177\n"
  "v_mul_f32 v178, s29, v178\n"
  "v_mul_f32 v179, s29, v179\n"
  "v_mul_f32 v180, s29, v180\n"
  "v_mul_f32 v181, s29, v181\n"
  "v_mfma_f32_32x32x16_bf16 v[160:175], v[152:155], v[192:195], v[160:175]\n"
  "v_mul_f32 v182, s29, v182\n"
  "v_mul_f32 v183, s29, v183\n"
  "v_mul_f32 v184, s29, v184\n"
  "v_mul_f32 v185, s29, v185\n"
  "v_mul_f32 v186, s29, v186\n"
  "v_mul_f32 v187, s29, v187\n"
  "v_mfma_f32_32x32x16_bf16 v[160:175], v[196:199], v[200:203], v[160:175]\n"
  "v_mul_f32 v188, s29, v188\n"
  "v_mul_f32 v189, s29, v189\n"
  "v_mul_f32 v190, s29, v190\n"
  "v_mul_f32 v191, s29, v191\n"
  "v_cvt_pk_bf16_f32 v176, v176, v177\n"
  "v_cvt_pk_bf16_f32 v177, v178, v179\n"
  "v_mfma_f32_32x32x16_bf16 v[96:111], v[48:51], v[48:51], 0\n"
  "v_cvt_pk_bf16_f32 v178, v180, v181\n"
  "v_cvt_pk_bf16_f32 v179, v182, v183\n"
  "v_cvt_pk_bf16_f32 v180, v184, v185\n"
  "v_cvt_pk_bf16_f32 v181, v186, v187\n"
  "v_cvt_pk_bf16_f32 v182, v188, v189\n"
  "v_cvt_pk_bf16_f32 v183, v190, v191\n"
  "v_mfma_f32_32x32x16_bf16 v[112:127], v[48:51], v[48:51], 0\n"
  "ds_write_b128 v251, v[176:179]\n"
  "v_cndmask_b32_e64 v182, v182, 0, s[4:5]\n"
  "v_cndmask_b32_e64 v183, v183, 0, s[4:5]\n"
  "ds_write_b128 v251, v[180:183] offset:1024\n"
  "v_mfma_f32_32x32x16_bf16 v[128:143], v[48:51], v[48:51], 0\n"
  "v_mfma_f32_32x32x16_bf16 v[144:159], v[48:51], v[48:51], 0\n"
  "s_branch LE%=\n"
  "LT11%=:\n"
  "v_subrev_u32 v233, 43008, v236\n"
  "s_waitcnt vmcnt(6) lgkmcnt(0)\n"
  "s_barrier\n"
  "ds_read_b128 v[192:195], v237 offset:32768\n"
  "ds_read_b64_tr_b16 v[196:197], v242 offset:40960\n"
  "ds_read_b64_tr_b16 v[198:199], v242 offset:41216\n"
  "ds_read_b128 v[200:203], v238 offset:32768\n"
  "ds_read_b64_tr_b16 v[204:205], v242 offset:41984\n"
  "ds_read_b64_tr_b16 v[206:207], v242 offset:42240\n"
  "ds_read_b128 v[208:211], v239 offset:32768\n"
  "ds_read_b64_tr_b16 v[212:213], v242 offset:43008\n"
  "ds_read_b64_tr_b16 v[214:215], v242 offset:43264\n"
  "ds_read_b128 v[224:227], v240 offset:32768\n"
  "ds_read_b64_tr_b16 v[228:229], v242 offset:44032\n"
  "ds_read_b64_tr_b16 v[230:231], v242 offset:44288\n"
  "s_add_u32 m0, s53, 67584\n"
  "s_add_u32 s66, s52, 43008\n"
  "buffer_load_dwordx4 v234, s[36:39], s66 offen lds\n"
  "s_add_u32 m0, s53, 68608\n"
  "s_nop 0\n"
  "buffer_load_dwordx4 v235, s[36:39], s66 offen lds\n"
  "s_add_u32 m0, s53, 69632\n"
  "s_add_u32 s66, s52, 57344\n"
  "buffer_load_dwordx4 v234, s[36:39], s66 offen lds\n"
  "s_add_u32 m0, s53, 70656\n"
  "s_nop 0\n"
  "buffer_load_dwordx4 v235, s[36:39], s66 offen lds\n"
  "s_add_u32 m0, s68, 76800\n"
  "s_mul_i32 s65, s52, 1344\n"
  "buffer_load_dwordx4 v233, s[40:43], s65 offen lds\n"
  "s_add_u32 m0, s68, 77824\n"
  "s_mul_i32 s65, s52, 1344\n"
  "buffer_load_dwordx4 v236, s[40:43], s65 offen lds\n"
  "s_add_u32 s52, s52, 128\n"
  "s_cmp_eq_u32 s52, 896\n"
  "s_cselect_b32 s52, 0, s52\n"
  "v_mbcnt_lo_u32_b32 v254, -1, 0\n"
  "v_mbcnt_hi_u32_b32 v254, -1, v254\n"
  "v_bfe_u32 v255, v254, 4, 1\n"
  "v_mul_u32_u24 v232, s63, v255\n"
  "v_lshrrev_b32 v255, 5, v254\n"
  "v_lshl_add_u32 v232, v255, 14, v232\n"
  "v_bfe_u32 v255, v254, 2, 2\n"
  "v_lshl_add_u32 v232, v255, 7, v232\n"
  "v_add_u32 v232, s62, v232\n"
  "s_waitcnt vmcnt(9) lgkmcnt(0)\n"
  "s_barrier\n"
  "v_mfma_f32_32x32x16_bf16 v[160:175], v[192:195], v[0:3], 0\n"
  "v_mfma_f32_32x32x16_bf16 v[176:191], v[196:199], v[80:83], 0\n"
  "v_mfma_f32_32x32x16_bf16 v[160:175], v[200:203], v[4:7], v[160:175]\n"
  "ds_read_b128 v[192:195], v237 offset:45056\n"
  "ds_read_b64_tr_b16 v[196:197], v242 offset:53248\n"
  "ds_read_b64_tr_b16 v[198:199], v242 offset:53504\n"
  "v_mfma_f32_32x32x16_bf16 v[176:191], v[204:207], v[84:87], v[176:191]\n"
  "v_mfma_f32_32x32x16_bf16 v[160:175], v[208:211], v[8:11], v[160:175]\n"
  "ds_read_b128 v[80:83], v238 offset:45056\n"
  "ds_read_b64_tr_b16 v[200:201], v242 offset:54272\n"
  "ds_read_b64_tr_b16 v[202:203], v242 offset:54528\n"
  "v_mfma_f32_32x32x16_bf16 v[176:191], v[212:215], v[88:91], v[176:191]\n"
  "ds_read_b128 v[204:207], v239 offset:45056\n"
  "ds_read_b64_tr_b16 v[84:85], v242 offset:55296\n"
  "ds_read_b64_tr_b16 v[86:87], v242 offset:55552\n"
  "v_mfma_f32_32x32x16_bf16 v[160:175], v[224:227], v[12:15], v[160:175]\n"
  "ds_read_b128 v[208:211], v240 offset:45056\n"
  "ds_read_b64_tr_b16 v[212:213], v242 offset:56320\n"
  "ds_read_b64_tr_b16 v[214:215], v242 offset:56576\n"
  "v_mfma_f32_32x32x16_bf16 v[176:191], v[228:231], v[92:95], v[176:191]\n"
  "s_add_u32 m0, s53, 30720\n"
  "s_add_u32 s66, s52, 43008\n"
  "buffer_load_dwordx4 v234, s[36:39], s66 offen lds\n"
  "s_add_u32 m0, s53, 31744\n"
  "s_nop 0\n"
  "buffer_load_dwordx4 v235, s[36:39], s66 offen lds\n"
  "s_add_u32 m0, s53, 32768\n"
  "s_add_u32 s66, s52, 57344\n"
  "buffer_load_dwordx4 v234, s[36:39], s66 offen lds\n"
  "s_add_u32 m0, s53, 33792\n"
  "s_nop 0\n"
  "buffer_load_dwordx4 v235, s[36:39], s66 offen lds\n"
  "s_add_u32 m0, s68, 39936\n"
  "s_mul_i32 s65, s52, 1344\n"
  "buffer_load_dwordx4 v233, s[40:43], s65 offen lds\n"
  "s_add_u32 m0, s68, 40960\n"
  "s_mul_i32 s65, s52, 1344\n"
  "buffer_load_dwordx4 v236, s[40:43], s65 offen lds\n"
  "s_add_u32 s52, s52, 128\n"
  "s_cmp_eq_u32 s52, 896\n"
  "s_cselect_b32 s52, 0, s52\n"
  "buffer_store_dwordx4 v[216:219], v243, s[44:47], s56 offen sc1\n"
  "buffer_store_dwordx4 v[220:223], v243, s[44:47], s67 offen sc1\n"
  "s_waitcnt vmcnt(14) lgkmcnt(0)\n"
  "s_barrier\n"
  "v_mfma_f32_32x32x16_bf16 v[160:175], v[192:195], v[16:19], v[160:175]\n"
  "ds_read_b128 v[88:91], v237 offset:57344\n"
  "ds_read_b64_tr_b16 v[224:225], v250 offset:32768\n"
  "ds_read_b64_tr_b16 v[226:227], v250 offset:33024\n"
  "v_mfma_f32_32x32x16_bf16 v[176:191], v[196:199], v[96:99], v[176:191]\n"
  "ds_read_b128 v[228:231], v238 offset:57344\n"
  "ds_read_b64_tr_b16 v[92:93], v250 offset:33792\n"
  "ds_read_b64_tr_b16 v[94:95], v250 offset:34048\n"
  "v_mfma_f32_32x32x16_bf16 v[160:175], v[80:83], v[20:23], v[160:175]\n"
  "ds_read_b128 v[192:195], v239 offset:57344\n"
  "ds_read_b64_tr_b16 v[196:197], v250 offset:34816\n"
  "ds_read_b64_tr_b16 v[198:199], v250 offset:35072\n"
  "v_mfma_f32_32x32x16_bf16 v[176:191], v[200:203], v[100:103], v[176:191]\n"
  "v_mfma_f32_32x32x16_bf16 v[160:175], v[204:207], v[24:27], v[160:175]\n"
  "ds_read_b128 v[96:99], v240 offset:57344\n"
  "ds_read_b64_tr_b16 v[80:81], v250 offset:35840\n"
  "ds_read_b64_tr_b16 v[82:83], v250 offset:36096\n"
  "v_mfma_f32_32x32x16_bf16 v[176:191], v[84:87], v[104:107], v[176:191]\n"
  "s_add_u32 m0, s53, 43008\n"
  "s_add_u32 s66, s52, 43008\n"
  "buffer_load_dwordx4 v234, s[36:39], s66 offen lds\n"
  "s_add_u32 m0, s53, 44032\n"
  "s_nop 0\n"
  "buffer_load_dwordx4 v235, s[36:39], s66 offen lds\n"
  "s_add_u32 m0, s53, 45056\n"
  "s_add_u32 s66, s52, 57344\n"
  "buffer_load_dwordx4 v234, s[36:39], s66 offen lds\n"
  "v_mfma_f32_32x32x16_bf16 v[160:175], v[208:211], v[28:31], v[160:175]\n"
  "s_add_u32 m0, s53, 46080\n"
  "s_nop 0\n"
  "buffer_load_dwordx4 v235, s[36:39], s66 offen lds\n"
  "s_add_u32 m0, s68, 52224\n"
  "s_mul_i32 s65, s52, 1344\n"
  "buffer_load_dwordx4 v233, s[40:43], s65 offen lds\n"
  "s_add_u32 m0, s68, 53248\n"
  "s_mul_i32 s65, s52, 1344\n"
  "buffer_load_dwordx4 v236, s[40:43], s65 offen lds\n"
  "s_add_u32 s52, s52, 128\n"
  "s_cmp_eq_u32 s52, 896\n"
  "s_cselect_b32 s52, 0, s52\n"
  "v_mfma_f32_32x32x16_bf16 v[176:191], v[212:215], v[108:111], v[176:191]\n"
  "v_mov_b32 v8, 0x3f803f80\n"
  "v_mov_b32 v9, 0x3f803f80\n"
  "v_mov_b32 v10, 0x3f803f80\n"
  "v_mov_b32 v11, 0x3f803f80\n"
  "v_mov_b32 v12, 0x3f803f80\n"
  "v_mov_b32 v13, 0x3f803f80\n"
  "v_mov_b32 v14, 0x3f803f80\n"
  "v_mov_b32 v15, 0x3f803f80\n"
  "s_waitcnt vmcnt(14) lgkmcnt(0)\n"
  "s_barrier\n"
  "v_mfma_f32_32x32x16_bf16 v[160:175], v[88:91], v[32:35], v[160:175]\n"
  "ds_read_b128 v[200:203], v245 offset:36864\n"
  "ds_read_b64_tr_b16 v[100:101], v250 offset:45056\n"
  "ds_read_b64_tr_b16 v[102:103], v250 offset:45312\n"
  "v_mfma_f32_32x32x16_bf16 v[176:191], v[224:227], v[112:115], v[176:191]\n"
  "ds_read_b128 v[204:207], v246 offset:36864\n"
  "ds_read_b64_tr_b16 v[84:85], v250 offset:46080\n"
  "ds_read_b64_tr_b16 v[86:87], v250 offset:46336\n"
  "v_mfma_f32_32x32x16_bf16 v[160:175], v[228:231], v[36:39], v[160:175]\n"
  "ds_read_b128 v[104:107], v247 offset:36864\n"
  "ds_read_b64_tr_b16 v[208:209], v250 offset:47104\n"
  "ds_read_b64_tr_b16 v[210:211], v250 offset:47360\n"
  "v_mfma_f32_32x32x16_bf16 v[176:191], v[92:95], v[116:119], v[176:191]\n"
  "ds_read_b128 v[212:215], v248 offset:36864\n"
  "ds_read_b64_tr_b16 v[108:109], v250 offset:48128\n"
  "ds_read_b64_tr_b16 v[110:111], v250 offset:48384\n"
  "v_mfma_f32_32x32x16_bf16 v[160:175], v[192:195], v[40:43], v[160:175]\n"
  "s_add_u32 m0, s53, 55296\n"
  "s_add_u32 s66, s52, 43008\n"
  "buffer_load_dwordx4 v234, s[36:39], s66 offen lds\n"
  "s_add_u32 m0, s53, 56320\n"
  "s_nop 0\n"
  "buffer_load_dwordx4 v235, s[36:39], s66 offen lds\n"
  "v_mfma_f32_32x32x16_bf16 v[176:191], v[196:199], v[120:123], v[176:191]\n"
  "s_add_u32 m0, s53, 57344\n"
  "s_add_u32 s66, s52, 57344\n"
  "buffer_load_dwordx4 v234, s[36:39], s66 offen lds\n"
  "s_add_u32 m0, s53, 58368\n"
  "s_nop 0\n"
  "buffer_load_dwordx4 v235, s[36:39], s66 offen lds\n"
  "v_mfma_f32_32x32x16_bf16 v[160:175], v[96:99], v[44:47], v[160:175]\n"
  "s_add_u32 m0, s68, 64512\n"
  "s_mul_i32 s65, s52, 1344\n"
  "buffer_load_dwordx4 v233, s[40:43], s65 offen lds\n"
  "s_add_u32 m0, s68, 65536\n"
  "s_mul_i32 s65, s52, 1344\n"
  "buffer_load_dwordx4 v236, s[40:43], s65 offen lds\n"
  "s_add_u32 s52, s52, 128\n"
  "s_cmp_eq_u32 s52, 896\n"
  "s_cselect_b32 s52, 0, s52\n"
  "v_mfma_f32_32x32x16_bf16 v[176:191], v[80:83], v[124:127], v[176:191]\n"
  "v_mov_b32 v24, 0x3f803f80\n"
  "v_mov_b32 v25, 0x3f803f80\n"
  "v_mov_b32 v26, 0x3f803f80\n"
  "v_mov_b32 v27, 0x3f803f80\n"
  "v_mov_b32 v28, 0x3f803f80\n"
  "v_mov_b32 v29, 0x3f803f80\n"
  "v_mov_b32 v30, 0x3f803f80\n"
  "v_mov_b32 v31, 0x3f803f80\n"
  "s_waitcnt vmcnt(14) lgkmcnt(0)\n"
  "s_barrier\n"
  "v_mfma_f32_32x32x16_bf16 v[160:175], v[200:203], v[48:51], v[160:175]\n"
  "ds_read_b128 v[88:91], v237 offset:32768\n"
  "ds_read_b64_tr_b16 v[224:225], v242 offset:40960\n"
  "ds_read_b64_tr_b16 v[226:227], v242 offset:41216\n"
  "v_mfma_f32_32x32x16_bf16 v[176:191], v[100:103], v[128:131], v[176:191]\n"
  "ds_read_b128 v[112:115], v238 offset:32768\n"
  "ds_read_b64_tr_b16 v[228:229], v242 offset:41984\n"
  "ds_read_b64_tr_b16 v[230:231], v242 offset:42240\n"
  "v_mfma_f32_32x32x16_bf16 v[160:175], v[204:207], v[52:55], v[160:175]\n"
  "ds_read_b128 v[92:95], v239 offset:32768\n"
  "ds_read_b64_tr_b16 v[116:117], v242 offset:43008\n"
  "ds_read_b64_tr_b16 v[118:119], v242 offset:43264\n"
  "v_mfma_f32_32x32x16_bf16 v[176:191], v[84:87], v[132:135], v[176:191]\n"
  "ds_read_b128 v[192:195], v240 offset:32768\n"
  "ds_read_b64_tr_b16 v[196:197], v242 offset:44032\n"
  "ds_read_b64_tr_b16 v[198:199], v242 offset:44288\n"
  "v_mfma_f32_32x32x16_bf16 v[160:175], v[104:107], v[56:59], v[160:175]\n"
  "v_mbcnt_lo_u32_b32 v236, -1, 0\n"
  "v_mbcnt_hi_u32_b32 v236, -1, v236\n"
  "v_and_b32 v235, 31, v236\n"
  "v_lshrrev_b32 v236, 5, v236\n"
  "v_lshl_add_u32 v233, v235, 4, s58\n"
  "v_mad_i32_i24 v233, v236, s59, v233\n"
  "v_lshl_add_u32 v234, s59, 1, v233\n"
  "v_mad_i32_i24 v234, v236, s60, v234\n"
  "v_mfma_f32_32x32x16_bf16 v[176:191], v[208:211], v[136:139], v[176:191]\n"
  "v_lshl_add_u32 v235, v235, 4, s61\n"
  "v_mul_u32_u24 v236, 0x1c00, v236\n"
  "v_add_u32 v235, v235, v236\n"
  "v_add_u32 v236, 0x3800, v235\n"
  "s_mul_i32 s52, s54, 917504\n"
  "s_add_u32 s52, s52, 1048576\n"
  "s_add_u32 s6, s44, s52\n"
  "s_addc_u32 s7, s45, 0\n"
  "v_mfma_f32_32x32x16_bf16 v[160:175], v[212:215], v[60:63], v[160:175]\n"
  "s_mov_b32 s70, 0x0fffffff\n"
  "s_mov_b32 s71, 0x0fffffff\n"
  "v_mov_b32 v40, 0x3f803f80\n"
  "v_mov_b32 v41, 0x3f803f80\n"
  "v_mov_b32 v42, 0x3f803f80\n"
  "v_mov_b32 v43, 0x3f803f80\n"
  "v_mfma_f32_32x32x16_bf16 v[176:191], v[108:111], v[140:143], v[176:191]\n"
  "v_mov_b32 v44, 0x3f803f80\n"
  "v_mov_b32 v45, 0x3f803f80\n"
  "v_mov_b32 v46, 0x3f803f80\n"
  "v_mov_b32 v47, 0x3f803f80\n"
  "s_waitcnt vmcnt(6) lgkmcnt(0)\n"
  "s_barrier\n"
  "v_mfma_f32_32x32x16_bf16 v[160:175], v[88:91], v[64:67], v[160:175]\n"
  "ds_read_b128 v[120:123], v237 offset:45056\n"
  "ds_read_b64_tr_b16 v[96:97], v242 offset:53248\n"
  "ds_read_b64_tr_b16 v[98:99], v242 offset:53504\n"
  "ds_read_b64_tr_b16 v[80:81], v241 offset:0\n"
  "ds_read_b64_tr_b16 v[82:83], v241 offset:512\n"
  "ds_read_b128 v[124:127], v237 offset:8192\n"
  "v_mfma_f32_32x32x16_bf16 v[176:191], v[224:227], v[144:147], v[176:191]\n"
  "ds_read_b128 v[200:203], v238 offset:45056\n"
  "ds_read_b64_tr_b16 v[100:101], v242 offset:54272\n"
  "ds_read_b64_tr_b16 v[102:103], v242 offset:54528\n"
  "ds_read_b64_tr_b16 v[128:129], v241 offset:2048\n"
  "ds_read_b64_tr_b16 v[130:131], v241 offset:2560\n"
  "ds_read_b128 v[204:207], v238 offset:8192\n"
  "v_mfma_f32_32x32x16_bf16 v[160:175], v[112:115], v[68:71], v[160:175]\n"
  "ds_read_b128 v[84:87], v239 offset:45056\n"
  "ds_read_b64_tr_b16 v[132:133], v242 offset:55296\n"
  "ds_read_b64_tr_b16 v[134:135], v242 offset:55552\n"
  "ds_read_b64_tr_b16 v[104:105], v241 offset:4096\n"
  "ds_read_b64_tr_b16 v[106:107], v241 offset:4608\n"
  "ds_read_b128 v[208:211], v239 offset:8192\n"
  "v_mfma_f32_32x32x16_bf16 v[176:191], v[228:231], v[148:151], v[176:191]\n"
  "ds_read_b128 v[136:139], v240 offset:45056\n"
  "ds_read_b64_tr_b16 v[212:213], v242 offset:56320\n"
  "ds_read_b64_tr_b16 v[214:215], v242 offset:56576\n"
  "ds_read_b64_tr_b16 v[108:109], v241 offset:6144\n"
  "ds_read_b64_tr_b16 v[110:111], v241 offset:6656\n"
  "ds_read_b128 v[140:143], v240 offset:8192\n"
  "v_mfma_f32_32x32x16_bf16 v[160:175], v[92:95], v[72:75], v[160:175]\n"
  "buffer_load_dwordx2 v[254:255], v232, s[44:47], 0 offen sc1\n"
  "v_mov_b32 v56, 0x3f803f80\n"
  "v_mov_b32 v57, 0x3f803f80\n"
  "v_mov_b32 v58, 0x3f803f80\n"
  "v_mov_b32 v59, 0x3f803f80\n"
  "v_mfma_f32_32x32x16_bf16 v[176:191], v[116:119], v[152:155], v[176:191]\n"
  "v_mov_b32 v60, 0x3f803f80\n"
  "v_mov_b32 v61, 0x3f803f80\n"
  "v_mov_b32 v62, 0x3f803f80\n"
  "v_mov_b32 v63, 0x3f803f80\n"
  "s_add_u32 s22, s54, 1\n"
  "s_and_b32 s22, s22, 3\n"
  "s_mul_i32 s55, s22, 12288\n"
  "s_add_u32 s23, s54, 2\n"
  "s_and_b32 s23, s23, 3\n"
  "s_mul_i32 s60, s23, 12288\n"
  "v_mfma_f32_32x32x16_bf16 v[160:175], v[192:195], v[76:79], v[160:175]\n"
  "s_add_u32 s60, s60, 4096\n"
  "s_add_u32 s21, s54, 3\n"
  "s_and_b32 s21, s21, 3\n"
  "s_mul_i32 s61, s21, 12288\n"
  "s_add_u32 s61, s61, 8192\n"
  "s_mul_i32 s63, s54, 12288\n"
  "s_mul_i32 s28, s54, 7168\n"
  "s_add_u32 s64, s64, s28\n"
  "s_lshl_b32 s28, s24, 6\n"
  "s_add_u32 s28, s28, s20\n"
  "s_lshl_b32 s28, s28, 7\n"
  "s_add_u32 s62, s28, 69632\n"
  "v_mfma_f32_32x32x16_bf16 v[176:191], v[196:199], v[156:159], v[176:191]\n"
  "s_mov_b32 s67, 0x8000\n"
  "s_mov_b32 s29, 0x3e8b97f6\n"
  "s_mov_b32 s4, 0\n"
  "s_mov_b32 s5, -1\n"
  "s_mov_b32 s28, 0x8000\n"
  "v_mov_b32 v48, 0\n"
  "v_mov_b32 v49, 0\n"
  "v_mov_b32 v50, 0\n"
  "v_mov_b32 v51, 0\n"
  "s_waitcnt vmcnt(1) lgkmcnt(0)\n"
  "s_barrier\n"
  "v_mfma_f32_32x32x16_bf16 v[160:175], v[120:123], v[80:83], v[160:175]\n"
  "ds_read_b128 v[64:67], v237 offset:57344\n"
  "ds_read_b64_tr_b16 v[88:89], v250 offset:32768\n"
  "ds_read_b64_tr_b16 v[90:91], v250 offset:33024\n"
  "ds_read_b64_tr_b16 v[224:225], v241 offset:16384\n"
  "ds_read_b64_tr_b16 v[226:227], v241 offset:16896\n"
  "ds_read_b128 v[144:147], v237 offset:24576\n"
  "v_mfma_f32_32x32x16_bf16 v[176:191], v[96:99], v[124:127], v[176:191]\n"
  "ds_read_b128 v[68:71], v238 offset:57344\n"
  "ds_read_b64_tr_b16 v[112:113], v250 offset:33792\n"
  "ds_read_b64_tr_b16 v[114:115], v250 offset:34048\n"
  "ds_read_b64_tr_b16 v[228:229], v241 offset:18432\n"
  "ds_read_b64_tr_b16 v[230:231], v241 offset:18944\n"
  "ds_read_b128 v[148:151], v238 offset:24576\n"
  "v_mfma_f32_32x32x16_bf16 v[160:175], v[200:203], v[128:131], v[160:175]\n"
  "ds_read_b128 v[72:75], v239 offset:57344\n"
  "ds_read_b64_tr_b16 v[92:93], v250 offset:34816\n"
  "ds_read_b64_tr_b16 v[94:95], v250 offset:35072\n"
  "ds_read_b64_tr_b16 v[116:117], v241 offset:20480\n"
  "ds_read_b64_tr_b16 v[118:119], v241 offset:20992\n"
  "ds_read_b128 v[152:155], v239 offset:24576\n"
  "v_mfma_f32_32x32x16_bf16 v[176:191], v[100:103], v[204:207], v[176:191]\n"
  "ds_read_b128 v[76:79], v240 offset:57344\n"
  "ds_read_b64_tr_b16 v[192:193], v250 offset:35840\n"
  "ds_read_b64_tr_b16 v[194:195], v250 offset:36096\n"
  "ds_read_b64_tr_b16 v[196:197], v241 offset:22528\n"
  "ds_read_b64_tr_b16 v[198:199], v241 offset:23040\n"
  "ds_read_b128 v[156:159], v240 offset:24576\n"
  "v_mfma_f32_32x32x16_bf16 v[160:175], v[84:87], v[104:107], v[160:175]\n"
  "v_and_b32 v238, 0x3f0, v251\n"
  "v_lshl_add_u32 v249, s22, 11, v238\n"
  "v_lshl_add_u32 v250, s23, 11, v238\n"
  "v_lshl_add_u32 v241, s21, 11, v238\n"
  "v_add_u32 v245, s55, v238\n"
  "v_mfma_f32_32x32x16_bf16 v[176:191], v[132:135], v[208:211], v[176:191]\n"
  "v_add_u32 v246, s60, v238\n"
  "v_add_u32 v247, s61, v238\n"
  "v_add_u32 v248, s63, v238\n"
  "v_mov_b32 v237, 0xdddddddd\n"
  "v_lshrrev_b32 v240, 7, v238\n"
  "v_mfma_f32_32x32x16_bf16 v[160:175], v[136:139], v[108:111], v[160:175]\n"
  "v_lshrrev_b32 v242, 2, v238\n"
  "v_and_b32 v242, 64, v242\n"
  "v_and_b32 v239, 0x70, v238\n"
  "v_mul_u32_u24 v240, 0x380, v240\n"
  "v_xad_u32 v239, v239, v242, v240\n"
  "v_mfma_f32_32x32x16_bf16 v[176:191], v[212:215], v[140:143], v[176:191]\n"
  "v_add_u32 v239, s64, v239\n"
  "v_bfe_u32 v240, v238, 4, 3\n"
  "v_and_b32 v242, 4, v240\n"
  "v_add_lshl_u32 v240, v240, v242, 11\n"
  "s_waitcnt vmcnt(0)\n"
  "v_xor_b32 v254, v254, v252\n"
  "v_xor_b32 v255, v255, v253\n"
  "v_or_b32 v254, v254, v255\n"
  "v_cmp_ne_u32 vcc, 0, v254\n"
  "s_cbranch_vccz LDa101%=\n"
  "s_mov_b32 s69, 0x40000\n"
  "LPa101%=:\n"
  "s_sleep 1\n"
  "buffer_load_dwordx2 v[254:255], v232, s[44:47], 0 offen sc1\n"
  "s_waitcnt vmcnt(0)\n"
  "v_xor_b32 v254, v254, v252\n"
  "v_xor_b32 v255, v255, v253\n"
  "v_or_b32 v254, v254, v255\n"
  "v_cmp_ne_u32 vcc, 0, v254\n"
  "s_cbranch_vccz LDa101%=\n"
  "s_sub_u32 s69, s69, 1\n"
  "s_cmp_lg_u32 s69, 0\n"
  "s_cbranch_scc1 LPa101%=\n"
  "LDa101%=:\n"
  "s_add_u32 s30, s6, 0\n"
  "s_addc_u32 s31, s7, 0\n"
  "global_load_dwordx4 v[0:3], v233, s[30:31] sc1\n"
  "global_load_dwordx4 v[4:7], v234, s[30:31] sc1\n"
  "s_waitcnt lgkmcnt(14)\n"
  "v_mfma_f32_32x32x16_bf16 v[176:191], v[88:91], v[144:147], v[176:191]\n"
  "s_add_u32 s30, s6, 229376\n"
  "s_addc_u32 s31, s7, 0\n"
  "global_load_dwordx4 v[16:19], v233, s[30:31] sc1\n"
  "global_load_dwordx4 v[20:23], v234, s[30:31] sc1\n"
  "s_waitcnt lgkmcnt(12)\n"
  "v_mfma_f32_32x32x16_bf16 v[176:191], v[112:115], v[148:151], v[176:191]\n"
  "s_add_u32 s30, s6, 0\n"
  "s_addc_u32 s31, s7, 0\n"
  "s_mov_b64 exec, s[70:71]\n"
  "global_load_dwordx4 v[8:11], v235, s[30:31] sc1\n"
  "global_load_dwordx4 v[12:15], v236, s[30:31] sc1\n"
  "s_mov_b64 exec, -1\n"
  "s_waitcnt lgkmcnt(6)\n"
  "v_mfma_f32_32x32x16_bf16 v[176:191], v[92:95], v[152:155], v[176:191]\n"
  "s_add_u32 s30, s6, 229376\n"
  "s_addc_u32 s31, s7, 0\n"
  "s_mov_b64 exec, s[70:71]\n"
  "global_load_dwordx4 v[24:27], v235, s[30:31] sc1\n"
  "global_load_dwordx4 v[28:31], v236, s[30:31] sc1\n"
  "s_mov_b64 exec, -1\n"
  "s_waitcnt lgkmcnt(0)\n"
  "v_mfma_f32_32x32x16_bf16 v[176:191], v[192:195], v[156:159], v[176:191]\n"
  "v_mfma_f32_32x32x16_bf16 v[160:175], v[64:67], v[224:227], v[160:175]\n"
  "v_mfma_f32_32x32x16_bf16 v[160:175], v[68:71], v[228:231], v[160:175]\n"
  "s_nop 9\n"
  "v_mul_f32 v176, s29, v176\n"
  "v_mul_f32 v177, s29, v177\n"
  "v_mul_f32 v178, s29, v178\n"
  "v_mul_f32 v179, s29, v179\n"
  "v_mul_f32 v180, s29, v180\n"
  "v_mul_f32 v181, s29, v181\n"
  "v_mfma_f32_32x32x16_bf16 v[160:175], v[72:75], v[116:119], v[160:175]\n"
  "v_mul_f32 v182, s29, v182\n"
  "v_mul_f32 v183, s29, v183\n"
  "v_mul_f32 v184, s29, v184\n"
  "v_mul_f32 v185, s29, v185\n"
  "v_mul_f32 v186, s29, v186\n"
  "v_mul_f32 v187, s29, v187\n"
  "v_mfma_f32_32x32x16_bf16 v[160:175], v[76:79], v[196:199], v[160:175]\n"
  "v_mul_f32 v188, s29, v188\n"
  "v_mul_f32 v189, s29, v189\n"
  "v_mul_f32 v190, s29, v190\n"
  "v_mul_f32 v191, s29, v191\n"
  "v_cvt_pk_bf16_f32 v176, v176, v177\n"
  "v_cvt_pk_bf16_f32 v177, v178, v179\n"
  "v_mfma_f32_32x32x16_bf16 v[96:111], v[48:51], v[48:51], 0\n"
  "v_cvt_pk_bf16_f32 v178, v180, v181\n"
  "v_cvt_pk_bf16_f32 v179, v182, v183\n"
  "v_cvt_pk_bf16_f32 v180, v184, v185\n"
  "v_cvt_pk_bf16_f32 v181, v186, v187\n"
  "v_cvt_pk_bf16_f32 v182, v188, v189\n"
  "v_cvt_pk_bf16_f32 v183, v190, v191\n"
  "v_mfma_f32_32x32x16_bf16 v[112:127], v[48:51], v[48:51], 0\n"
  "ds_write_b128 v251, v[176:179]\n"
  "v_cndmask_b32_e64 v182, v182, 0, s[4:5]\n"
  "v_cndmask_b32_e64 v183, v183, 0, s[4:5]\n"
  "ds_write_b128 v251, v[180:183] offset:1024\n"
  "v_mfma_f32_32x32x16_bf16 v[128:143], v[48:51], v[48:51], 0\n"
  "v_mfma_f32_32x32x16_bf16 v[144:159], v[48:51], v[48:51], 0\n"
  "s_branch LE%=\n"
  "LT13%=:\n"
  "v_subrev_u32 v233, 43008, v236\n"
  "s_waitcnt vmcnt(6) lgkmcnt(0)\n"
  "s_barrier\n"
  "ds_read_b128 v[192:195], v237 offset:32768\n"
  "ds_read_b64_tr_b16 v[196:197], v242 offset:40960\n"
  "ds_read_b64_tr_b16 v[198:199], v242 offset:41216\n"
  "ds_read_b128 v[200:203], v238 offset:32768\n"
  "ds_read_b64_tr_b16 v[204:205], v242 offset:41984\n"
  "ds_read_b64_tr_b16 v[206:207], v242 offset:42240\n"
  "ds_read_b128 v[208:211], v239 offset:32768\n"
  "ds_read_b64_tr_b16 v[212:213], v242 offset:43008\n"
  "ds_read_b64_tr_b16 v[214:215], v242 offset:43264\n"
  "ds_read_b128 v[224:227], v240 offset:32768\n"
  "ds_read_b64_tr_b16 v[228:229], v242 offset:44032\n"
  "ds_read_b64_tr_b16 v[230:231], v242 offset:44288\n"
  "s_add_u32 m0, s53, 67584\n"
  "s_add_u32 s66, s52, 43008\n"
  "buffer_load_dwordx4 v234, s[36:39], s66 offen lds\n"
  "s_add_u32 m0, s53, 68608\n"
  "s_nop 0\n"
  "buffer_load_dwordx4 v235, s[36:39], s66 offen lds\n"
  "s_add_u32 m0, s53, 69632\n"
  "s_add_u32 s66, s52, 57344\n"
  "buffer_load_dwordx4 v234, s[36:39], s66 offen lds\n"
  "s_add_u32 m0, s53, 70656\n"
  "s_nop 0\n"
  "buffer_load_dwordx4 v235, s[36:39], s66 offen lds\n"
  "s_add_u32 m0, s68, 76800\n"
  "s_mul_i32 s65, s52, 1344\n"
  "buffer_load_dwordx4 v233, s[40:43], s65 offen lds\n"
  "s_add_u32 m0, s68, 77824\n"
  "s_mul_i32 s65, s52, 1344\n"
  "buffer_load_dwordx4 v236, s[40:43], s65 offen lds\n"
  "s_add_u32 s52, s52, 128\n"
  "s_cmp_eq_u32 s52, 896\n"
  "s_cselect_b32 s52, 0, s52\n"
  "v_mbcnt_lo_u32_b32 v254, -1, 0\n"
  "v_mbcnt_hi_u32_b32 v254, -1, v254\n"
  "v_bfe_u32 v255, v254, 4, 1\n"
  "v_mul_u32_u24 v232, s63, v255\n"
  "v_lshrrev_b32 v255, 5, v254\n"
  "v_lshl_add_u32 v232, v255, 14, v232\n"
  "v_bfe_u32 v255, v254, 2, 2\n"
  "v_lshl_add_u32 v232, v255, 7, v232\n"
  "v_add_u32 v232, s62, v232\n"
  "s_waitcnt vmcnt(9) lgkmcnt(0)\n"
  "s_barrier\n"
  "v_mfma_f32_32x32x16_bf16 v[160:175], v[192:195], v[0:3], 0\n"
  "ds_read_b128 v[80:83], v237 offset:45056\n"
  "ds_read_b64_tr_b16 v[84:85], v242 offset:53248\n"
  "ds_read_b64_tr_b16 v[86:87], v242 offset:53504\n"
  "v_mfma_f32_32x32x16_bf16 v[176:191], v[196:199], v[192:195], 0\n"
  "ds_read_b128 v[88:91], v238 offset:45056\n"
  "ds_read_b64_tr_b16 v[92:93], v242 offset:54272\n"
  "ds_read_b64_tr_b16 v[94:95], v242 offset:54528\n"
  "v_mfma_f32_32x32x16_bf16 v[160:175], v[200:203], v[4:7], v[160:175]\n"
  "ds_read_b128 v[96:99], v239 offset:45056\n"
  "ds_read_b64_tr_b16 v[100:101], v242 offset:55296\n"
  "ds_read_b64_tr_b16 v[102:103], v242 offset:55552\n"
  "v_mfma_f32_32x32x16_bf16 v[176:191], v[204:207], v[200:203], v[176:191]\n"
  "ds_read_b128 v[104:107], v240 offset:45056\n"
  "ds_read_b64_tr_b16 v[108:109], v242 offset:56320\n"
  "ds_read_b64_tr_b16 v[110:111], v242 offset:56576\n"
  "v_mfma_f32_32x32x16_bf16 v[160:175], v[208:211], v[8:11], v[160:175]\n"
  "s_add_u32 m0, s53, 30720\n"
  "s_add_u32 s66, s52, 43008\n"
  "buffer_load_dwordx4 v234, s[36:39], s66 offen lds\n"
  "s_add_u32 m0, s53, 31744\n"
  "s_nop 0\n"
  "buffer_load_dwordx4 v235, s[36:39], s66 offen lds\n"
  "v_mfma_f32_32x32x16_bf16 v[176:191], v[212:215], v[208:211], v[176:191]\n"
  "s_add_u32 m0, s53, 32768\n"
  "s_add_u32 s66, s52, 57344\n"
  "buffer_load_dwordx4 v234, s[36:39], s66 offen lds\n"
  "s_add_u32 m0, s53, 33792\n"
  "s_nop 0\n"
  "buffer_load_dwordx4 v235, s[36:39], s66 offen lds\n"
  "v_mfma_f32_32x32x16_bf16 v[160:175], v[224:227], v[12:15], v[160:175]\n"
  "s_add_u32 m0, s68, 39936\n"
  "s_mul_i32 s65, s52, 1344\n"
  "buffer_load_dwordx4 v233, s[40:43], s65 offen lds\n"
  "s_add_u32 m0, s68, 40960\n"
  "s_mul_i32 s65, s52, 1344\n"
  "buffer_load_dwordx4 v236, s[40:43], s65 offen lds\n"
  "s_add_u32 s52, s52, 128\n"
  "s_cmp_eq_u32 s52, 896\n"
  "s_cselect_b32 s52, 0, s52\n"
  "v_mfma_f32_32x32x16_bf16 v[176:191], v[228:231], v[224:227], v[176:191]\n"
  "buffer_store_dwordx4 v[216:219], v243, s[44:47], s56 offen sc1\n"
  "buffer_store_dwordx4 v[220:223], v243, s[44:47], s67 offen sc1\n"
  "s_waitcnt vmcnt(14) lgkmcnt(0)\n"
  "s_barrier\n"
  "v_mfma_f32_32x32x16_bf16 v[160:175], v[80:83], v[16:19], v[160:175]\n"
  "ds_read_b128 v[112:115], v237 offset:57344\n"
  "ds_read_b64_tr_b16 v[116:117], v250 offset:32768\n"
  "ds_read_b64_tr_b16 v[118:119], v250 offset:33024\n"
  "v_mfma_f32_32x32x16_bf16 v[176:191], v[84:87], v[80:83], v[176:191]\n"
  "ds_read_b128 v[120:123], v238 offset:57344\n"
  "ds_read_b64_tr_b16 v[124:125], v250 offset:33792\n"
  "ds_read_b64_tr_b16 v[126:127], v250 offset:34048\n"
  "v_mfma_f32_32x32x16_bf16 v[160:175], v[88:91], v[20:23], v[160:175]\n"
  "ds_read_b128 v[128:131], v239 offset:57344\n"
  "ds_read_b64_tr_b16 v[132:133], v250 offset:34816\n"
  "ds_read_b64_tr_b16 v[134:135], v250 offset:35072\n"
  "v_mfma_f32_32x32x16_bf16 v[176:191], v[92:95], v[88:91], v[176:191]\n"
  "ds_read_b128 v[136:139], v240 offset:57344\n"
  "ds_read_b64_tr_b16 v[140:141], v250 offset:35840\n"
  "ds_read_b64_tr_b16 v[142:143], v250 offset:36096\n"
  "v_mfma_f32_32x32x16_bf16 v[160:175], v[96:99], v[24:27], v[160:175]\n"
  "s_add_u32 m0, s53, 43008\n"
  "s_add_u32 s66, s52, 43008\n"
  "buffer_load_dwordx4 v234, s[36:39], s66 offen lds\n"
  "s_add_u32 m0, s53, 44032\n"
  "s_nop 0\n"
  "buffer_load_dwordx4 v235, s[36:39], s66 offen lds\n"
  "v_mfma_f32_32x32x16_bf16 v[176:191], v[100:103], v[96:99], v[176:191]\n"
  "s_add_u32 m0, s53, 45056\n"
  "s_add_u32 s66, s52, 57344\n"
  "buffer_load_dwordx4 v234, s[36:39], s66 offen lds\n"
  "s_add_u32 m0, s53, 46080\n"
  "s_nop 0\n"
  "buffer_load_dwordx4 v235, s[36:39], s66 offen lds\n"
  "v_mfma_f32_32x32x16_bf16 v[160:175], v[104:107], v[28:31], v[160:175]\n"
  "s_add_u32 m0, s68, 52224\n"
  "s_mul_i32 s65, s52, 1344\n"
  "buffer_load_dwordx4 v233, s[40:43], s65 offen lds\n"
  "s_add_u32 m0, s68, 53248\n"
  "s_mul_i32 s65, s52, 1344\n"
  "buffer_load_dwordx4 v236, s[40:43], s65 offen lds\n"
  "s_add_u32 s52, s52, 128\n"
  "s_cmp_eq_u32 s52, 896\n"
  "s_cselect_b32 s52, 0, s52\n"
  "v_mfma_f32_32x32x16_bf16 v[176:191], v[108:111], v[104:107], v[176:191]\n"
  "v_mov_b32 v8, 0x3f803f80\n"
  "v_mov_b32 v9, 0x3f803f80\n"
  "v_mov_b32 v10, 0x3f803f80\n"
  "v_mov_b32 v11, 0x3f803f80\n"
  "v_mov_b32 v12, 0x3f803f80\n"
  "v_mov_b32 v13, 0x3f803f80\n"
  "v_mov_b32 v14, 0x3f803f80\n"
  "v_mov_b32 v15, 0x3f803f80\n"
  "s_waitcnt vmcnt(14) lgkmcnt(0)\n"
  "s_barrier\n"
  "v_mfma_f32_32x32x16_bf16 v[160:175], v[112:115], v[32:35], v[160:175]\n"
  "ds_read_b128 v[144:147], v245 offset:36864\n"
  "ds_read_b64_tr_b16 v[148:149], v250 offset:45056\n"
  "ds_read_b64_tr_b16 v[150:151], v250 offset:45312\n"
  "v_mfma_f32_32x32x16_bf16 v[176:191], v[116:119], v[112:115], v[176:191]\n"
  "ds_read_b128 v[152:155], v246 offset:36864\n"
  "ds_read_b64_tr_b16 v[156:157], v250 offset:46080\n"
  "ds_read_b64_tr_b16 v[158:159], v250 offset:46336\n"
  "v_mfma_f32_32x32x16_bf16 v[160:175], v[120:123], v[36:39], v[160:175]\n"
  "ds_read_b128 v[192:195], v247 offset:36864\n"
  "ds_read_b64_tr_b16 v[196:197], v250 offset:47104\n"
  "ds_read_b64_tr_b16 v[198:199], v250 offset:47360\n"
  "v_mfma_f32_32x32x16_bf16 v[176:191], v[124:127], v[120:123], v[176:191]\n"
  "ds_read_b128 v[200:203], v248 offset:36864\n"
  "ds_read_b64_tr_b16 v[204:205], v250 offset:48128\n"
  "ds_read_b64_tr_b16 v[206:207], v250 offset:48384\n"
  "v_mfma_f32_32x32x16_bf16 v[160:175], v[128:131], v[40:43], v[160:175]\n"
  "s_add_u32 m0, s53, 55296\n"
  "s_add_u32 s66, s52, 43008\n"
  "buffer_load_dwordx4 v234, s[36:39], s66 offen lds\n"
  "s_add_u32 m0, s53, 56320\n"
  "s_nop 0\n"
  "buffer_load_dwordx4 v235, s[36:39], s66 offen lds\n"
  "v_mfma_f32_32x32x16_bf16 v[176:191], v[132:135], v[128:131], v[176:191]\n"
  "s_add_u32 m0, s53, 57344\n"
  "s_add_u32 s66, s52, 57344\n"
  "buffer_load_dwordx4 v234, s[36:39], s66 offen lds\n"
  "s_add_u32 m0, s53, 58368\n"
  "s_nop 0\n"
  "buffer_load_dwordx4 v235, s[36:39], s66 offen lds\n"
  "v_mfma_f32_32x32x16_bf16 v[160:175], v[136:139], v[44:47], v[160:175]\n"
  "s_add_u32 m0, s68, 64512\n"
  "s_mul_i32 s65, s52, 1344\n"
  "buffer_load_dwordx4 v233, s[40:43], s65 offen lds\n"
  "s_add_u32 m0, s68, 65536\n"
  "s_mul_i32 s65, s52, 1344\n"
  "buffer_load_dwordx4 v236, s[40:43], s65 offen lds\n"
  "s_add_u32 s52, s52, 128\n"
  "s_cmp_eq_u32 s52, 896\n"
  "s_cselect_b32 s52, 0, s52\n"
  "v_mfma_f32_32x32x16_bf16 v[176:191], v[140:143], v[136:139], v[176:191]\n"
  "v_mov_b32 v24, 0x3f803f80\n"
  "v_mov_b32 v25, 0x3f803f80\n"
  "v_mov_b32 v26, 0x3f803f80\n"
  "v_mov_b32 v27, 0x3f803f80\n"
  "v_mov_b32 v28, 0x3f803f80\n"
  "v_mov_b32 v29, 0x3f803f80\n"
  "v_mov_b32 v30, 0x3f803f80\n"
  "v_mov_b32 v31, 0x3f803f80\n"
  "s_waitcnt vmcnt(14) lgkmcnt(0)\n"
  "s_barrier\n"
  "v_mfma_f32_32x32x16_bf16 v[160:175], v[144:147], v[48:51], v[160:175]\n"
  "ds_read_b128 v[208:211], v237 offset:32768\n"
  "ds_read_b64_tr_b16 v[212:213], v242 offset:40960\n"
  "ds_read_b64_tr_b16 v[214:215], v242 offset:41216\n"
  "v_mfma_f32_32x32x16_bf16 v[176:191], v[148:151], v[144:147], v[176:191]\n"
  "ds_read_b128 v[224:227], v238 offset:32768\n"
  "ds_read_b64_tr_b16 v[228:229], v242 offset:41984\n"
  "ds_read_b64_tr_b16 v[230:231], v242 offset:42240\n"
  "v_mfma_f32_32x32x16_bf16 v[160:175], v[152:155], v[52:55], v[160:175]\n"
  "ds_read_b128 v[80:83], v239 offset:32768\n"
  "ds_read_b64_tr_b16 v[84:85], v242 offset:43008\n"
  "ds_read_b64_tr_b16 v[86:87], v242 offset:43264\n"
  "v_mfma_f32_32x32x16_bf16 v[176:191], v[156:159], v[152:155], v[176:191]\n"
  "ds_read_b128 v[88:91], v240 offset:32768\n"
  "ds_read_b64_tr_b16 v[92:93], v242 offset:44032\n"
  "ds_read_b64_tr_b16 v[94:95], v242 offset:44288\n"
  "v_mfma_f32_32x32x16_bf16 v[160:175], v[192:195], v[56:59], v[160:175]\n"
  "v_mbcnt_lo_u32_b32 v236, -1, 0\n"
  "v_mbcnt_hi_u32_b32 v236, -1, v236\n"
  "v_and_b32 v235, 31, v236\n"
  "v_lshrrev_b32 v236, 5, v236\n"
  "v_lshl_add_u32 v233, v235, 4, s58\n"
  "v_mad_i32_i24 v233, v236, s59, v233\n"
  "v_lshl_add_u32 v234, s59, 1, v233\n"
  "v_mad_i32_i24 v234, v236, s60, v234\n"
  "v_mfma_f32_32x32x16_bf16 v[176:191], v[196:199], v[192:195], v[176:191]\n"
  "v_lshl_add_u32 v235, v235, 4, s61\n"
  "v_mul_u32_u24 v236, 0x1c00, v236\n"
  "v_add_u32 v235, v235, v236\n"
  "v_add_u32 v236, 0x3800, v235\n"
  "s_mul_i32 s52, s54, 917504\n"
  "s_add_u32 s52, s52, 1048576\n"
  "s_add_u32 s6, s44, s52\n"
  "s_addc_u32 s7, s45, 0\n"
  "v_mfma_f32_32x32x16_bf16 v[160:175], v[200:203], v[60:63], v[160:175]\n"
  "s_mov_b32 s70, 0x0fffffff\n"
  "s_mov_b32 s71, 0x0fffffff\n"
  "v_mov_b32 v40, 0x3f803f80\n"
  "v_mov_b32 v41, 0x3f803f80\n"
  "v_mov_b32 v42, 0x3f803f80\n"
  "v_mov_b32 v43, 0x3f803f80\n"
  "v_mfma_f32_32x32x16_bf16 v[176:191], v[204:207], v[200:203], v[176:191]\n"
  "v_mov_b32 v44, 0x3f803f80\n"
  "v_mov_b32 v45, 0x3f803f80\n"
  "v_mov_b32 v46, 0x3f803f80\n"
  "v_mov_b32 v47, 0x3f803f80\n"
  "s_waitcnt vmcnt(6) lgkmcnt(0)\n"
  "s_barrier\n"
  "v_mfma_f32_32x32x16_bf16 v[160:175], v[208:211], v[64:67], v[160:175]\n"
  "ds_read_b128 v[96:99], v237 offset:45056\n"
  "ds_read_b64_tr_b16 v[100:101], v242 offset:53248\n"
  "ds_read_b64_tr_b16 v[102:103], v242 offset:53504\n"
  "ds_read_b64_tr_b16 v[104:105], v241 offset:0\n"
  "ds_read_b64_tr_b16 v[106:107], v241 offset:512\n"
  "v_mfma_f32_32x32x16_bf16 v[176:191], v[212:215], v[208:211], v[176:191]\n"
  "ds_read_b128 v[108:111], v238 offset:45056\n"
  "ds_read_b64_tr_b16 v[112:113], v242 offset:54272\n"
  "ds_read_b64_tr_b16 v[114:115], v242 offset:54528\n"
  "ds_read_b64_tr_b16 v[116:117], v241 offset:2048\n"
  "ds_read_b64_tr_b16 v[118:119], v241 offset:2560\n"
  "v_mfma_f32_32x32x16_bf16 v[160:175], v[224:227], v[68:71], v[160:175]\n"
  "ds_read_b128 v[120:123], v239 offset:45056\n"
  "ds_read_b64_tr_b16 v[124:125], v242 offset:55296\n"
  "ds_read_b64_tr_b16 v[126:127], v242 offset:55552\n"
  "ds_read_b64_tr_b16 v[128:129], v241 offset:4096\n"
  "ds_read_b64_tr_b16 v[130:131], v241 offset:4608\n"
  "v_mfma_f32_32x32x16_bf16 v[176:191], v[228:231], v[224:227], v[176:191]\n"
  "ds_read_b128 v[132:135], v240 offset:45056\n"
  "ds_read_b64_tr_b16 v[136:137], v242 offset:56320\n"
  "ds_read_b64_tr_b16 v[138:139], v242 offset:56576\n"
  "ds_read_b64_tr_b16 v[140:141], v241 offset:6144\n"
  "ds_read_b64_tr_b16 v[142:143], v241 offset:6656\n"
  "v_mfma_f32_32x32x16_bf16 v[160:175], v[80:83], v[72:75], v[160:175]\n"
  "buffer_load_dwordx2 v[254:255], v232, s[44:47], 0 offen sc1\n"
  "v_mov_b32 v56, 0x3f803f80\n"
  "v_mov_b32 v57, 0x3f803f80\n"
  "v_mov_b32 v58, 0x3f803f80\n"
  "v_mov_b32 v59, 0x3f803f80\n"
  "v_mfma_f32_32x32x16_bf16 v[176:191], v[84:87], v[80:83], v[176:191]\n"
  "v_mov_b32 v60, 0x3f803f80\n"
  "v_mov_b32 v61, 0x3f803f80\n"
  "v_mov_b32 v62, 0x3f803f80\n"
  "v_mov_b32 v63, 0x3f803f80\n"
  "s_add_u32 s22, s54, 1\n"
  "s_and_b32 s22, s22, 3\n"
  "s_mul_i32 s55, s22, 12288\n"
  "s_add_u32 s23, s54, 2\n"
  "s_and_b32 s23, s23, 3\n"
  "s_mul_i32 s60, s23, 12288\n"
  "v_mfma_f32_32x32x16_bf16 v[160:175], v[88:91], v[76:79], v[160:175]\n"
  "s_add_u32 s60, s60, 4096\n"
  "s_add_u32 s21, s54, 3\n"
  "s_and_b32 s21, s21, 3\n"
  "s_mul_i32 s61, s21, 12288\n"
  "s_add_u32 s61, s61, 8192\n"
  "s_mul_i32 s63, s54, 12288\n"
  "s_mul_i32 s28, s54, 7168\n"
  "s_add_u32 s64, s64, s28\n"
  "s_lshl_b32 s28, s24, 6\n"
  "s_add_u32 s28, s28, s20\n"
  "s_lshl_b32 s28, s28, 7\n"
  "s_add_u32 s62, s28, 69632\n"
  "v_mfma_f32_32x32x16_bf16 v[176:191], v[92:95], v[88:91], v[176:191]\n"
  "s_mov_b32 s67, 0x8000\n"
  "s_mov_b32 s29, 0x3e8b97f6\n"
  "s_mov_b32 s4, 0\n"
  "s_mov_b32 s5, -1\n"
  "s_mov_b32 s28, 0x8000\n"
  "v_mov_b32 v48, 0\n"
  "v_mov_b32 v49, 0\n"
  "v_mov_b32 v50, 0\n"
  "v_mov_b32 v51, 0\n"
  "s_waitcnt vmcnt(1) lgkmcnt(0)\n"
  "s_barrier\n"
  "v_mfma_f32_32x32x16_bf16 v[160:175], v[96:99], v[104:107], v[160:175]\n"
  "ds_read_b128 v[144:147], v237 offset:57344\n"
  "ds_read_b64_tr_b16 v[148:149], v250 offset:32768\n"
  "ds_read_b64_tr_b16 v[150:151], v250 offset:33024\n"
  "ds_read_b64_tr_b16 v[152:153], v241 offset:16384\n"
  "ds_read_b64_tr_b16 v[154:155], v241 offset:16896\n"
  "v_mfma_f32_32x32x16_bf16 v[176:191], v[100:103], v[96:99], v[176:191]\n"
  "ds_read_b128 v[156:159], v238 offset:57344\n"
  "ds_read_b64_tr_b16 v[192:193], v250 offset:33792\n"
  "ds_read_b64_tr_b16 v[194:195], v250 offset:34048\n"
  "ds_read_b64_tr_b16 v[196:197], v241 offset:18432\n"
  "ds_read_b64_tr_b16 v[198:199], v241 offset:18944\n"
  "v_mfma_f32_32x32x16_bf16 v[160:175], v[108:111], v[116:119], v[160:175]\n"
  "ds_read_b128 v[200:203], v239 offset:57344\n"
  "ds_read_b64_tr_b16 v[204:205], v250 offset:34816\n"
  "ds_read_b64_tr_b16 v[206:207], v250 offset:35072\n"
  "ds_read_b64_tr_b16 v[64:65], v241 offset:20480\n"
  "ds_read_b64_tr_b16 v[66:67], v241 offset:20992\n"
  "v_mfma_f32_32x32x16_bf16 v[176:191], v[112:115], v[108:111], v[176:191]\n"
  "ds_read_b128 v[208:211], v240 offset:57344\n"
  "ds_read_b64_tr_b16 v[212:213], v250 offset:35840\n"
  "ds_read_b64_tr_b16 v[214:215], v250 offset:36096\n"
  "ds_read_b64_tr_b16 v[68:69], v241 offset:22528\n"
  "ds_read_b64_tr_b16 v[70:71], v241 offset:23040\n"
  "v_mfma_f32_32x32x16_bf16 v[160:175], v[120:123], v[128:131], v[160:175]\n"
  "v_and_b32 v238, 0x3f0, v251\n"
  "v_lshl_add_u32 v249, s22, 11, v238\n"
  "v_lshl_add_u32 v250, s23, 11, v238\n"
  "v_lshl_add_u32 v241, s21, 11, v238\n"
  "v_add_u32 v245, s55, v238\n"
  "v_mfma_f32_32x32x16_bf16 v[176:191], v[124:127], v[120:123], v[176:191]\n"
  "v_add_u32 v246, s60, v238\n"
  "v_add_u32 v247, s61, v238\n"
  "v_add_u32 v248, s63, v238\n"
  "v_mov_b32 v237, 0xdddddddd\n"
  "v_lshrrev_b32 v240, 7, v238\n"
  "v_mfma_f32_32x32x16_bf16 v[160:175], v[132:135], v[140:143], v[160:175]\n"
  "v_lshrrev_b32 v242, 2, v238\n"
  "v_and_b32 v242, 64, v242\n"
  "v_and_b32 v239, 0x70, v238\n"
  "v_mul_u32_u24 v240, 0x380, v240\n"
  "v_xad_u32 v239, v239, v242, v240\n"
  "v_mfma_f32_32x32x16_bf16 v[176:191], v[136:139], v[132:135], v[176:191]\n"
  "v_add_u32 v239, s64, v239\n"
  "v_bfe_u32 v240, v238, 4, 3\n"
  "v_and_b32 v242, 4, v240\n"
  "v_add_lshl_u32 v240, v240, v242, 11\n"
  "s_waitcnt vmcnt(0)\n"
  "v_xor_b32 v254, v254, v252\n"
  "v_xor_b32 v255, v255, v253\n"
  "v_or_b32 v254, v254, v255\n"
  "v_cmp_ne_u32 vcc, 0, v254\n"
  "s_cbranch_vccz LDa111%=\n"
  "s_mov_b32 s69, 0x40000\n"
  "LPa111%=:\n"
  "s_sleep 1\n"
  "buffer_load_dwordx2 v[254:255], v232, s[44:47], 0 offen sc1\n"
  "s_waitcnt vmcnt(0)\n"
  "v_xor_b32 v254, v254, v252\n"
  "v_xor_b32 v255, v255, v253\n"
  "v_or_b32 v254, v254, v255\n"
  "v_cmp_ne_u32 vcc, 0, v254\n"
  "s_cbranch_vccz LDa111%=\n"
  "s_sub_u32 s69, s69, 1\n"
  "s_cmp_lg_u32 s69, 0\n"
  "s_cbranch_scc1 LPa111%=\n"
  "LDa111%=:\n"
  "s_add_u32 s30, s6, 0\n"
  "s_addc_u32 s31, s7, 0\n"
  "global_load_dwordx4 v[0:3], v233, s[30:31] sc1\n"
  "global_load_dwordx4 v[4:7], v234, s[30:31] sc1\n"
  "s_waitcnt lgkmcnt(14)\n"
  "v_mfma_f32_32x32x16_bf16 v[176:191], v[148:151], v[144:147], v[176:191]\n"
  "s_add_u32 s30, s6, 229376\n"
  "s_addc_u32 s31, s7, 0\n"
  "global_load_dwordx4 v[16:19], v233, s[30:31] sc1\n"
  "global_load_dwordx4 v[20:23], v234, s[30:31] sc1\n"
  "s_waitcnt lgkmcnt(10)\n"
  "v_mfma_f32_32x32x16_bf16 v[176:191], v[192:195], v[156:159], v[176:191]\n"
  "s_add_u32 s30, s6, 0\n"
  "s_addc_u32 s31, s7, 0\n"
  "s_mov_b64 exec, s[70:71]\n"
  "global_load_dwordx4 v[8:11], v235, s[30:31] sc1\n"
  "global_load_dwordx4 v[12:15], v236, s[30:31] sc1\n"
  "s_mov_b64 exec, -1\n"
  "s_waitcnt lgkmcnt(5)\n"
  "v_mfma_f32_32x32x16_bf16 v[176:191], v[204:207], v[200:203], v[176:191]\n"
  "s_add_u32 s30, s6, 229376\n"
  "s_addc_u32 s31, s7, 0\n"
  "s_mov_b64 exec, s[70:71]\n"
  "global_load_dwordx4 v[24:27], v235, s[30:31] sc1\n"
  "global_load_dwordx4 v[28:31], v236, s[30:31] sc1\n"
  "s_mov_b64 exec, -1\n"
  "s_waitcnt lgkmcnt(0)\n"
  "v_mfma_f32_32x32x16_bf16 v[176:191], v[212:215], v[208:211], v[176:191]\n"
  "v_mfma_f32_32x32x16_bf16 v[160:175], v[144:147], v[152:155], v[160:175]\n"
  "v_mfma_f32_32x32x16_bf16 v[160:175], v[156:159], v[196:199], v[160:175]\n"
  "s_nop 9\n"
  "v_mul_f32 v176, s29, v176\n"
  "v_mul_f32 v177, s29, v177\n"
  "v_mul_f32 v178, s29, v178\n"
  "v_mul_f32 v179, s29, v179\n"
  "v_mul_f32 v180, s29, v180\n"
  "v_mul_f32 v181, s29, v181\n"
  "v_mfma_f32_32x32x16_bf16 v[160:175], v[200:203], v[64:67], v[160:175]\n"
  "v_mul_f32 v182, s29, v182\n"
  "v_mul_f32 v183, s29, v183\n"
  "v_mul_f32 v184, s29, v184\n"
  "v_mul_f32 v185, s29, v185\n"
  "v_mul_f32 v186, s29, v186\n"
  "v_mul_f32 v187, s29, v187\n"
  "v_mfma_f32_32x32x16_bf16 v[160:175], v[208:211], v[68:71], v[160:175]\n"
  "v_mul_f32 v188, s29, v188\n"
  "v_mul_f32 v189, s29, v189\n"
  "v_mul_f32 v190, s29, v190\n"
  "v_mul_f32 v191, s29, v191\n"
  "v_cvt_pk_bf16_f32 v176, v176, v177\n"
  "v_cvt_pk_bf16_f32 v177, v178, v179\n"
  "v_mfma_f32_32x32x16_bf16 v[96:111], v[48:51], v[48:51], 0\n"
  "v_cvt_pk_bf16_f32 v178, v180, v181\n"
  "v_cvt_pk_bf16_f32 v179, v182, v183\n"
  "v_cvt_pk_bf16_f32 v180, v184, v185\n"
  "v_cvt_pk_bf16_f32 v181, v186, v187\n"
  "v_cvt_pk_bf16_f32 v182, v188, v189\n"
  "v_cvt_pk_bf16_f32 v183, v190, v191\n"
  "v_mfma_f32_32x32x16_bf16 v[112:127], v[48:51], v[48:51], 0\n"
  "ds_write_b128 v251, v[176:179]\n"
  "v_cndmask_b32_e64 v182, v182, 0, s[4:5]\n"
  "v_cndmask_b32_e64 v183, v183, 0, s[4:5]\n"
  "ds_write_b128 v251, v[180:183] offset:1024\n"
  "v_mfma_f32_32x32x16_bf16 v[128:143], v[48:51], v[48:51], 0\n"
  "v_mfma_f32_32x32x16_bf16 v[144:159], v[48:51], v[48:51], 0\n"
  "s_nop 0\n"
  "LE%=:\n"
  "s_waitcnt lgkmcnt(0)\n"
  "s_barrier\n"
  "ds_read_b128 v[64:67], v249\n"
  "ds_read_b128 v[68:71], v249 offset:1024\n"
  "ds_read_b128 v[72:75], v250\n"
  "ds_read_b128 v[76:79], v250 offset:1024\n"
  "ds_read_b128 v[80:83], v241\n"
  "ds_read_b128 v[84:87], v241 offset:1024\n"
  "ds_read_b128 v[88:91], v251\n"
  "ds_read_b128 v[92:95], v251 offset:1024\n"
  "v_cvt_pk_bf16_f32 v208, v160, v161\n"
  "v_cvt_pk_bf16_f32 v209, v162, v163\n"
  "v_cvt_pk_bf16_f32 v210, v164, v165\n"
  "v_cvt_pk_bf16_f32 v211, v166, v167\n"
  "v_cvt_pk_bf16_f32 v212, v168, v169\n"
  "v_cvt_pk_bf16_f32 v213, v170, v171\n"
  "v_cvt_pk_bf16_f32 v214, v172, v173\n"
  "v_cvt_pk_bf16_f32 v215, v174, v175\n"
  "v_mov_b32 v216, -1\n"
  "v_mov_b32 v217, -1\n"
  "v_mov_b32 v218, -1\n"
  "v_mov_b32 v219, -1\n"
  "v_mov_b32 v242, 0x3f80\n"
  "s_waitcnt lgkmcnt(0)\n"
  "s_waitcnt vmcnt(6)\n"
  "v_mfma_f32_32x32x16_bf16 v[160:175], v[0:3], v[64:67], 0\n"
  "v_cndmask_b32_e64 v6, v6, v242, s[4:5]\n"
  "s_nop 1\n"
  "v_mfma_f32_32x32x16_bf16 v[160:175], v[4:7], v[68:71], v[160:175]\n"
  "s_add_u32 s30, s6, 458752\n"
  "s_addc_u32 s31, s7, 0\n"
  "global_load_dwordx4 v[32:35], v233, s[30:31] sc1\n"
  "global_load_dwordx4 v[36:39], v234, s[30:31] sc1\n"
  "s_mov_b64 exec, s[70:71]\n"
  "global_load_dwordx4 v[40:43], v235, s[30:31] sc1\n"
  "global_load_dwordx4 v[44:47], v236, s[30:31] sc1\n"
  "s_mov_b64 exec, -1\n"
  "s_add_u32 s30, s6, 688128\n"
  "s_addc_u32 s31, s7, 0\n"
  "global_load_dwordx4 v[48:51], v233, s[30:31] sc1\n"
  "global_load_dwordx4 v[52:55], v234, s[30:31] sc1\n"
  "s_mov_b64 exec, s[70:71]\n"
  "global_load_dwordx4 v[56:59], v235, s[30:31] sc1\n"
  "global_load_dwordx4 v[60:63], v236, s[30:31] sc1\n"
  "s_mov_b64 exec, -1\n"
  "v_max3_f32 v192, v160, v161, v162\n"
  "v_max3_f32 v193, v163, v164, v165\n"
  "v_max3_f32 v193, v166, v167, v193\n"
  "v_max3_f32 v192, v168, v169, v192\n"
  "v_max3_f32 v193, v170, v171, v193\n"
  "v_max3_f32 v192, v172, v173, v192\n"
  "v_max3_f32 v193, v174, v175, v193\n"
  "v_max_f32 v194, v192, v193\n"
  "v_max_f32 v195, v192, v193\n"
  "s_nop 1\n"
  "v_permlane32_swap_b32 v194, v195\n"
  "v_max_f32 v192, v194, v195\n"
  "v_cvt_pk_bf16_f32 v192, -v192, 0\n"
  "v_cndmask_b32_e64 v70, v70, v192, s[4:5]\n"
  "v_lshlrev_b32 v192, 16, v192\n"
  "ds_write_b32 v249, v70 offset:8192\n"
  "s_waitcnt vmcnt(12)\n"
  "v_cndmask_b32_e64 v22, v22, v242, s[4:5]\n"
  "v_add_f32 v160, v192, v160\n"
  "v_add_f32 v161, v192, v161\n"
  "v_add_f32 v162, v192, v162\n"
  "v_add_f32 v163, v192, v163\n"
  "v_add_f32 v164, v192, v164\n"
  "v_add_f32 v165, v192, v165\n"
  "v_add_f32 v166, v192, v166\n"
  "v_add_f32 v167, v192, v167\n"
  "v_add_f32 v168, v192, v168\n"
  "v_add_f32 v169, v192, v169\n"
  "v_add_f32 v170, v192, v170\n"
  "v_add_f32 v171, v192, v171\n"
  "v_add_f32 v172, v192, v172\n"
  "v_add_f32 v173, v192, v173\n"
  "v_add_f32 v174, v192, v174\n"
  "v_add_f32 v175, v192, v175\n"
  "v_mfma_f32_32x32x16_bf16 v[176:191], v[16:19], v[64:67], 0\n"
  "v_exp_f32 v192, v160\n"
  "v_exp_f32 v193, v161\n"
  "v_exp_f32 v194, v162\n"
  "v_exp_f32 v195, v163\n"
  "v_mfma_f32_32x32x16_bf16 v[176:191], v[20:23], v[68:71], v[176:191]\n"
  "v_exp_f32 v196, v164\n"
  "v_exp_f32 v197, v165\n"
  "v_exp_f32 v198, v166\n"
  "v_exp_f32 v199, v167\n"
  "v_exp_f32 v200, v168\n"
  "v_exp_f32 v201, v169\n"
  "s_waitcnt vmcnt(10)\n"
  "v_smfmac_f32_32x32x32_bf16 v[96:111], v[8:11], v[192:199], v237\n"
  "s_waitcnt lgkmcnt(0)\n"
  "s_barrier\n"
  "ds_read_b32 v78, v250 offset:8192\n"
  "ds_read_b32 v86, v241 offset:8192\n"
  "ds_read_b32 v94, v251 offset:8192\n"
  "v_exp_f32 v202, v170\n"
  "v_exp_f32 v203, v171\n"
  "v_exp_f32 v204, v172\n"
  "v_exp_f32 v205, v173\n"
  "v_exp_f32 v206, v174\n"
  "v_exp_f32 v207, v175\n"
  "v_mfma_f32_32x32x16_bf16 v[160:175], v[0:3], v[72:75], 0\n"
  "v_exp_f32 v192, v176\n"
  "v_exp_f32 v193, v177\n"
  "v_exp_f32 v194, v178\n"
  "v_exp_f32 v195, v179\n"
  "s_waitcnt lgkmcnt(0)\n"
  "v_mfma_f32_32x32x16_bf16 v[160:175], v[4:7], v[76:79], v[160:175]\n"
  "v_exp_f32 v196, v180\n"
  "v_exp_f32 v197, v181\n"
  "v_exp_f32 v198, v182\n"
  "v_smfmac_f32_32x32x32_bf16 v[96:111], v[12:15], v[200:207], v237\n"
  "v_exp_f32 v199, v183\n"
  "v_exp_f32 v200, v184\n"
  "v_exp_f32 v201, v185\n"
  "s_waitcnt vmcnt(8)\n"
  "v_smfmac_f32_32x32x32_bf16 v[96:111], v[24:27], v[192:199], v237\n"
  "s_setprio 1\n"
  "v_exp_f32 v202, v186\n"
  "v_exp_f32 v203, v187\n"
  "v_exp_f32 v204, v188\n"
  "v_exp_f32 v205, v189\n"
  "v_exp_f32 v206, v190\n"
  "v_exp_f32 v207, v191\n"
  "v_mfma_f32_32x32x16_bf16 v[176:191], v[0:3], v[80:83], 0\n"
  "v_exp_f32 v192, v160\n"
  "v_exp_f32 v193, v161\n"
  "v_exp_f32 v194, v162\n"
  "v_exp_f32 v195, v163\n"
  "v_mfma_f32_32x32x16_bf16 v[176:191], v[4:7], v[84:87], v[176:191]\n"
  "v_exp_f32 v196, v164\n"
  "v_exp_f32 v197, v165\n"
  "v_exp_f32 v198, v166\n"
  "v_smfmac_f32_32x32x32_bf16 v[96:111], v[28:31], v[200:207], v237\n"
  "v_exp_f32 v199, v167\n"
  "v_exp_f32 v200, v168\n"
  "v_exp_f32 v201, v169\n"
  "v_smfmac_f32_32x32x32_bf16 v[112:127], v[8:11], v[192:199], v237\n"
  "v_exp_f32 v202, v170\n"
  "v_exp_f32 v203, v171\n"
  "v_exp_f32 v204, v172\n"
  "v_exp_f32 v205, v173\n"
  "v_exp_f32 v206, v174\n"
  "v_exp_f32 v207, v175\n"
  "v_mfma_f32_32x32x16_bf16 v[160:175], v[0:3], v[88:91], 0\n"
  "v_exp_f32 v192, v176\n"
  "v_exp_f32 v193, v177\n"
  "v_exp_f32 v194, v178\n"
  "v_exp_f32 v195, v179\n"
  "v_mfma_f32_32x32x16_bf16 v[160:175], v[4:7], v[92:95], v[160:175]\n"
  "buffer_load_dwordx2 v[254:255], v232, s[44:47], s67 offen sc1\n"
  "v_exp_f32 v196, v180\n"
  "v_exp_f32 v197, v181\n"
  "v_exp_f32 v198, v182\n"
  "v_smfmac_f32_32x32x32_bf16 v[112:127], v[12:15], v[200:207], v237\n"
  "v_exp_f32 v199, v183\n"
  "v_exp_f32 v200, v184\n"
  "v_exp_f32 v201, v185\n"
  "v_smfmac_f32_32x32x32_bf16 v[128:143], v[8:11], v[192:199], v237\n"
  "v_exp_f32 v202, v186\n"
  "v_exp_f32 v203, v187\n"
  "v_exp_f32 v204, v188\n"
  "v_exp_f32 v205, v189\n"
  "v_exp_f32 v206, v190\n"
  "v_exp_f32 v207, v191\n"
  "v_mfma_f32_32x32x16_bf16 v[176:191], v[16:19], v[72:75], 0\n"
  "s_add_u32 s28, s56, 114688\n"
  "v_exp_f32 v192, v160\n"
  "v_exp_f32 v193, v161\n"
  "v_exp_f32 v194, v162\n"
  "v_exp_f32 v195, v163\n"
  "v_mfma_f32_32x32x16_bf16 v[176:191], v[20:23], v[76:79], v[176:191]\n"
  "buffer_store_dwordx4 v[208:211], v243, s[44:47], s28 offen sc1\n"
  "s_add_u32 s29, s28, s57\n"
  "v_exp_f32 v196, v164\n"
  "v_exp_f32 v197, v165\n"
  "v_exp_f32 v198, v166\n"
  "v_smfmac_f32_32x32x32_bf16 v[128:143], v[12:15], v[200:207], v237\n"
  "buffer_store_dwordx4 v[212:215], v243, s[44:47], s29 offen sc1\n"
  "v_exp_f32 v199, v167\n"
  "v_exp_f32 v200, v168\n"
  "v_exp_f32 v201, v169\n"
  "v_smfmac_f32_32x32x32_bf16 v[144:159], v[8:11], v[192:199], v237\n"
  "buffer_store_dwordx4 v[216:219], v238, s[44:47], s12 offen offset:0 sc1\n"
  "v_exp_f32 v202, v170\n"
  "v_exp_f32 v203, v171\n"
  "v_exp_f32 v204, v172\n"
  "v_exp_f32 v205, v173\n"
  "v_exp_f32 v206, v174\n"
  "v_exp_f32 v207, v175\n"
  "v_mfma_f32_32x32x16_bf16 v[160:175], v[16:19], v[80:83], 0\n"
  "buffer_store_dwordx4 v[216:219], v238, s[44:47], s12 offen offset:1024 sc1\n"
  "v_exp_f32 v192, v176\n"
  "v_exp_f32 v193, v177\n"
  "v_exp_f32 v194, v178\n"
  "v_exp_f32 v195, v179\n"
  "v_mfma_f32_32x32x16_bf16 v[160:175], v[20:23], v[84:87], v[160:175]\n"
  "v_exp_f32 v196, v180\n"
  "v_exp_f32 v197, v181\n"
  "v_exp_f32 v198, v182\n"
  "v_smfmac_f32_32x32x32_bf16 v[144:159], v[12:15], v[200:207], v237\n"
  "v_exp_f32 v199, v183\n"
  "v_exp_f32 v200, v184\n"
  "v_exp_f32 v201, v185\n"
  "v_smfmac_f32_32x32x32_bf16 v[112:127], v[24:27], v[192:199], v237\n"
  "v_exp_f32 v202, v186\n"
  "v_exp_f32 v203, v187\n"
  "v_exp_f32 v204, v188\n"
  "v_exp_f32 v205, v189\n"
  "v_exp_f32 v206, v190\n"
  "v_exp_f32 v207, v191\n"
  "v_mfma_f32_32x32x16_bf16 v[176:191], v[16:19], v[88:91], 0\n"
  "v_exp_f32 v192, v160\n"
  "v_exp_f32 v193, v161\n"
  "v_exp_f32 v194, v162\n"
  "v_exp_f32 v195, v163\n"
  "v_mfma_f32_32x32x16_bf16 v[176:191], v[20:23], v[92:95], v[176:191]\n"
  "v_exp_f32 v196, v164\n"
  "v_exp_f32 v197, v165\n"
  "v_exp_f32 v198, v166\n"
  "v_smfmac_f32_32x32x32_bf16 v[112:127], v[28:31], v[200:207], v237\n"
  "v_exp_f32 v199, v167\n"
  "v_exp_f32 v200, v168\n"
  "v_exp_f32 v201, v169\n"
  "v_smfmac_f32_32x32x32_bf16 v[128:143], v[24:27], v[192:199], v237\n"
  "s_waitcnt vmcnt(9)\n"
  "v_cndmask_b32_e64 v38, v38, v242, s[4:5]\n"
  "v_exp_f32 v202, v170\n"
  "v_exp_f32 v203, v171\n"
  "v_exp_f32 v204, v172\n"
  "v_exp_f32 v205, v173\n"
  "v_exp_f32 v206, v174\n"
  "v_exp_f32 v207, v175\n"
  "v_mfma_f32_32x32x16_bf16 v[160:175], v[32:35], v[64:67], 0\n"
  "v_exp_f32 v192, v176\n"
  "v_exp_f32 v193, v177\n"
  "v_exp_f32 v194, v178\n"
  "v_exp_f32 v195, v179\n"
  "v_mfma_f32_32x32x16_bf16 v[160:175], v[36:39], v[68:71], v[160:175]\n"
  "v_exp_f32 v196, v180\n"
  "v_exp_f32 v197, v181\n"
  "v_exp_f32 v198, v182\n"
  "v_smfmac_f32_32x32x32_bf16 v[128:143], v[28:31], v[200:207], v237\n"
  "v_exp_f32 v199, v183\n"
  "v_exp_f32 v200, v184\n"
  "v_exp_f32 v201, v185\n"
  "v_smfmac_f32_32x32x32_bf16 v[144:159], v[24:27], v[192:199], v237\n"
  "v_exp_f32 v202, v186\n"
  "v_exp_f32 v203, v187\n"
  "v_exp_f32 v204, v188\n"
  "v_exp_f32 v205, v189\n"
  "v_exp_f32 v206, v190\n"
  "v_exp_f32 v207, v191\n"
  "v_mfma_f32_32x32x16_bf16 v[176:191], v[32:35], v[72:75], 0\n"
  "v_exp_f32 v192, v160\n"
  "v_exp_f32 v193, v161\n"
  "v_exp_f32 v194, v162\n"
  "v_exp_f32 v195, v163\n"
  "v_mfma_f32_32x32x16_bf16 v[176:191], v[36:39], v[76:79], v[176:191]\n"
  "v_exp_f32 v196, v164\n"
  "v_exp_f32 v197, v165\n"
  "v_exp_f32 v198, v166\n"
  "v_smfmac_f32_32x32x32_bf16 v[144:159], v[28:31], v[200:207], v237\n"
  "v_exp_f32 v199, v167\n"
  "v_exp_f32 v200, v168\n"
  "v_exp_f32 v201, v169\n"
  "v_smfmac_f32_32x32x32_bf16 v[96:111], v[40:43], v[192:199], v237\n"
  "s_add_u32 s30, s6, 57344\n"
  "s_addc_u32 s31, s7, 0\n"
  "s_waitcnt vmcnt(4)\n"
  "v_xor_b32 v254, v254, v252\n"
  "v_xor_b32 v255, v255, v253\n"
  "v_or_b32 v254, v254, v255\n"
  "v_cmp_ne_u32 vcc, 0, v254\n"
  "s_cbranch_vccz LDd%=\n"
  "s_mov_b32 s69, 0x40000\n"
  "LPd%=:\n"
  "s_sleep 1\n"
  "buffer_load_dwordx2 v[254:255], v232, s[44:47], s67 offen sc1\n"
  "s_waitcnt vmcnt(0)\n"
  "v_xor_b32 v254, v254, v252\n"
  "v_xor_b32 v255, v255, v253\n"
  "v_or_b32 v254, v254, v255\n"
  "v_cmp_ne_u32 vcc, 0, v254\n"
  "s_cbranch_vccz LDd%=\n"
  "s_sub_u32 s69, s69, 1\n"
  "s_cmp_lg_u32 s69, 0\n"
  "s_cbranch_scc1 LPd%=\n"
  "LDd%=:\n"
  "s_mov_b32 s67, 0x10000\n"
  "v_exp_f32 v202, v170\n"
  "v_exp_f32 v203, v171\n"
  "v_exp_f32 v204, v172\n"
  "v_exp_f32 v205, v173\n"
  "v_exp_f32 v206, v174\n"
  "v_exp_f32 v207, v175\n"
  "v_mfma_f32_32x32x16_bf16 v[160:175], v[32:35], v[80:83], 0\n"
  "global_load_dwordx4 v[0:3], v233, s[30:31] sc1\n"
  "v_exp_f32 v192, v176\n"
  "v_exp_f32 v193, v177\n"
  "v_exp_f32 v194, v178\n"
  "v_exp_f32 v195, v179\n"
  "v_mfma_f32_32x32x16_bf16 v[160:175], v[36:39], v[84:87], v[160:175]\n"
  "global_load_dwordx4 v[4:7], v234, s[30:31] sc1\n"
  "v_exp_f32 v196, v180\n"
  "v_exp_f32 v197, v181\n"
  "v_exp_f32 v198, v182\n"
  "v_smfmac_f32_32x32x32_bf16 v[96:111], v[44:47], v[200:207], v237\n"
  "s_mov_b64 exec, s[70:71]\n"
  "global_load_dwordx4 v[8:11], v235, s[30:31] sc1\n"
  "global_load_dwordx4 v[12:15], v236, s[30:31] sc1\n"
  "s_mov_b64 exec, -1\n"
  "s_add_u32 s30, s6, 286720\n"
  "s_addc_u32 s31, s7, 0\n"
  "v_exp_f32 v199, v183\n"
  "v_exp_f32 v200, v184\n"
  "v_exp_f32 v201, v185\n"
  "v_smfmac_f32_32x32x32_bf16 v[112:127], v[40:43], v[192:199], v237\n"
  "global_load_dwordx4 v[16:19], v233, s[30:31] sc1\n"
  "v_exp_f32 v202, v186\n"
  "v_exp_f32 v203, v187\n"
  "v_exp_f32 v204, v188\n"
  "v_exp_f32 v205, v189\n"
  "v_exp_f32 v206, v190\n"
  "v_exp_f32 v207, v191\n"
  "v_mfma_f32_32x32x16_bf16 v[176:191], v[32:35], v[88:91], 0\n"
  "global_load_dwordx4 v[20:23], v234, s[30:31] sc1\n"
  "v_exp_f32 v192, v160\n"
  "v_exp_f32 v193, v161\n"
  "v_exp_f32 v194, v162\n"
  "v_exp_f32 v195, v163\n"
  "v_mfma_f32_32x32x16_bf16 v[176:191], v[36:39], v[92:95], v[176:191]\n"
  "s_mov_b64 exec, s[70:71]\n"
  "global_load_dwordx4 v[24:27], v235, s[30:31] sc1\n"
  "global_load_dwordx4 v[28:31], v236, s[30:31] sc1\n"
  "s_mov_b64 exec, -1\n"
  "v_exp_f32 v196, v164\n"
  "v_exp_f32 v197, v165\n"
  "v_exp_f32 v198, v166\n"
  "v_smfmac_f32_32x32x32_bf16 v[112:127], v[44:47], v[200:207], v237\n"
  "s_add_u32 s30, s6, 516096\n"
  "s_addc_u32 s31, s7, 0\n"
  "v_exp_f32 v199, v167\n"
  "v_exp_f32 v200, v168\n"
  "v_exp_f32 v201, v169\n"
  "v_smfmac_f32_32x32x32_bf16 v[128:143], v[40:43], v[192:199], v237\n"
  "global_load_dwordx4 v[32:35], v233, s[30:31] sc1\n"
  "v_cndmask_b32_e64 v54, v54, v242, s[4:5]\n"
  "v_exp_f32 v202, v170\n"
  "v_exp_f32 v203, v171\n"
  "v_exp_f32 v204, v172\n"
  "v_exp_f32 v205, v173\n"
  "v_exp_f32 v206, v174\n"
  "v_exp_f32 v207, v175\n"
  "v_mfma_f32_32x32x16_bf16 v[160:175], v[48:51], v[64:67], 0\n"
  "global_load_dwordx4 v[36:39], v234, s[30:31] sc1\n"
  "v_exp_f32 v192, v176\n"
  "v_exp_f32 v193, v177\n"
  "v_exp_f32 v194, v178\n"
  "v_exp_f32 v195, v179\n"
  "v_mfma_f32_32x32x16_bf16 v[160:175], v[52:55], v[68:71], v[160:175]\n"
  "v_exp_f32 v196, v180\n"
  "v_exp_f32 v197, v181\n"
  "v_exp_f32 v198, v182\n"
  "v_smfmac_f32_32x32x32_bf16 v[128:143], v[44:47], v[200:207], v237\n"
  "v_exp_f32 v199, v183\n"
  "v_exp_f32 v200, v184\n"
  "v_exp_f32 v201, v185\n"
  "v_smfmac_f32_32x32x32_bf16 v[144:159], v[40:43], v[192:199], v237\n"
  "v_exp_f32 v202, v186\n"
  "v_exp_f32 v203, v187\n"
  "v_exp_f32 v204, v188\n"
  "v_exp_f32 v205, v189\n"
  "v_exp_f32 v206, v190\n"
  "v_exp_f32 v207, v191\n"
  "v_mfma_f32_32x32x16_bf16 v[176:191], v[48:51], v[72:75], 0\n"
  "s_waitcnt vmcnt(10)\n"
  "s_barrier\n"
  "v_exp_f32 v192, v160\n"
  "v_exp_f32 v193, v161\n"
  "v_exp_f32 v194, v162\n"
  "v_exp_f32 v195, v163\n"
  "v_mfma_f32_32x32x16_bf16 v[176:191], v[52:55], v[76:79], v[176:191]\n"
  "buffer_store_dwordx2 v[252:253], v244, s[44:47], s67 offen sc1\n"
  "v_exp_f32 v196, v164\n"
  "v_exp_f32 v197, v165\n"
  "v_exp_f32 v198, v166\n"
  "v_smfmac_f32_32x32x32_bf16 v[144:159], v[44:47], v[200:207], v237\n"
  "s_mov_b64 exec, s[70:71]\n"
  "global_load_dwordx4 v[40:43], v235, s[30:31] sc1\n"
  "global_load_dwordx4 v[44:47], v236, s[30:31] sc1\n"
  "s_mov_b64 exec, -1\n"
  "v_exp_f32 v199, v167\n"
  "v_exp_f32 v200, v168\n"
  "v_exp_f32 v201, v169\n"
  "v_smfmac_f32_32x32x32_bf16 v[96:111], v[56:59], v[192:199], v237\n"
  "v_exp_f32 v202, v170\n"
  "v_exp_f32 v203, v171\n"
  "v_exp_f32 v204, v172\n"
  "v_exp_f32 v205, v173\n"
  "v_exp_f32 v206, v174\n"
  "v_exp_f32 v207, v175\n"
  "v_mfma_f32_32x32x16_bf16 v[160:175], v[48:51], v[80:83], 0\n"
  "v_exp_f32 v192, v176\n"
  "v_exp_f32 v193, v177\n"
  "v_exp_f32 v194, v178\n"
  "v_exp_f32 v195, v179\n"
  "v_mfma_f32_32x32x16_bf16 v[160:175], v[52:55], v[84:87], v[160:175]\n"
  "v_exp_f32 v196, v180\n"
  "v_exp_f32 v197, v181\n"
  "v_exp_f32 v198, v182\n"
  "v_smfmac_f32_32x32x32_bf16 v[96:111], v[60:63], v[200:207], v237\n"
  "v_exp_f32 v199, v183\n"
  "v_exp_f32 v200, v184\n"
  "v_exp_f32 v201, v185\n"
  "v_smfmac_f32_32x32x32_bf16 v[112:127], v[56:59], v[192:199], v237\n"
  "v_exp_f32 v202, v186\n"
  "v_exp_f32 v203, v187\n"
  "v_exp_f32 v204, v188\n"
  "v_exp_f32 v205, v189\n"
  "v_exp_f32 v206, v190\n"
  "v_exp_f32 v207, v191\n"
  "v_mfma_f32_32x32x16_bf16 v[176:191], v[48:51], v[88:91], 0\n"
  "v_exp_f32 v192, v160\n"
  "v_exp_f32 v193, v161\n"
  "v_exp_f32 v194, v162\n"
  "v_exp_f32 v195, v163\n"
  "v_mfma_f32_32x32x16_bf16 v[176:191], v[52:55], v[92:95], v[176:191]\n"
  "v_exp_f32 v196, v164\n"
  "v_exp_f32 v197, v165\n"
  "v_exp_f32 v198, v166\n"
  "v_smfmac_f32_32x32x32_bf16 v[112:127], v[60:63], v[200:207], v237\n"
  "s_add_u32 s30, s6, 745472\n"
  "s_addc_u32 s31, s7, 0\n"
  "v_exp_f32 v199, v167\n"
  "v_exp_f32 v200, v168\n"
  "v_exp_f32 v201, v169\n"
  "v_smfmac_f32_32x32x32_bf16 v[128:143], v[56:59], v[192:199], v237\n"
  "global_load_dwordx4 v[48:51], v233, s[30:31] sc1\n"
  "s_waitcnt vmcnt(10)\n"
  "v_cndmask_b32_e64 v6, v6, v242, s[4:5]\n"
  "v_exp_f32 v202, v170\n"
  "v_exp_f32 v203, v171\n"
  "v_exp_f32 v204, v172\n"
  "v_exp_f32 v205, v173\n"
  "v_exp_f32 v206, v174\n"
  "v_exp_f32 v207, v175\n"
  "v_mfma_f32_32x32x16_bf16 v[160:175], v[0:3], v[64:67], 0\n"
  "global_load_dwordx4 v[52:55], v234, s[30:31] sc1\n"
  "v_exp_f32 v192, v176\n"
  "v_exp_f32 v193, v177\n"
  "v_exp_f32 v194, v178\n"
  "v_exp_f32 v195, v179\n"
  "v_mfma_f32_32x32x16_bf16 v[160:175], v[4:7], v[68:71], v[160:175]\n"
  "v_exp_f32 v196, v180\n"
  "v_exp_f32 v197, v181\n"
  "v_exp_f32 v198, v182\n"
  "v_smfmac_f32_32x32x32_bf16 v[128:143], v[60:63], v[200:207], v237\n"
  "v_exp_f32 v199, v183\n"
  "v_exp_f32 v200, v184\n"
  "v_exp_f32 v201, v185\n"
  "v_smfmac_f32_32x32x32_bf16 v[144:159], v[56:59], v[192:199], v237\n"
  "v_exp_f32 v202, v186\n"
  "v_exp_f32 v203, v187\n"
  "v_exp_f32 v204, v188\n"
  "v_exp_f32 v205, v189\n"
  "v_exp_f32 v206, v190\n"
  "v_exp_f32 v207, v191\n"
  "v_mfma_f32_32x32x16_bf16 v[176:191], v[0:3], v[72:75], 0\n"
  "v_exp_f32 v192, v160\n"
  "v_exp_f32 v193, v161\n"
  "v_exp_f32 v194, v162\n"
  "v_exp_f32 v195, v163\n"
  "v_mfma_f32_32x32x16_bf16 v[176:191], v[4:7], v[76:79], v[176:191]\n"
  "v_exp_f32 v196, v164\n"
  "v_exp_f32 v197, v165\n"
  "v_exp_f32 v198, v166\n"
  "v_smfmac_f32_32x32x32_bf16 v[144:159], v[60:63], v[200:207], v237\n"
  "s_mov_b64 exec, s[70:71]\n"
  "global_load_dwordx4 v[56:59], v235, s[30:31] sc1\n"
  "global_load_dwordx4 v[60:63], v236, s[30:31] sc1\n"
  "s_mov_b64 exec, -1\n"
  "v_exp_f32 v199, v167\n"
  "v_exp_f32 v200, v168\n"
  "v_exp_f32 v201, v169\n"
  "v_smfmac_f32_32x32x32_bf16 v[96:111], v[8:11], v[192:199], v237\n"
  "v_exp_f32 v202, v170\n"
  "v_exp_f32 v203, v171\n"
  "v_exp_f32 v204, v172\n"
  "v_exp_f32 v205, v173\n"
  "v_exp_f32 v206, v174\n"
  "v_exp_f32 v207, v175\n"
  "v_mfma_f32_32x32x16_bf16 v[160:175], v[0:3], v[80:83], 0\n"
  "v_exp_f32 v192, v176\n"
  "v_exp_f32 v193, v177\n"
  "v_exp_f32 v194, v178\n"
  "v_exp_f32 v195, v179\n"
  "v_mfma_f32_32x32x16_bf16 v[160:175], v[4:7], v[84:87], v[160:175]\n"
  "v_exp_f32 v196, v180\n"
  "v_exp_f32 v197, v181\n"
  "v_exp_f32 v198, v182\n"
  "v_smfmac_f32_32x32x32_bf16 v[96:111], v[12:15], v[200:207], v237\n"
  "s_add_u32 s58, s48, 0\n"
  "s_addc_u32 s59, s49, 0\n"
  "v_exp_f32 v199, v183\n"
  "v_exp_f32 v200, v184\n"
  "v_exp_f32 v201, v185\n"
  "v_smfmac_f32_32x32x32_bf16 v[112:127], v[8:11], v[192:199], v237\n"
  "global_load_dwordx4 v[208:211], v239, s[58:59]\n"
  "v_exp_f32 v202, v186\n"
  "v_exp_f32 v203, v187\n"
  "v_exp_f32 v204, v188\n"
  "v_exp_f32 v205, v189\n"
  "v_exp_f32 v206, v190\n"
  "v_exp_f32 v207, v191\n"
  "v_mfma_f32_32x32x16_bf16 v[176:191], v[0:3], v[88:91], 0\n"
  "v_exp_f32 v192, v160\n"
  "v_exp_f32 v193, v161\n"
  "v_exp_f32 v194, v162\n"
  "v_exp_f32 v195, v163\n"
  "v_mfma_f32_32x32x16_bf16 v[176:191], v[4:7], v[92:95], v[176:191]\n"
  "v_exp_f32 v196, v164\n"
  "v_exp_f32 v197, v165\n"
  "v_exp_f32 v198, v166\n"
  "v_smfmac_f32_32x32x32_bf16 v[112:127], v[12:15], v[200:207], v237\n"
  "s_add_u32 s58, s48, 28672\n"
  "s_addc_u32 s59, s49, 0\n"
  "v_exp_f32 v199, v167\n"
  "v_exp_f32 v200, v168\n"
  "v_exp_f32 v201, v169\n"
  "v_smfmac_f32_32x32x32_bf16 v[128:143], v[8:11], v[192:199], v237\n"
  "global_load_dwordx4 v[212:215], v239, s[58:59]\n"
  "s_waitcnt vmcnt(11)\n"
  "v_cndmask_b32_e64 v22, v22, v242, s[4:5]\n"
  "v_exp_f32 v202, v170\n"
  "v_exp_f32 v203, v171\n"
  "v_exp_f32 v204, v172\n"
  "v_exp_f32 v205, v173\n"
  "v_exp_f32 v206, v174\n"
  "v_exp_f32 v207, v175\n"
  "v_mfma_f32_32x32x16_bf16 v[160:175], v[16:19], v[64:67], 0\n"
  "v_exp_f32 v192, v176\n"
  "v_exp_f32 v193, v177\n"
  "v_exp_f32 v194, v178\n"
  "v_exp_f32 v195, v179\n"
  "v_mfma_f32_32x32x16_bf16 v[160:175], v[20:23], v[68:71], v[160:175]\n"
  "v_exp_f32 v196, v180\n"
  "v_exp_f32 v197, v181\n"
  "v_exp_f32 v198, v182\n"
  "v_smfmac_f32_32x32x32_bf16 v[128:143], v[12:15], v[200:207], v237\n"
  "s_add_u32 s58, s48, 57344\n"
  "s_addc_u32 s59, s49, 0\n"
  "v_exp_f32 v199, v183\n"
  "v_exp_f32 v200, v184\n"
  "v_exp_f32 v201, v185\n"
  "v_smfmac_f32_32x32x32_bf16 v[144:159], v[8:11], v[192:199], v237\n"
  "global_load_dwordx4 v[216:219], v239, s[58:59]\n"
  "v_exp_f32 v202, v186\n"
  "v_exp_f32 v203, v187\n"
  "v_exp_f32 v204, v188\n"
  "v_exp_f32 v205, v189\n"
  "v_exp_f32 v206, v190\n"
  "v_exp_f32 v207, v191\n"
  "v_mfma_f32_32x32x16_bf16 v[176:191], v[16:19], v[72:75], 0\n"
  "v_exp_f32 v192, v160\n"
  "v_exp_f32 v193, v161\n"
  "v_exp_f32 v194, v162\n"
  "v_exp_f32 v195, v163\n"
  "v_mfma_f32_32x32x16_bf16 v[176:191], v[20:23], v[76:79], v[176:191]\n"
  "buffer_load_dwordx2 v[254:255], v232, s[44:47], s67 offen sc1\n"
  "v_exp_f32 v196, v164\n"
  "v_exp_f32 v197, v165\n"
  "v_exp_f32 v198, v166\n"
  "v_smfmac_f32_32x32x32_bf16 v[144:159], v[12:15], v[200:207], v237\n"
  "v_exp_f32 v199, v167\n"
  "v_exp_f32 v200, v168\n"
  "v_exp_f32 v201, v169\n"
  "v_smfmac_f32_32x32x32_bf16 v[96:111], v[24:27], v[192:199], v237\n"
  "v_exp_f32 v202, v170\n"
  "v_exp_f32 v203, v171\n"
  "v_exp_f32 v204, v172\n"
  "v_exp_f32 v205, v173\n"
  "v_exp_f32 v206, v174\n"
  "v_exp_f32 v207, v175\n"
  "v_mfma_f32_32x32x16_bf16 v[160:175], v[16:19], v[80:83], 0\n"
  "v_exp_f32 v192, v176\n"
  "v_exp_f32 v193, v177\n"
  "v_exp_f32 v194, v178\n"
  "v_exp_f32 v195, v179\n"
  "v_mfma_f32_32x32x16_bf16 v[160:175], v[20:23], v[84:87], v[160:175]\n"
  "v_exp_f32 v196, v180\n"
  "v_exp_f32 v197, v181\n"
  "v_exp_f32 v198, v182\n"
  "v_smfmac_f32_32x32x32_bf16 v[96:111], v[28:31], v[200:207], v237\n"
  "s_add_u32 s58, s48, 86016\n"
  "s_addc_u32 s59, s49, 0\n"
  "v_exp_f32 v199, v183\n"
  "v_exp_f32 v200, v184\n"
  "v_exp_f32 v201, v185\n"
  "v_smfmac_f32_32x32x32_bf16 v[112:127], v[24:27], v[192:199], v237\n"
  "global_load_dwordx4 v[220:223], v239, s[58:59]\n"
  "v_exp_f32 v202, v186\n"
  "v_exp_f32 v203, v187\n"
  "v_exp_f32 v204, v188\n"
  "v_exp_f32 v205, v189\n"
  "v_exp_f32 v206, v190\n"
  "v_exp_f32 v207, v191\n"
  "v_mfma_f32_32x32x16_bf16 v[176:191], v[16:19], v[88:91], 0\n"
  "v_exp_f32 v192, v160\n"
  "v_exp_f32 v193, v161\n"
  "v_exp_f32 v194, v162\n"
  "v_exp_f32 v195, v163\n"
  "v_mfma_f32_32x32x16_bf16 v[176:191], v[20:23], v[92:95], v[176:191]\n"
  "v_exp_f32 v196, v164\n"
  "v_exp_f32 v197, v165\n"
  "v_exp_f32 v198, v166\n"
  "v_smfmac_f32_32x32x32_bf16 v[112:127], v[28:31], v[200:207], v237\n"
  "s_add_u32 s58, s48, 114688\n"
  "s_addc_u32 s59, s49, 0\n"
  "v_exp_f32 v199, v167\n"
  "v_exp_f32 v200, v168\n"
  "v_exp_f32 v201, v169\n"
  "v_smfmac_f32_32x32x32_bf16 v[128:143], v[24:27], v[192:199], v237\n"
  "global_load_dwordx4 v[224:227], v239, s[58:59]\n"
  "s_waitcnt vmcnt(10)\n"
  "v_cndmask_b32_e64 v38, v38, v242, s[4:5]\n"
  "v_exp_f32 v202, v170\n"
  "v_exp_f32 v203, v171\n"
  "v_exp_f32 v204, v172\n"
  "v_exp_f32 v205, v173\n"
  "v_exp_f32 v206, v174\n"
  "v_exp_f32 v207, v175\n"
  "v_mfma_f32_32x32x16_bf16 v[160:175], v[32:35], v[64:67], 0\n"
  "v_exp_f32 v192, v176\n"
  "v_exp_f32 v193, v177\n"
  "v_exp_f32 v194, v178\n"
  "v_exp_f32 v195, v179\n"
  "v_mfma_f32_32x32x16_bf16 v[160:175], v[36:39], v[68:71], v[160:175]\n"
  "v_exp_f32 v196, v180\n"
  "v_exp_f32 v197, v181\n"
  "v_exp_f32 v198, v182\n"
  "v_smfmac_f32_32x32x32_bf16 v[128:143], v[28:31], v[200:207], v237\n"
  "s_add_u32 s58, s48, 143360\n"
  "s_addc_u32 s59, s49, 0\n"
  "v_exp_f32 v199, v183\n"
  "v_exp_f32 v200, v184\n"
  "v_exp_f32 v201, v185\n"
  "v_smfmac_f32_32x32x32_bf16 v[144:159], v[24:27], v[192:199], v237\n"
  "global_load_dwordx4 v[228:231], v239, s[58:59]\n"
  "v_exp_f32 v202, v186\n"
  "v_exp_f32 v203, v187\n"
  "v_exp_f32 v204, v188\n"
  "v_exp_f32 v205, v189\n"
  "v_exp_f32 v206, v190\n"
  "v_exp_f32 v207, v191\n"
  "v_mfma_f32_32x32x16_bf16 v[176:191], v[32:35], v[72:75], 0\n"
  "v_exp_f32 v192, v160\n"
  "v_exp_f32 v193, v161\n"
  "v_exp_f32 v194, v162\n"
  "v_exp_f32 v195, v163\n"
  "v_mfma_f32_32x32x16_bf16 v[176:191], v[36:39], v[76:79], v[176:191]\n"
  "v_exp_f32 v196, v164\n"
  "v_exp_f32 v197, v165\n"
  "v_exp_f32 v198, v166\n"
  "v_smfmac_f32_32x32x32_bf16 v[144:159], v[28:31], v[200:207], v237\n"
  "v_exp_f32 v199, v167\n"
  "v_exp_f32 v200, v168\n"
  "v_exp_f32 v201, v169\n"
  "v_smfmac_f32_32x32x32_bf16 v[96:111], v[40:43], v[192:199], v237\n"
  "v_exp_f32 v202, v170\n"
  "v_exp_f32 v203, v171\n"
  "v_exp_f32 v204, v172\n"
  "v_exp_f32 v205, v173\n"
  "v_exp_f32 v206, v174\n"
  "v_exp_f32 v207, v175\n"
  "v_mfma_f32_32x32x16_bf16 v[160:175], v[32:35], v[80:83], 0\n"
  "v_exp_f32 v192, v176\n"
  "v_exp_f32 v193, v177\n"
  "v_exp_f32 v194, v178\n"
  "v_exp_f32 v195, v179\n"
  "v_mfma_f32_32x32x16_bf16 v[160:175], v[36:39], v[84:87], v[160:175]\n"
  "v_exp_f32 v196, v180\n"
  "v_exp_f32 v197, v181\n"
  "v_exp_f32 v198, v182\n"
  "v_smfmac_f32_32x32x32_bf16 v[96:111], v[44:47], v[200:207], v237\n"
  "v_exp_f32 v199, v183\n"
  "v_exp_f32 v200, v184\n"
  "v_exp_f32 v201, v185\n"
  "v_smfmac_f32_32x32x32_bf16 v[112:127], v[40:43], v[192:199], v237\n"
  "s_add_u32 s30, s6, 114688\n"
  "s_addc_u32 s31, s7, 0\n"
  "s_waitcnt vmcnt(3)\n"
  "v_xor_b32 v254, v254, v252\n"
  "v_xor_b32 v255, v255, v253\n"
  "v_or_b32 v254, v254, v255\n"
  "v_cmp_ne_u32 vcc, 0, v254\n"
  "s_cbranch_vccz LDb%=\n"
  "s_mov_b32 s69, 0x40000\n"
  "LPb%=:\n"
  "s_sleep 1\n"
  "buffer_load_dwordx2 v[254:255], v232, s[44:47], s67 offen sc1\n"
  "s_waitcnt vmcnt(0)\n"
  "v_xor_b32 v254, v254, v252\n"
  "v_xor_b32 v255, v255, v253\n"
  "v_or_b32 v254, v254, v255\n"
  "v_cmp_ne_u32 vcc, 0, v254\n"
  "s_cbranch_vccz LDb%=\n"
  "s_sub_u32 s69, s69, 1\n"
  "s_cmp_lg_u32 s69, 0\n"
  "s_cbranch_scc1 LPb%=\n"
  "LDb%=:\n"
  "v_exp_f32 v202, v186\n"
  "v_exp_f32 v203, v187\n"
  "v_exp_f32 v204, v188\n"
  "v_exp_f32 v205, v189\n"
  "v_exp_f32 v206, v190\n"
  "v_exp_f32 v207, v191\n"
  "v_mfma_f32_32x32x16_bf16 v[176:191], v[32:35], v[88:91], 0\n"
  "global_load_dwordx4 v[0:3], v233, s[30:31] sc1\n"
  "v_exp_f32 v192, v160\n"
  "v_exp_f32 v193, v161\n"
  "v_exp_f32 v194, v162\n"
  "v_exp_f32 v195, v163\n"
  "v_mfma_f32_32x32x16_bf16 v[176:191], v[36:39], v[92:95], v[176:191]\n"
  "global_load_dwordx4 v[4:7], v234, s[30:31] sc1\n"
  "v_exp_f32 v196, v164\n"
  "v_exp_f32 v197, v165\n"
  "v_exp_f32 v198, v166\n"
  "v_smfmac_f32_32x32x32_bf16 v[112:127], v[44:47], v[200:207], v237\n"
  "s_mov_b64 exec, s[70:71]\n"
  "global_load_dwordx4 v[8:11], v235, s[30:31] sc1\n"
  "global_load_dwordx4 v[12:15], v236, s[30:31] sc1\n"
  "s_mov_b64 exec, -1\n"
  "s_add_u32 s30, s6, 172032\n"
  "s_addc_u32 s31, s7, 0\n"
  "v_exp_f32 v199, v167\n"
  "v_exp_f32 v200, v168\n"
  "v_exp_f32 v201, v169\n"
  "v_smfmac_f32_32x32x32_bf16 v[128:143], v[40:43], v[192:199], v237\n"
  "global_load_dwordx4 v[16:19], v233, s[30:31] sc1\n"
  "v_cndmask_b32_e64 v54, v54, v242, s[4:5]\n"
  "v_exp_f32 v202, v170\n"
  "v_exp_f32 v203, v171\n"
  "v_exp_f32 v204, v172\n"
  "v_exp_f32 v205, v173\n"
  "v_exp_f32 v206, v174\n"
  "v_exp_f32 v207, v175\n"
  "v_mfma_f32_32x32x16_bf16 v[160:175], v[48:51], v[64:67], 0\n"
  "global_load_dwordx4 v[20:23], v234, s[30:31] sc1\n"
  "v_exp_f32 v192, v176\n"
  "v_exp_f32 v193, v177\n"
  "v_exp_f32 v194, v178\n"
  "v_exp_f32 v195, v179\n"
  "v_mfma_f32_32x32x16_bf16 v[160:175], v[52:55], v[68:71], v[160:175]\n"
  "s_mov_b64 exec, s[70:71]\n"
  "global_load_dwordx4 v[24:27], v235, s[30:31] sc1\n"
  "global_load_dwordx4 v[28:31], v236, s[30:31] sc1\n"
  "s_mov_b64 exec, -1\n"
  "s_add_u32 s30, s6, 344064\n"
  "s_addc_u32 s31, s7, 0\n"
  "v_exp_f32 v196, v180\n"
  "v_exp_f32 v197, v181\n"
  "v_exp_f32 v198, v182\n"
  "v_smfmac_f32_32x32x32_bf16 v[128:143], v[44:47], v[200:207], v237\n"
  "global_load_dwordx4 v[32:35], v233, s[30:31] sc1\n"
  "v_exp_f32 v199, v183\n"
  "v_exp_f32 v200, v184\n"
  "v_exp_f32 v201, v185\n"
  "v_smfmac_f32_32x32x32_bf16 v[144:159], v[40:43], v[192:199], v237\n"
  "global_load_dwordx4 v[36:39], v234, s[30:31] sc1\n"
  "v_exp_f32 v202, v186\n"
  "v_exp_f32 v203, v187\n"
  "v_exp_f32 v204, v188\n"
  "v_exp_f32 v205, v189\n"
  "v_exp_f32 v206, v190\n"
  "v_exp_f32 v207, v191\n"
  "v_mfma_f32_32x32x16_bf16 v[176:191], v[48:51], v[72:75], 0\n"
  "v_exp_f32 v192, v160\n"
  "v_exp_f32 v193, v161\n"
  "v_exp_f32 v194, v162\n"
  "v_exp_f32 v195, v163\n"
  "v_mfma_f32_32x32x16_bf16 v[176:191], v[52:55], v[76:79], v[176:191]\n"
  "v_exp_f32 v196, v164\n"
  "v_exp_f32 v197, v165\n"
  "v_exp_f32 v198, v166\n"
  "v_smfmac_f32_32x32x32_bf16 v[144:159], v[44:47], v[200:207], v237\n"
  "s_mov_b64 exec, s[70:71]\n"
  "global_load_dwordx4 v[40:43], v235, s[30:31] sc1\n"
  "global_load_dwordx4 v[44:47], v236, s[30:31] sc1\n"
  "s_mov_b64 exec, -1\n"
  "v_exp_f32 v199, v167\n"
  "v_exp_f32 v200, v168\n"
  "v_exp_f32 v201, v169\n"
  "v_smfmac_f32_32x32x32_bf16 v[96:111], v[56:59], v[192:199], v237\n"
  "v_exp_f32 v202, v170\n"
  "v_exp_f32 v203, v171\n"
  "v_exp_f32 v204, v172\n"
  "v_exp_f32 v205, v173\n"
  "v_exp_f32 v206, v174\n"
  "v_exp_f32 v207, v175\n"
  "v_mfma_f32_32x32x16_bf16 v[160:175], v[48:51], v[80:83], 0\n"
  "v_exp_f32 v192, v176\n"
  "v_exp_f32 v193, v177\n"
  "v_exp_f32 v194, v178\n"
  "v_exp_f32 v195, v179\n"
  "v_mfma_f32_32x32x16_bf16 v[160:175], v[52:55], v[84:87], v[160:175]\n"
  "v_exp_f32 v196, v180\n"
  "v_exp_f32 v197, v181\n"
  "v_exp_f32 v198, v182\n"
  "v_smfmac_f32_32x32x32_bf16 v[96:111], v[60:63], v[200:207], v237\n"
  "v_exp_f32 v199, v183\n"
  "v_exp_f32 v200, v184\n"
  "v_exp_f32 v201, v185\n"
  "v_smfmac_f32_32x32x32_bf16 v[112:127], v[56:59], v[192:199], v237\n"
  "v_exp_f32 v202, v186\n"
  "v_exp_f32 v203, v187\n"
  "v_exp_f32 v204, v188\n"
  "v_exp_f32 v205, v189\n"
  "v_exp_f32 v206, v190\n"
  "v_exp_f32 v207, v191\n"
  "v_mfma_f32_32x32x16_bf16 v[176:191], v[48:51], v[88:91], 0\n"
  "v_exp_f32 v192, v160\n"
  "v_exp_f32 v193, v161\n"
  "v_exp_f32 v194, v162\n"
  "v_exp_f32 v195, v163\n"
  "v_mfma_f32_32x32x16_bf16 v[176:191], v[52:55], v[92:95], v[176:191]\n"
  "v_exp_f32 v196, v164\n"
  "v_exp_f32 v197, v165\n"
  "v_exp_f32 v198, v166\n"
  "v_smfmac_f32_32x32x32_bf16 v[112:127], v[60:63], v[200:207], v237\n"
  "s_add_u32 s30, s6, 401408\n"
  "s_addc_u32 s31, s7, 0\n"
  "v_exp_f32 v199, v167\n"
  "v_exp_f32 v200, v168\n"
  "v_exp_f32 v201, v169\n"
  "v_smfmac_f32_32x32x32_bf16 v[128:143], v[56:59], v[192:199], v237\n"
  "global_load_dwordx4 v[48:51], v233, s[30:31] sc1\n"
  "s_waitcnt vmcnt(9)\n"
  "v_cndmask_b32_e64 v6, v6, v242, s[4:5]\n"
  "v_exp_f32 v202, v170\n"
  "v_exp_f32 v203, v171\n"
  "v_exp_f32 v204, v172\n"
  "v_exp_f32 v205, v173\n"
  "v_exp_f32 v206, v174\n"
  "v_exp_f32 v207, v175\n"
  "v_mfma_f32_32x32x16_bf16 v[160:175], v[0:3], v[64:67], 0\n"
  "global_load_dwordx4 v[52:55], v234, s[30:31] sc1\n"
  "v_exp_f32 v192, v176\n"
  "v_exp_f32 v193, v177\n"
  "v_exp_f32 v194, v178\n"
  "v_exp_f32 v195, v179\n"
  "v_mfma_f32_32x32x16_bf16 v[160:175], v[4:7], v[68:71], v[160:175]\n"
  "v_exp_f32 v196, v180\n"
  "v_exp_f32 v197, v181\n"
  "v_exp_f32 v198, v182\n"
  "v_smfmac_f32_32x32x32_bf16 v[128:143], v[60:63], v[200:207], v237\n"
  "v_exp_f32 v199, v183\n"
  "v_exp_f32 v200, v184\n"
  "v_exp_f32 v201, v185\n"
  "v_smfmac_f32_32x32x32_bf16 v[144:159], v[56:59], v[192:199], v237\n"
  "v_exp_f32 v202, v186\n"
  "v_exp_f32 v203, v187\n"
  "v_exp_f32 v204, v188\n"
  "v_exp_f32 v205, v189\n"
  "v_exp_f32 v206, v190\n"
  "v_exp_f32 v207, v191\n"
  "v_mfma_f32_32x32x16_bf16 v[176:191], v[0:3], v[72:75], 0\n"
  "v_exp_f32 v192, v160\n"
  "v_exp_f32 v193, v161\n"
  "v_exp_f32 v194, v162\n"
  "v_exp_f32 v195, v163\n"
  "v_mfma_f32_32x32x16_bf16 v[176:191], v[4:7], v[76:79], v[176:191]\n"
  "v_exp_f32 v196, v164\n"
  "v_exp_f32 v197, v165\n"
  "v_exp_f32 v198, v166\n"
  "v_smfmac_f32_32x32x32_bf16 v[144:159], v[60:63], v[200:207], v237\n"
  "s_mov_b64 exec, s[70:71]\n"
  "global_load_dwordx4 v[56:59], v235, s[30:31] sc1\n"
  "global_load_dwordx4 v[60:63], v236, s[30:31] sc1\n"
  "s_mov_b64 exec, -1\n"
  "v_exp_f32 v199, v167\n"
  "v_exp_f32 v200, v168\n"
  "v_exp_f32 v201, v169\n"
  "v_smfmac_f32_32x32x32_bf16 v[96:111], v[8:11], v[192:199], v237\n"
  "v_exp_f32 v202, v170\n"
  "v_exp_f32 v203, v171\n"
  "v_exp_f32 v204, v172\n"
  "v_exp_f32 v205, v173\n"
  "v_exp_f32 v206, v174\n"
  "v_exp_f32 v207, v175\n"
  "v_mfma_f32_32x32x16_bf16 v[160:175], v[0:3], v[80:83], 0\n"
  "v_exp_f32 v192, v176\n"
  "v_exp_f32 v193, v177\n"
  "v_exp_f32 v194, v178\n"
  "v_exp_f32 v195, v179\n"
  "v_mfma_f32_32x32x16_bf16 v[160:175], v[4:7], v[84:87], v[160:175]\n"
  "s_add_u32 m0, s68, 49152\n"
  "v_exp_f32 v196, v180\n"
  "v_exp_f32 v197, v181\n"
  "v_exp_f32 v198, v182\n"
  "v_smfmac_f32_32x32x32_bf16 v[96:111], v[12:15], v[200:207], v237\n"
  "s_add_u32 s58, s48, 172032\n"
  "s_addc_u32 s59, s49, 0\n"
  "v_exp_f32 v199, v183\n"
  "v_exp_f32 v200, v184\n"
  "v_exp_f32 v201, v185\n"
  "v_smfmac_f32_32x32x32_bf16 v[112:127], v[8:11], v[192:199], v237\n"
  "global_load_lds_dwordx4 v239, s[58:59]\n"
  "v_exp_f32 v202, v186\n"
  "v_exp_f32 v203, v187\n"
  "v_exp_f32 v204, v188\n"
  "v_exp_f32 v205, v189\n"
  "v_exp_f32 v206, v190\n"
  "v_exp_f32 v207, v191\n"
  "v_mfma_f32_32x32x16_bf16 v[176:191], v[0:3], v[88:91], 0\n"
  "v_exp_f32 v192, v160\n"
  "v_exp_f32 v193, v161\n"
  "v_exp_f32 v194, v162\n"
  "v_exp_f32 v195, v163\n"
  "v_mfma_f32_32x32x16_bf16 v[176:191], v[4:7], v[92:95], v[176:191]\n"
  "v_exp_f32 v196, v164\n"
  "v_exp_f32 v197, v165\n"
  "v_exp_f32 v198, v166\n"
  "v_smfmac_f32_32x32x32_bf16 v[112:127], v[12:15], v[200:207], v237\n"
  "s_add_u32 s30, s6, 573440\n"
  "s_addc_u32 s31, s7, 0\n"
  "v_exp_f32 v199, v167\n"
  "v_exp_f32 v200, v168\n"
  "v_exp_f32 v201, v169\n"
  "v_smfmac_f32_32x32x32_bf16 v[128:143], v[8:11], v[192:199], v237\n"
  "global_load_dwordx4 v[0:3], v233, s[30:31] sc1\n"
  "s_waitcnt vmcnt(10)\n"
  "v_cndmask_b32_e64 v22, v22, v242, s[4:5]\n"
  "v_exp_f32 v202, v170\n"
  "v_exp_f32 v203, v171\n"
  "v_exp_f32 v204, v172\n"
  "v_exp_f32 v205, v173\n"
  "v_exp_f32 v206, v174\n"
  "v_exp_f32 v207, v175\n"
  "v_mfma_f32_32x32x16_bf16 v[160:175], v[16:19], v[64:67], 0\n"
  "global_load_dwordx4 v[4:7], v234, s[30:31] sc1\n"
  "v_exp_f32 v192, v176\n"
  "v_exp_f32 v193, v177\n"
  "v_exp_f32 v194, v178\n"
  "v_exp_f32 v195, v179\n"
  "v_mfma_f32_32x32x16_bf16 v[160:175], v[20:23], v[68:71], v[160:175]\n"
  "v_exp_f32 v196, v180\n"
  "v_exp_f32 v197, v181\n"
  "v_exp_f32 v198, v182\n"
  "v_smfmac_f32_32x32x32_bf16 v[128:143], v[12:15], v[200:207], v237\n"
  "v_exp_f32 v199, v183\n"
  "v_exp_f32 v200, v184\n"
  "v_exp_f32 v201, v185\n"
  "v_smfmac_f32_32x32x32_bf16 v[144:159], v[8:11], v[192:199], v237\n"
  "v_exp_f32 v202, v186\n"
  "v_exp_f32 v203, v187\n"
  "v_exp_f32 v204, v188\n"
  "v_exp_f32 v205, v189\n"
  "v_exp_f32 v206, v190\n"
  "v_exp_f32 v207, v191\n"
  "v_mfma_f32_32x32x16_bf16 v[176:191], v[16:19], v[72:75], 0\n"
  "v_exp_f32 v192, v160\n"
  "v_exp_f32 v193, v161\n"
  "v_exp_f32 v194, v162\n"
  "v_exp_f32 v195, v163\n"
  "v_mfma_f32_32x32x16_bf16 v[176:191], v[20:23], v[76:79], v[176:191]\n"
  "v_exp_f32 v196, v164\n"
  "v_exp_f32 v197, v165\n"
  "v_exp_f32 v198, v166\n"
  "v_smfmac_f32_32x32x32_bf16 v[144:159], v[12:15], v[200:207], v237\n"
  "s_mov_b64 exec, s[70:71]\n"
  "global_load_dwordx4 v[8:11], v235, s[30:31] sc1\n"
  "global_load_dwordx4 v[12:15], v236, s[30:31] sc1\n"
  "s_mov_b64 exec, -1\n"
  "v_exp_f32 v199, v167\n"
  "v_exp_f32 v200, v168\n"
  "v_exp_f32 v201, v169\n"
  "v_smfmac_f32_32x32x32_bf16 v[96:111], v[24:27], v[192:199], v237\n"
  "v_exp_f32 v202, v170\n"
  "v_exp_f32 v203, v171\n"
  "v_exp_f32 v204, v172\n"
  "v_exp_f32 v205, v173\n"
  "v_exp_f32 v206, v174\n"
  "v_exp_f32 v207, v175\n"
  "v_mfma_f32_32x32x16_bf16 v[160:175], v[16:19], v[80:83], 0\n"
  "v_exp_f32 v192, v176\n"
  "v_exp_f32 v193, v177\n"
  "v_exp_f32 v194, v178\n"
  "v_exp_f32 v195, v179\n"
  "v_mfma_f32_32x32x16_bf16 v[160:175], v[20:23], v[84:87], v[160:175]\n"
  "s_add_u32 m0, s68, 53248\n"
  "v_exp_f32 v196, v180\n"
  "v_exp_f32 v197, v181\n"
  "v_exp_f32 v198, v182\n"
  "v_smfmac_f32_32x32x32_bf16 v[96:111], v[28:31], v[200:207], v237\n"
  "s_add_u32 s58, s48, 200704\n"
  "s_addc_u32 s59, s49, 0\n"
  "v_exp_f32 v199, v183\n"
  "v_exp_f32 v200, v184\n"
  "v_exp_f32 v201, v185\n"
  "v_smfmac_f32_32x32x32_bf16 v[112:127], v[24:27], v[192:199], v237\n"
  "global_load_lds_dwordx4 v239, s[58:59]\n"
  "v_exp_f32 v202, v186\n"
  "v_exp_f32 v203, v187\n"
  "v_exp_f32 v204, v188\n"
  "v_exp_f32 v205, v189\n"
  "v_exp_f32 v206, v190\n"
  "v_exp_f32 v207, v191\n"
  "v_mfma_f32_32x32x16_bf16 v[176:191], v[16:19], v[88:91], 0\n"
  "v_exp_f32 v192, v160\n"
  "v_exp_f32 v193, v161\n"
  "v_exp_f32 v194, v162\n"
  "v_exp_f32 v195, v163\n"
  "v_mfma_f32_32x32x16_bf16 v[176:191], v[20:23], v[92:95], v[176:191]\n"
  "buffer_load_dwordx2 v[254:255], v240, s[44:47], s62 offen sc1\n"
  "v_exp_f32 v196, v164\n"
  "v_exp_f32 v197, v165\n"
  "v_exp_f32 v198, v166\n"
  "v_smfmac_f32_32x32x32_bf16 v[112:127], v[28:31], v[200:207], v237\n"
  "s_add_u32 s30, s6, 630784\n"
  "s_addc_u32 s31, s7, 0\n"
  "v_exp_f32 v199, v167\n"
  "v_exp_f32 v200, v168\n"
  "v_exp_f32 v201, v169\n"
  "v_smfmac_f32_32x32x32_bf16 v[128:143], v[24:27], v[192:199], v237\n"
  "global_load_dwordx4 v[16:19], v233, s[30:31] sc1\n"
  "s_waitcnt vmcnt(12)\n"
  "v_cndmask_b32_e64 v38, v38, v242, s[4:5]\n"
  "v_exp_f32 v202, v170\n"
  "v_exp_f32 v203, v171\n"
  "v_exp_f32 v204, v172\n"
  "v_exp_f32 v205, v173\n"
  "v_exp_f32 v206, v174\n"
  "v_exp_f32 v207, v175\n"
  "v_mfma_f32_32x32x16_bf16 v[160:175], v[32:35], v[64:67], 0\n"
  "global_load_dwordx4 v[20:23], v234, s[30:31] sc1\n"
  "v_exp_f32 v192, v176\n"
  "v_exp_f32 v193, v177\n"
  "v_exp_f32 v194, v178\n"
  "v_exp_f32 v195, v179\n"
  "v_mfma_f32_32x32x16_bf16 v[160:175], v[36:39], v[68:71], v[160:175]\n"
  "v_exp_f32 v196, v180\n"
  "v_exp_f32 v197, v181\n"
  "v_exp_f32 v198, v182\n"
  "v_smfmac_f32_32x32x32_bf16 v[128:143], v[28:31], v[200:207], v237\n"
  "v_exp_f32 v199, v183\n"
  "v_exp_f32 v200, v184\n"
  "v_exp_f32 v201, v185\n"
  "v_smfmac_f32_32x32x32_bf16 v[144:159], v[24:27], v[192:199], v237\n"
  "v_exp_f32 v202, v186\n"
  "v_exp_f32 v203, v187\n"
  "v_exp_f32 v204, v188\n"
  "v_exp_f32 v205, v189\n"
  "v_exp_f32 v206, v190\n"
  "v_exp_f32 v207, v191\n"
  "v_mfma_f32_32x32x16_bf16 v[176:191], v[32:35], v[72:75], 0\n"
  "v_exp_f32 v192, v160\n"
  "v_exp_f32 v193, v161\n"
  "v_exp_f32 v194, v162\n"
  "v_exp_f32 v195, v163\n"
  "v_mfma_f32_32x32x16_bf16 v[176:191], v[36:39], v[76:79], v[176:191]\n"
  "v_exp_f32 v196, v164\n"
  "v_exp_f32 v197, v165\n"
  "v_exp_f32 v198, v166\n"
  "v_smfmac_f32_32x32x32_bf16 v[144:159], v[28:31], v[200:207], v237\n"
  "s_mov_b64 exec, s[70:71]\n"
  "global_load_dwordx4 v[24:27], v235, s[30:31] sc1\n"
  "global_load_dwordx4 v[28:31], v236, s[30:31] sc1\n"
  "s_mov_b64 exec, -1\n"
  "v_exp_f32 v199, v167\n"
  "v_exp_f32 v200, v168\n"
  "v_exp_f32 v201, v169\n"
  "v_smfmac_f32_32x32x32_bf16 v[96:111], v[40:43], v[192:199], v237\n"
  "v_exp_f32 v202, v170\n"
  "v_exp_f32 v203, v171\n"
  "v_exp_f32 v204, v172\n"
  "v_exp_f32 v205, v173\n"
  "v_exp_f32 v206, v174\n"
  "v_exp_f32 v207, v175\n"
  "v_mfma_f32_32x32x16_bf16 v[160:175], v[32:35], v[80:83], 0\n"
  "v_exp_f32 v192, v176\n"
  "v_exp_f32 v193, v177\n"
  "v_exp_f32 v194, v178\n"
  "v_exp_f32 v195, v179\n"
  "v_mfma_f32_32x32x16_bf16 v[160:175], v[36:39], v[84:87], v[160:175]\n"
  "s_add_u32 m0, s68, 57344\n"
  "v_exp_f32 v196, v180\n"
  "v_exp_f32 v197, v181\n"
  "v_exp_f32 v198, v182\n"
  "v_smfmac_f32_32x32x32_bf16 v[96:111], v[44:47], v[200:207], v237\n"
  "s_add_u32 s58, s48, 229376\n"
  "s_addc_u32 s59, s49, 0\n"
  "v_exp_f32 v199, v183\n"
  "v_exp_f32 v200, v184\n"
  "v_exp_f32 v201, v185\n"
  "v_smfmac_f32_32x32x32_bf16 v[112:127], v[40:43], v[192:199], v237\n"
  "global_load_lds_dwordx4 v239, s[58:59]\n"
  "v_exp_f32 v202, v186\n"
  "v_exp_f32 v203, v187\n"
  "v_exp_f32 v204, v188\n"
  "v_exp_f32 v205, v189\n"
  "v_exp_f32 v206, v190\n"
  "v_exp_f32 v207, v191\n"
  "v_mfma_f32_32x32x16_bf16 v[176:191], v[32:35], v[88:91], 0\n"
  "v_exp_f32 v192, v160\n"
  "v_exp_f32 v193, v161\n"
  "v_exp_f32 v194, v162\n"
  "v_exp_f32 v195, v163\n"
  "v_mfma_f32_32x32x16_bf16 v[176:191], v[36:39], v[92:95], v[176:191]\n"
  "v_exp_f32 v196, v164\n"
  "v_exp_f32 v197, v165\n"
  "v_exp_f32 v198, v166\n"
  "v_smfmac_f32_32x32x32_bf16 v[112:127], v[44:47], v[200:207], v237\n"
  "s_add_u32 s30, s6, 802816\n"
  "s_addc_u32 s31, s7, 0\n"
  "v_exp_f32 v199, v167\n"
  "v_exp_f32 v200, v168\n"
  "v_exp_f32 v201, v169\n"
  "v_smfmac_f32_32x32x32_bf16 v[128:143], v[40:43], v[192:199], v237\n"
  "global_load_dwordx4 v[32:35], v233, s[30:31] sc1\n"
  "s_waitcnt vmcnt(13)\n"
  "v_cndmask_b32_e64 v54, v54, v242, s[4:5]\n"
  "v_exp_f32 v202, v170\n"
  "v_exp_f32 v203, v171\n"
  "v_exp_f32 v204, v172\n"
  "v_exp_f32 v205, v173\n"
  "v_exp_f32 v206, v174\n"
  "v_exp_f32 v207, v175\n"
  "v_mfma_f32_32x32x16_bf16 v[160:175], v[48:51], v[64:67], 0\n"
  "global_load_dwordx4 v[36:39], v234, s[30:31] sc1\n"
  "v_exp_f32 v192, v176\n"
  "v_exp_f32 v193, v177\n"
  "v_exp_f32 v194, v178\n"
  "v_exp_f32 v195, v179\n"
  "v_mfma_f32_32x32x16_bf16 v[160:175], v[52:55], v[68:71], v[160:175]\n"
  "v_exp_f32 v196, v180\n"
  "v_exp_f32 v197, v181\n"
  "v_exp_f32 v198, v182\n"
  "v_smfmac_f32_32x32x32_bf16 v[128:143], v[44:47], v[200:207], v237\n"
  "v_exp_f32 v199, v183\n"
  "v_exp_f32 v200, v184\n"
  "v_exp_f32 v201, v185\n"
  "v_smfmac_f32_32x32x32_bf16 v[144:159], v[40:43], v[192:199], v237\n"
  "v_exp_f32 v202, v186\n"
  "v_exp_f32 v203, v187\n"
  "v_exp_f32 v204, v188\n"
  "v_exp_f32 v205, v189\n"
  "v_exp_f32 v206, v190\n"
  "v_exp_f32 v207, v191\n"
  "v_mfma_f32_32x32x16_bf16 v[176:191], v[48:51], v[72:75], 0\n"
  "v_exp_f32 v192, v160\n"
  "v_exp_f32 v193, v161\n"
  "v_exp_f32 v194, v162\n"
  "v_exp_f32 v195, v163\n"
  "v_mfma_f32_32x32x16_bf16 v[176:191], v[52:55], v[76:79], v[176:191]\n"
  "v_exp_f32 v196, v164\n"
  "v_exp_f32 v197, v165\n"
  "v_exp_f32 v198, v166\n"
  "v_smfmac_f32_32x32x32_bf16 v[144:159], v[44:47], v[200:207], v237\n"
  "s_mov_b64 exec, s[70:71]\n"
  "global_load_dwordx4 v[40:43], v235, s[30:31] sc1\n"
  "global_load_dwordx4 v[44:47], v236, s[30:31] sc1\n"
  "s_mov_b64 exec, -1\n"
  "v_exp_f32 v199, v167\n"
  "v_exp_f32 v200, v168\n"
  "v_exp_f32 v201, v169\n"
  "v_smfmac_f32_32x32x32_bf16 v[96:111], v[56:59], v[192:199], v237\n"
  "s_waitcnt vmcnt(9)\n"
  "v_xor_b32 v254, v254, v252\n"
  "v_xor_b32 v255, v255, v253\n"
  "v_or_b32 v254, v254, v255\n"
  "v_cmp_ne_u32 vcc, 0, v254\n"
  "s_cbranch_vccz LDe%=\n"
  "s_mov_b32 s69, 0x40000\n"
  "LPe%=:\n"
  "s_sleep 1\n"
  "buffer_load_dwordx2 v[254:255], v240, s[44:47], s62 offen sc1\n"
  "s_waitcnt vmcnt(0)\n"
  "v_xor_b32 v254, v254, v252\n"
  "v_xor_b32 v255, v255, v253\n"
  "v_or_b32 v254, v254, v255\n"
  "v_cmp_ne_u32 vcc, 0, v254\n"
  "s_cbranch_vccz LDe%=\n"
  "s_sub_u32 s69, s69, 1\n"
  "s_cmp_lg_u32 s69, 0\n"
  "s_cbranch_scc1 LPe%=\n"
  "LDe%=:\n"
  "v_exp_f32 v202, v170\n"
  "v_exp_f32 v203, v171\n"
  "v_exp_f32 v204, v172\n"
  "v_exp_f32 v205, v173\n"
  "v_exp_f32 v206, v174\n"
  "v_exp_f32 v207, v175\n"
  "v_mfma_f32_32x32x16_bf16 v[160:175], v[48:51], v[80:83], 0\n"
  "v_exp_f32 v192, v176\n"
  "v_exp_f32 v193, v177\n"
  "v_exp_f32 v194, v178\n"
  "v_exp_f32 v195, v179\n"
  "v_mfma_f32_32x32x16_bf16 v[160:175], v[52:55], v[84:87], v[160:175]\n"
  "s_add_u32 m0, s68, 61440\n"
  "v_exp_f32 v196, v180\n"
  "v_exp_f32 v197, v181\n"
  "v_exp_f32 v198, v182\n"
  "v_smfmac_f32_32x32x32_bf16 v[96:111], v[60:63], v[200:207], v237\n"
  "s_add_u32 s58, s48, 258048\n"
  "s_addc_u32 s59, s49, 0\n"
  "v_exp_f32 v199, v183\n"
  "v_exp_f32 v200, v184\n"
  "v_exp_f32 v201, v185\n"
  "v_smfmac_f32_32x32x32_bf16 v[112:127], v[56:59], v[192:199], v237\n"
  "global_load_lds_dwordx4 v239, s[58:59]\n"
  "v_exp_f32 v202, v186\n"
  "v_exp_f32 v203, v187\n"
  "v_exp_f32 v204, v188\n"
  "v_exp_f32 v205, v189\n"
  "v_exp_f32 v206, v190\n"
  "v_exp_f32 v207, v191\n"
  "v_mfma_f32_32x32x16_bf16 v[176:191], v[48:51], v[88:91], 0\n"
  "v_exp_f32 v192, v160\n"
  "v_exp_f32 v193, v161\n"
  "v_exp_f32 v194, v162\n"
  "v_exp_f32 v195, v163\n"
  "v_mfma_f32_32x32x16_bf16 v[176:191], v[52:55], v[92:95], v[176:191]\n"
  "v_exp_f32 v196, v164\n"
  "v_exp_f32 v197, v165\n"
  "v_exp_f32 v198, v166\n"
  "v_smfmac_f32_32x32x32_bf16 v[112:127], v[60:63], v[200:207], v237\n"
  "s_add_u32 s30, s6, 860160\n"
  "s_addc_u32 s31, s7, 0\n"
  "v_exp_f32 v199, v167\n"
  "v_exp_f32 v200, v168\n"
  "v_exp_f32 v201, v169\n"
  "v_smfmac_f32_32x32x32_bf16 v[128:143], v[56:59], v[192:199], v237\n"
  "global_load_dwordx4 v[48:51], v233, s[30:31] sc1\n"
  "v_cndmask_b32_e64 v6, v6, v242, s[4:5]\n"
  "v_exp_f32 v202, v170\n"
  "v_exp_f32 v203, v171\n"
  "v_exp_f32 v204, v172\n"
  "v_exp_f32 v205, v173\n"
  "v_exp_f32 v206, v174\n"
  "v_exp_f32 v207, v175\n"
  "v_mfma_f32_32x32x16_bf16 v[160:175], v[0:3], v[64:67], 0\n"
  "global_load_dwordx4 v[52:55], v234, s[30:31] sc1\n"
  "v_exp_f32 v192, v176\n"
  "v_exp_f32 v193, v177\n"
  "v_exp_f32 v194, v178\n"
  "v_exp_f32 v195, v179\n"
  "v_mfma_f32_32x32x16_bf16 v[160:175], v[4:7], v[68:71], v[160:175]\n"
  "v_exp_f32 v196, v180\n"
  "v_exp_f32 v197, v181\n"
  "v_exp_f32 v198, v182\n"
  "v_smfmac_f32_32x32x32_bf16 v[128:143], v[60:63], v[200:207], v237\n"
  "v_exp_f32 v199, v183\n"
  "v_exp_f32 v200, v184\n"
  "v_exp_f32 v201, v185\n"
  "v_smfmac_f32_32x32x32_bf16 v[144:159], v[56:59], v[192:199], v237\n"
  "v_exp_f32 v202, v186\n"
  "v_exp_f32 v203, v187\n"
  "v_exp_f32 v204, v188\n"
  "v_exp_f32 v205, v189\n"
  "v_exp_f32 v206, v190\n"
  "v_exp_f32 v207, v191\n"
  "v_mfma_f32_32x32x16_bf16 v[176:191], v[0:3], v[72:75], 0\n"
  "v_exp_f32 v192, v160\n"
  "v_exp_f32 v193, v161\n"
  "v_exp_f32 v194, v162\n"
  "v_exp_f32 v195, v163\n"
  "v_mfma_f32_32x32x16_bf16 v[176:191], v[4:7], v[76:79], v[176:191]\n"
  "v_exp_f32 v196, v164\n"
  "v_exp_f32 v197, v165\n"
  "v_exp_f32 v198, v166\n"
  "v_smfmac_f32_32x32x32_bf16 v[144:159], v[60:63], v[200:207], v237\n"
  "s_mov_b64 exec, s[70:71]\n"
  "global_load_dwordx4 v[56:59], v235, s[30:31] sc1\n"
  "global_load_dwordx4 v[60:63], v236, s[30:31] sc1\n"
  "s_mov_b64 exec, -1\n"
  "v_exp_f32 v199, v167\n"
  "v_exp_f32 v200, v168\n"
  "v_exp_f32 v201, v169\n"
  "v_smfmac_f32_32x32x32_bf16 v[96:111], v[8:11], v[192:199], v237\n"
  "v_exp_f32 v202, v170\n"
  "v_exp_f32 v203, v171\n"
  "v_exp_f32 v204, v172\n"
  "v_exp_f32 v205, v173\n"
  "v_exp_f32 v206, v174\n"
  "v_exp_f32 v207, v175\n"
  "v_mfma_f32_32x32x16_bf16 v[160:175], v[0:3], v[80:83], 0\n"
  "v_exp_f32 v192, v176\n"
  "v_exp_f32 v193, v177\n"
  "v_exp_f32 v194, v178\n"
  "v_exp_f32 v195, v179\n"
  "v_mfma_f32_32x32x16_bf16 v[160:175], v[4:7], v[84:87], v[160:175]\n"
  "s_add_u32 m0, s68, 65536\n"
  "v_exp_f32 v196, v180\n"
  "v_exp_f32 v197, v181\n"
  "v_exp_f32 v198, v182\n"
  "v_smfmac_f32_32x32x32_bf16 v[96:111], v[12:15], v[200:207], v237\n"
  "s_add_u32 s58, s48, 286720\n"
  "s_addc_u32 s59, s49, 0\n"
  "v_exp_f32 v199, v183\n"
  "v_exp_f32 v200, v184\n"
  "v_exp_f32 v201, v185\n"
  "v_smfmac_f32_32x32x32_bf16 v[112:127], v[8:11], v[192:199], v237\n"
  "global_load_lds_dwordx4 v239, s[58:59]\n"
  "v_exp_f32 v202, v186\n"
  "v_exp_f32 v203, v187\n"
  "v_exp_f32 v204, v188\n"
  "v_exp_f32 v205, v189\n"
  "v_exp_f32 v206, v190\n"
  "v_exp_f32 v207, v191\n"
  "v_mfma_f32_32x32x16_bf16 v[176:191], v[0:3], v[88:91], 0\n"
  "v_exp_f32 v192, v160\n"
  "v_exp_f32 v193, v161\n"
  "v_exp_f32 v194, v162\n"
  "v_exp_f32 v195, v163\n"
  "v_mfma_f32_32x32x16_bf16 v[176:191], v[4:7], v[92:95], v[176:191]\n"
  "s_add_u32 m0, s68, 69632\n"
  "v_exp_f32 v196, v164\n"
  "v_exp_f32 v197, v165\n"
  "v_exp_f32 v198, v166\n"
  "v_smfmac_f32_32x32x32_bf16 v[112:127], v[12:15], v[200:207], v237\n"
  "s_add_u32 s58, s48, 315392\n"
  "s_addc_u32 s59, s49, 0\n"
  "v_exp_f32 v199, v167\n"
  "v_exp_f32 v200, v168\n"
  "v_exp_f32 v201, v169\n"
  "v_smfmac_f32_32x32x32_bf16 v[128:143], v[8:11], v[192:199], v237\n"
  "global_load_lds_dwordx4 v239, s[58:59]\n"
  "s_waitcnt vmcnt(12)\n"
  "v_cndmask_b32_e64 v22, v22, v242, s[4:5]\n"
  "v_exp_f32 v202, v170\n"
  "v_exp_f32 v203, v171\n"
  "v_exp_f32 v204, v172\n"
  "v_exp_f32 v205, v173\n"
  "v_exp_f32 v206, v174\n"
  "v_exp_f32 v207, v175\n"
  "v_mfma_f32_32x32x16_bf16 v[160:175], v[16:19], v[64:67], 0\n"
  "v_exp_f32 v192, v176\n"
  "v_exp_f32 v193, v177\n"
  "v_exp_f32 v194, v178\n"
  "v_exp_f32 v195, v179\n"
  "v_mfma_f32_32x32x16_bf16 v[160:175], v[20:23], v[68:71], v[160:175]\n"
  "s_add_u32 m0, s68, 73728\n"
  "v_exp_f32 v196, v180\n"
  "v_exp_f32 v197, v181\n"
  "v_exp_f32 v198, v182\n"
  "v_smfmac_f32_32x32x32_bf16 v[128:143], v[12:15], v[200:207], v237\n"
  "s_add_u32 s58, s48, 344064\n"
  "s_addc_u32 s59, s49, 0\n"
  "v_exp_f32 v199, v183\n"
  "v_exp_f32 v200, v184\n"
  "v_exp_f32 v201, v185\n"
  "v_smfmac_f32_32x32x32_bf16 v[144:159], v[8:11], v[192:199], v237\n"
  "global_load_lds_dwordx4 v239, s[58:59]\n"
  "s_waitcnt vmcnt(8)\n"
  "v_cndmask_b32_e64 v38, v38, v242, s[4:5]\n"
  "v_exp_f32 v202, v186\n"
  "v_exp_f32 v203, v187\n"
  "v_exp_f32 v204, v188\n"
  "v_exp_f32 v205, v189\n"
  "v_exp_f32 v206, v190\n"
  "v_exp_f32 v207, v191\n"
  "v_mfma_f32_32x32x16_bf16 v[176:191], v[32:35], v[64:67], 0\n"
  "v_exp_f32 v192, v160\n"
  "v_exp_f32 v193, v161\n"
  "v_exp_f32 v194, v162\n"
  "v_exp_f32 v195, v163\n"
  "v_mfma_f32_32x32x16_bf16 v[176:191], v[36:39], v[68:71], v[176:191]\n"
  "s_add_u32 m0, s68, 77824\n"
  "v_exp_f32 v196, v164\n"
  "v_exp_f32 v197, v165\n"
  "v_exp_f32 v198, v166\n"
  "v_smfmac_f32_32x32x32_bf16 v[144:159], v[12:15], v[200:207], v237\n"
  "s_add_u32 s58, s48, 372736\n"
  "s_addc_u32 s59, s49, 0\n"
  "v_exp_f32 v199, v167\n"
  "v_exp_f32 v200, v168\n"
  "v_exp_f32 v201, v169\n"
  "v_smfmac_f32_32x32x32_bf16 v[96:111], v[24:27], v[192:199], v237\n"
  "global_load_lds_dwordx4 v239, s[58:59]\n"
  "v_exp_f32 v202, v170\n"
  "v_exp_f32 v203, v171\n"
  "v_exp_f32 v204, v172\n"
  "v_exp_f32 v205, v173\n"
  "v_exp_f32 v206, v174\n"
  "v_exp_f32 v207, v175\n"
  "v_mfma_f32_32x32x16_bf16 v[160:175], v[16:19], v[72:75], 0\n"
  "v_exp_f32 v192, v176\n"
  "v_exp_f32 v193, v177\n"
  "v_exp_f32 v194, v178\n"
  "v_exp_f32 v195, v179\n"
  "v_mfma_f32_32x32x16_bf16 v[160:175], v[20:23], v[76:79], v[160:175]\n"
  "v_exp_f32 v196, v180\n"
  "v_exp_f32 v197, v181\n"
  "v_exp_f32 v198, v182\n"
  "v_smfmac_f32_32x32x32_bf16 v[96:111], v[28:31], v[200:207], v237\n"
  "v_exp_f32 v199, v183\n"
  "v_exp_f32 v200, v184\n"
  "v_exp_f32 v201, v185\n"
  "v_smfmac_f32_32x32x32_bf16 v[96:111], v[40:43], v[192:199], v237\n"
  "s_waitcnt vmcnt(4)\n"
  "v_cndmask_b32_e64 v54, v54, v242, s[4:5]\n"
  "v_exp_f32 v202, v186\n"
  "v_exp_f32 v203, v187\n"
  "v_exp_f32 v204, v188\n"
  "v_exp_f32 v205, v189\n"
  "v_exp_f32 v206, v190\n"
  "v_exp_f32 v207, v191\n"
  "v_mfma_f32_32x32x16_bf16 v[176:191], v[48:51], v[64:67], 0\n"
  "v_exp_f32 v192, v160\n"
  "v_exp_f32 v193, v161\n"
  "v_exp_f32 v194, v162\n"
  "v_exp_f32 v195, v163\n"
  "v_mfma_f32_32x32x16_bf16 v[176:191], v[52:55], v[68:71], v[176:191]\n"
  "v_exp_f32 v196, v164\n"
  "v_exp_f32 v197, v165\n"
  "v_exp_f32 v198, v166\n"
  "v_smfmac_f32_32x32x32_bf16 v[96:111], v[44:47], v[200:207], v237\n"
  "v_exp_f32 v199, v167\n"
  "v_exp_f32 v200, v168\n"
  "v_exp_f32 v201, v169\n"
  "v_smfmac_f32_32x32x32_bf16 v[112:127], v[24:27], v[192:199], v237\n"
  "v_exp_f32 v202, v170\n"
  "v_exp_f32 v203, v171\n"
  "v_exp_f32 v204, v172\n"
  "v_exp_f32 v205, v173\n"
  "v_exp_f32 v206, v174\n"
  "v_exp_f32 v207, v175\n"
  "v_mfma_f32_32x32x16_bf16 v[160:175], v[32:35], v[72:75], 0\n"
  "v_exp_f32 v192, v176\n"
  "v_exp_f32 v193, v177\n"
  "v_exp_f32 v194, v178\n"
  "v_exp_f32 v195, v179\n"
  "v_mfma_f32_32x32x16_bf16 v[160:175], v[36:39], v[76:79], v[160:175]\n"
  "v_exp_f32 v196, v180\n"
  "v_exp_f32 v197, v181\n"
  "v_exp_f32 v198, v182\n"
  "v_smfmac_f32_32x32x32_bf16 v[112:127], v[28:31], v[200:207], v237\n"
  "v_exp_f32 v199, v183\n"
  "v_exp_f32 v200, v184\n"
  "v_exp_f32 v201, v185\n"
  "v_smfmac_f32_32x32x32_bf16 v[96:111], v[56:59], v[192:199], v237\n"
  "v_exp_f32 v202, v186\n"
  "v_exp_f32 v203, v187\n"
  "v_exp_f32 v204, v188\n"
  "v_exp_f32 v205, v189\n"
  "v_exp_f32 v206, v190\n"
  "v_exp_f32 v207, v191\n"
  "v_mfma_f32_32x32x16_bf16 v[176:191], v[16:19], v[80:83], 0\n"
  "v_exp_f32 v192, v160\n"
  "v_exp_f32 v193, v161\n"
  "v_exp_f32 v194, v162\n"
  "v_exp_f32 v195, v163\n"
  "v_mfma_f32_32x32x16_bf16 v[176:191], v[20:23], v[84:87], v[176:191]\n"
  "v_exp_f32 v196, v164\n"
  "v_exp_f32 v197, v165\n"
  "v_exp_f32 v198, v166\n"
  "v_smfmac_f32_32x32x32_bf16 v[96:111], v[60:63], v[200:207], v237\n"
  "v_exp_f32 v199, v167\n"
  "v_exp_f32 v200, v168\n"
  "v_exp_f32 v201, v169\n"
  "v_smfmac_f32_32x32x32_bf16 v[112:127], v[40:43], v[192:199], v237\n"
  "v_exp_f32 v202, v170\n"
  "v_exp_f32 v203, v171\n"
  "v_exp_f32 v204, v172\n"
  "v_exp_f32 v205, v173\n"
  "v_exp_f32 v206, v174\n"
  "v_exp_f32 v207, v175\n"
  "v_mfma_f32_32x32x16_bf16 v[160:175], v[48:51], v[72:75], 0\n"
  "v_exp_f32 v192, v176\n"
  "v_exp_f32 v193, v177\n"
  "v_exp_f32 v194, v178\n"
  "v_exp_f32 v195, v179\n"
  "v_mfma_f32_32x32x16_bf16 v[160:175], v[52:55], v[76:79], v[160:175]\n"
  "ds_write_b128 v245, v[96:99] offset:0\n"
  "v_exp_f32 v196, v180\n"
  "v_exp_f32 v197, v181\n"
  "v_exp_f32 v198, v182\n"
  "v_smfmac_f32_32x32x32_bf16 v[112:127], v[44:47], v[200:207], v237\n"
  "v_exp_f32 v199, v183\n"
  "v_exp_f32 v200, v184\n"
  "v_exp_f32 v201, v185\n"
  "v_smfmac_f32_32x32x32_bf16 v[128:143], v[24:27], v[192:199], v237\n"
  "ds_write_b128 v245, v[100:103] offset:1024\n"
  "v_exp_f32 v202, v186\n"
  "v_exp_f32 v203, v187\n"
  "v_exp_f32 v204, v188\n"
  "v_exp_f32 v205, v189\n"
  "v_exp_f32 v206, v190\n"
  "v_exp_f32 v207, v191\n"
  "v_mfma_f32_32x32x16_bf16 v[176:191], v[32:35], v[80:83], 0\n"
  "v_exp_f32 v192, v160\n"
  "v_exp_f32 v193, v161\n"
  "v_exp_f32 v194, v162\n"
  "v_exp_f32 v195, v163\n"
  "v_mfma_f32_32x32x16_bf16 v[176:191], v[36:39], v[84:87], v[176:191]\n"
  "ds_write_b128 v245, v[104:107] offset:2048\n"
  "v_exp_f32 v196, v164\n"
  "v_exp_f32 v197, v165\n"
  "v_exp_f32 v198, v166\n"
  "v_smfmac_f32_32x32x32_bf16 v[128:143], v[28:31], v[200:207], v237\n"
  "v_exp_f32 v199, v167\n"
  "v_exp_f32 v200, v168\n"
  "v_exp_f32 v201, v169\n"
  "v_smfmac_f32_32x32x32_bf16 v[112:127], v[56:59], v[192:199], v237\n"
  "ds_write_b128 v245, v[108:111] offset:3072\n"
  "v_exp_f32 v202, v170\n"
  "v_exp_f32 v203, v171\n"
  "v_exp_f32 v204, v172\n"
  "v_exp_f32 v205, v173\n"
  "v_exp_f32 v206, v174\n"
  "v_exp_f32 v207, v175\n"
  "v_mfma_f32_32x32x16_bf16 v[160:175], v[48:51], v[80:83], 0\n"
  "v_exp_f32 v192, v176\n"
  "v_exp_f32 v193, v177\n"
  "v_exp_f32 v194, v178\n"
  "v_exp_f32 v195, v179\n"
  "v_mfma_f32_32x32x16_bf16 v[160:175], v[52:55], v[84:87], v[160:175]\n"
  "v_exp_f32 v196, v180\n"
  "v_exp_f32 v197, v181\n"
  "v_exp_f32 v198, v182\n"
  "v_smfmac_f32_32x32x32_bf16 v[112:127], v[60:63], v[200:207], v237\n"
  "v_exp_f32 v199, v183\n"
  "v_exp_f32 v200, v184\n"
  "v_exp_f32 v201, v185\n"
  "v_smfmac_f32_32x32x32_bf16 v[128:143], v[40:43], v[192:199], v237\n"
  "v_exp_f32 v202, v186\n"
  "v_exp_f32 v203, v187\n"
  "v_exp_f32 v204, v188\n"
  "v_exp_f32 v205, v189\n"
  "v_exp_f32 v206, v190\n"
  "v_exp_f32 v207, v191\n"
  "v_mfma_f32_32x32x16_bf16 v[176:191], v[16:19], v[88:91], 0\n"
  "v_exp_f32 v192, v160\n"
  "v_exp_f32 v193, v161\n"
  "v_exp_f32 v194, v162\n"
  "v_exp_f32 v195, v163\n"
  "v_mfma_f32_32x32x16_bf16 v[176:191], v[20:23], v[92:95], v[176:191]\n"
  "ds_write_b128 v246, v[112:115] offset:0\n"
  "v_exp_f32 v196, v164\n"
  "v_exp_f32 v197, v165\n"
  "v_exp_f32 v198, v166\n"
  "v_smfmac_f32_32x32x32_bf16 v[128:143], v[44:47], v[200:207], v237\n"
  "v_exp_f32 v199, v167\n"
  "v_exp_f32 v200, v168\n"
  "v_exp_f32 v201, v169\n"
  "v_smfmac_f32_32x32x32_bf16 v[128:143], v[56:59], v[192:199], v237\n"
  "ds_write_b128 v246, v[116:119] offset:1024\n"
  "v_exp_f32 v202, v170\n"
  "v_exp_f32 v203, v171\n"
  "v_exp_f32 v204, v172\n"
  "v_exp_f32 v205, v173\n"
  "v_exp_f32 v206, v174\n"
  "v_exp_f32 v207, v175\n"
  "v_mfma_f32_32x32x16_bf16 v[160:175], v[32:35], v[88:91], 0\n"
  "v_exp_f32 v192, v176\n"
  "v_exp_f32 v193, v177\n"
  "v_exp_f32 v194, v178\n"
  "v_exp_f32 v195, v179\n"
  "v_mfma_f32_32x32x16_bf16 v[160:175], v[36:39], v[92:95], v[160:175]\n"
  "ds_write_b128 v246, v[120:123] offset:2048\n"
  "v_exp_f32 v196, v180\n"
  "v_exp_f32 v197, v181\n"
  "v_exp_f32 v198, v182\n"
  "v_smfmac_f32_32x32x32_bf16 v[128:143], v[60:63], v[200:207], v237\n"
  "v_exp_f32 v199, v183\n"
  "v_exp_f32 v200, v184\n"
  "v_exp_f32 v201, v185\n"
  "v_smfmac_f32_32x32x32_bf16 v[144:159], v[24:27], v[192:199], v237\n"
  "ds_write_b128 v246, v[124:127] offset:3072\n"
  "v_exp_f32 v202, v186\n"
  "v_exp_f32 v203, v187\n"
  "v_exp_f32 v204, v188\n"
  "v_exp_f32 v205, v189\n"
  "v_exp_f32 v206, v190\n"
  "v_exp_f32 v207, v191\n"
  "v_mfma_f32_32x32x16_bf16 v[176:191], v[48:51], v[88:91], 0\n"
  "v_exp_f32 v192, v160\n"
  "v_exp_f32 v193, v161\n"
  "v_exp_f32 v194, v162\n"
  "v_exp_f32 v195, v163\n"
  "v_mfma_f32_32x32x16_bf16 v[176:191], v[52:55], v[92:95], v[176:191]\n"
  "ds_write_b128 v247, v[128:131] offset:0\n"
  "v_exp_f32 v196, v164\n"
  "v_exp_f32 v197, v165\n"
  "v_exp_f32 v198, v166\n"
  "v_smfmac_f32_32x32x32_bf16 v[144:159], v[28:31], v[200:207], v237\n"
  "ds_write_b128 v247, v[132:135] offset:1024\n"
  "v_exp_f32 v199, v167\n"
  "v_exp_f32 v200, v168\n"
  "v_exp_f32 v201, v169\n"
  "v_smfmac_f32_32x32x32_bf16 v[144:159], v[40:43], v[192:199], v237\n"
  "ds_write_b128 v247, v[136:139] offset:2048\n"
  "v_exp_f32 v202, v170\n"
  "v_exp_f32 v203, v171\n"
  "v_exp_f32 v204, v172\n"
  "v_exp_f32 v205, v173\n"
  "v_exp_f32 v206, v174\n"
  "v_exp_f32 v207, v175\n"
  "ds_write_b128 v247, v[140:143] offset:3072\n"
  "v_exp_f32 v192, v176\n"
  "v_exp_f32 v193, v177\n"
  "v_exp_f32 v194, v178\n"
  "v_exp_f32 v195, v179\n"
  "v_exp_f32 v196, v180\n"
  "v_exp_f32 v197, v181\n"
  "v_exp_f32 v198, v182\n"
  "v_smfmac_f32_32x32x32_bf16 v[144:159], v[44:47], v[200:207], v237\n"
  "s_waitcnt vmcnt(0) lgkmcnt(0)\n"
  "s_barrier\n"
  "v_exp_f32 v199, v183\n"
  "v_exp_f32 v200, v184\n"
  "v_exp_f32 v201, v185\n"
  "v_smfmac_f32_32x32x32_bf16 v[144:159], v[56:59], v[192:199], v237\n"
  "ds_read_b128 v[108:111], v248 offset:3072\n"
  "ds_read_b128 v[124:127], v248 offset:7168\n"
  "ds_read_b128 v[140:143], v248 offset:11264\n"
  "ds_read_b128 v[96:99], v248 offset:0\n"
  "ds_read_b128 v[112:115], v248 offset:4096\n"
  "ds_read_b128 v[128:131], v248 offset:8192\n"
  "ds_read_b128 v[100:103], v248 offset:1024\n"
  "ds_read_b128 v[116:119], v248 offset:5120\n"
  "ds_read_b128 v[132:135], v248 offset:9216\n"
  "ds_read_b128 v[104:107], v248 offset:2048\n"
  "ds_read_b128 v[120:123], v248 offset:6144\n"
  "ds_read_b128 v[136:139], v248 offset:10240\n"
  "v_exp_f32 v202, v186\n"
  "v_exp_f32 v203, v187\n"
  "v_exp_f32 v204, v188\n"
  "v_exp_f32 v205, v189\n"
  "v_exp_f32 v206, v190\n"
  "v_exp_f32 v207, v191\n"
  "s_setprio 3\n"
  "s_nop 0\n"
  "v_smfmac_f32_32x32x32_bf16 v[144:159], v[60:63], v[200:207], v237\n"
  "s_waitcnt lgkmcnt(9)\n"
  "v_add_f32 v108, v108, v124\n"
  "v_add_f32 v109, v109, v125\n"
  "v_add_f32 v110, v110, v126\n"
  "v_add_f32 v111, v111, v127\n"
  "v_add_f32 v108, v108, v140\n"
  "v_add_f32 v109, v109, v141\n"
  "v_add_f32 v110, v110, v142\n"
  "v_add_f32 v111, v111, v143\n"
  "s_waitcnt lgkmcnt(6)\n"
  "v_add_f32 v96, v96, v112\n"
  "v_add_f32 v97, v97, v113\n"
  "v_add_f32 v98, v98, v114\n"
  "v_add_f32 v99, v99, v115\n"
  "v_add_f32 v96, v96, v128\n"
  "v_add_f32 v97, v97, v129\n"
  "v_add_f32 v98, v98, v130\n"
  "v_add_f32 v99, v99, v131\n"
  "s_waitcnt lgkmcnt(3)\n"
  "v_add_f32 v100, v100, v116\n"
  "v_add_f32 v101, v101, v117\n"
  "v_add_f32 v102, v102, v118\n"
  "v_add_f32 v103, v103, v119\n"
  "v_add_f32 v100, v100, v132\n"
  "v_add_f32 v101, v101, v133\n"
  "v_add_f32 v102, v102, v134\n"
  "v_add_f32 v103, v103, v135\n"
  "s_waitcnt lgkmcnt(0)\n"
  "v_add_f32 v104, v104, v120\n"
  "v_add_f32 v105, v105, v121\n"
  "v_add_f32 v106, v106, v122\n"
  "v_add_f32 v107, v107, v123\n"
  "v_add_f32 v104, v104, v136\n"
  "v_add_f32 v105, v105, v137\n"
  "v_add_f32 v106, v106, v138\n"
  "v_add_f32 v107, v107, v139\n"
  "v_add_f32 v8, v156, v108\n"
  "v_add_f32 v9, v156, v108\n"
  "v_add_f32 v144, v144, v96\n"
  "v_add_f32 v145, v145, v97\n"
  "v_permlane32_swap_b32 v8, v9\n"
  "v_add_f32 v146, v146, v98\n"
  "v_add_f32 v147, v147, v99\n"
  "v_add_f32 v148, v148, v100\n"
  "v_add_f32 v149, v149, v101\n"
  "v_add_f32 v150, v150, v102\n"
  "v_add_f32 v151, v151, v103\n"
  "v_add_f32 v152, v152, v104\n"
  "v_add_f32 v153, v153, v105\n"
  "v_add_f32 v154, v154, v106\n"
  "v_add_f32 v155, v155, v107\n"
  "v_add_f32 v156, v156, v108\n"
  "v_add_f32 v157, v157, v109\n"
  "v_add_f32 v158, v158, v110\n"
  "v_add_f32 v159, v159, v111\n"
  "s_mov_b32 s65, 0x7f7fffff\n"
  "v_maximum3_f32 v0, |v144|, |v145|, |v146|\n"
  "v_maximum3_f32 v1, |v147|, |v148|, |v149|\n"
  "v_maximum3_f32 v1, |v150|, |v151|, v1\n"
  "v_maximum3_f32 v0, |v152|, |v153|, v0\n"
  "v_maximum3_f32 v1, |v154|, |v155|, v1\n"
  "v_maximum3_f32 v0, |v156|, |v157|, v0\n"
  "v_maximum3_f32 v1, |v158|, |v159|, v1\n"
  "v_subrev_u32 v8, 0x1f800000, v9\n"
  "v_add_f32 v0, v0, v1\n"
  "v_cmp_nle_f32 s[28:29], v0, s65\n"
  "v_cmp_lt_u32 vcc, 0x40000000, v8\n"
  "v_rcp_f32 v8, v9\n"
  "s_or_b64 s[28:29], s[28:29], vcc\n"
  "s_cbranch_scc1 LF%=\n"
  "LR%=:\n"
  "v_cndmask_b32_e64 v2, -2, 0, s[4:5]\n"
  "v_and_b32 v3, 0xfc00007f, v244\n"
  "v_mul_f32 v144, v8, v144\n"
  "v_mul_f32 v145, v8, v145\n"
  "v_mul_f32 v146, v8, v146\n"
  "v_mul_f32 v147, v8, v147\n"
  "v_mul_f32 v148, v8, v148\n"
  "v_mul_f32 v149, v8, v149\n"
  "v_mul_f32 v150, v8, v150\n"
  "v_mul_f32 v151, v8, v151\n"
  "v_cvt_pk_bf16_f32 v10, v144, v145\n"
  "v_cvt_pk_bf16_f32 v11, v146, v147\n"
  "v_cvt_pk_bf16_f32 v12, v148, v149\n"
  "v_cvt_pk_bf16_f32 v13, v150, v151\n"
  "v_min_u32 v10, -2, v10\n"
  "v_min_u32 v11, -2, v11\n"
  "v_min_u32 v12, -2, v12\n"
  "v_min_u32 v13, -2, v13\n"
  "buffer_store_dwordx4 v[10:13], v238, s[44:47], s12 offen offset:0 sc1\n"
  "v_mul_f32 v152, v8, v152\n"
  "v_mul_f32 v153, v8, v153\n"
  "v_mul_f32 v154, v8, v154\n"
  "v_mul_f32 v155, v8, v155\n"
  "v_mul_f32 v156, v8, v156\n"
  "v_mul_f32 v157, v8, v157\n"
  "v_mul_f32 v158, v8, v158\n"
  "v_mul_f32 v159, v8, v159\n"
  "v_cvt_pk_bf16_f32 v14, v152, v153\n"
  "v_cvt_pk_bf16_f32 v15, v154, v155\n"
  "v_cvt_pk_bf16_f32 v16, v156, v157\n"
  "v_cvt_pk_bf16_f32 v17, v158, v159\n"
  "v_min_u32 v14, -2, v14\n"
  "v_min_u32 v15, -2, v15\n"
  "v_min_u32 v16, v2, v16\n"
  "v_min_u32 v17, v2, v17\n"
  "buffer_store_dwordx4 v[14:17], v238, s[44:47], s12 offen offset:1024 sc1\n"
  "buffer_store_dwordx2 v[252:253], v3, s[44:47], s13 offen sc1\n"
  "s_cmp_eq_u32 s17, 0\n"
  "s_cbranch_scc1 LX%=\n"
  "v_mov_b32 v1, v238\n"
  "v_lshrrev_b32 v0, 4, v238\n"
  "s_barrier\n"
  "v_mov_b32 v6, v252\n"
  "v_mov_b32 v7, v253\n"
  "v_lshl_add_u32 v3, s54, 10, v1\n"
  "ds_write_b128 v3, v[208:211] offset:24576\n"
  "ds_write_b128 v3, v[212:215] offset:28672\n"
  "ds_write_b128 v3, v[216:219] offset:32768\n"
  "ds_write_b128 v3, v[220:223] offset:36864\n"
  "ds_write_b128 v3, v[224:227] offset:40960\n"
  "ds_write_b128 v3, v[228:231] offset:45056\n"
  "v_and_b32 v5, 7, v0\n"
  "v_lshlrev_b32 v149, 4, v5\n"
  "v_and_b32 v8, 4, v5\n"
  "v_add_lshl_u32 v5, v5, v8, 7\n"
  "s_lshr_b32 s65, s54, 1\n"
  "s_mul_i32 s66, s65, 14336\n"
  "s_add_u32 s66, s66, 24576\n"
  "v_lshrrev_b32 v8, 5, v0\n"
  "v_bfe_u32 v9, v0, 2, 2\n"
  "v_lshl_add_u32 v3, v8, 2, v9\n"
  "v_lshl_add_u32 v3, v3, 7, s66\n"
  "v_bfe_u32 v9, v0, 4, 1\n"
  "v_lshl_add_u32 v3, v9, 5, v3\n"
  "v_and_b32 v9, 3, v0\n"
  "v_lshl_add_u32 v3, v9, 3, v3\n"
  "v_bfe_u32 v9, v0, 3, 1\n"
  "v_lshl_add_u32 v3, v9, 6, v3\n"
  "v_xor_b32 v4, 64, v3\n"
  "v_lshlrev_b32 v8, 10, v8\n"
  "v_sub_u32 v18, v3, v8\n"
  "v_sub_u32 v19, v4, v8\n"
  "s_add_u32 s65, s14, 0\n"
  "s_add_u32 s36, s44, s65\n"
  "s_addc_u32 s37, s45, 0\n"
  "s_add_u32 s65, s14, 4096\n"
  "s_add_u32 s38, s44, s65\n"
  "s_addc_u32 s39, s45, 0\n"
  "s_add_u32 s65, s14, 16384\n"
  "s_add_u32 s40, s44, s65\n"
  "s_addc_u32 s41, s45, 0\n"
  "s_add_u32 s65, s14, 20480\n"
  "s_add_u32 s42, s44, s65\n"
  "s_addc_u32 s43, s45, 0\n"
  "s_waitcnt lgkmcnt(0)\n"
  "s_barrier\n"
  "ds_read_b64_tr_b16 v[20:21], v3 offset:0\n"
  "ds_read_b64_tr_b16 v[22:23], v3 offset:1024\n"
  "ds_read_b64_tr_b16 v[24:25], v4 offset:0\n"
  "ds_read_b64_tr_b16 v[26:27], v4 offset:1024\n"
  "ds_read_b64_tr_b16 v[28:29], v3 offset:2048\n"
  "ds_read_b64_tr_b16 v[30:31], v3 offset:3072\n"
  "ds_read_b64_tr_b16 v[32:33], v4 offset:2048\n"
  "ds_read_b64_tr_b16 v[34:35], v4 offset:3072\n"
  "ds_read_b64_tr_b16 v[36:37], v3 offset:3584\n"
  "ds_read_b64_tr_b16 v[38:39], v3 offset:4608\n"
  "ds_read_b64_tr_b16 v[40:41], v4 offset:3584\n"
  "ds_read_b64_tr_b16 v[42:43], v4 offset:4608\n"
  "ds_read_b64_tr_b16 v[44:45], v3 offset:5632\n"
  "ds_read_b64_tr_b16 v[46:47], v3 offset:6656\n"
  "ds_read_b64_tr_b16 v[48:49], v4 offset:5632\n"
  "ds_read_b64_tr_b16 v[50:51], v4 offset:6656\n"
  "ds_read_b64_tr_b16 v[52:53], v3 offset:7168\n"
  "ds_read_b64_tr_b16 v[54:55], v3 offset:8192\n"
  "ds_read_b64_tr_b16 v[56:57], v4 offset:7168\n"
  "ds_read_b64_tr_b16 v[58:59], v4 offset:8192\n"
  "ds_read_b64_tr_b16 v[60:61], v3 offset:9216\n"
  "ds_read_b64_tr_b16 v[62:63], v3 offset:10240\n"
  "ds_read_b64_tr_b16 v[64:65], v4 offset:9216\n"
  "ds_read_b64_tr_b16 v[66:67], v4 offset:10240\n"
  "ds_read_b64_tr_b16 v[68:69], v3 offset:10752\n"
  "ds_read_b64_tr_b16 v[70:71], v3 offset:11776\n"
  "ds_read_b64_tr_b16 v[72:73], v4 offset:10752\n"
  "ds_read_b64_tr_b16 v[74:75], v4 offset:11776\n"
  "ds_read_b64_tr_b16 v[76:77], v3 offset:12800\n"
  "ds_read_b64_tr_b16 v[78:79], v3 offset:13824\n"
  "ds_read_b64_tr_b16 v[80:81], v4 offset:12800\n"
  "ds_read_b64_tr_b16 v[82:83], v4 offset:13824\n"
  "ds_read_b64_tr_b16 v[84:85], v3 offset:28672\n"
  "ds_read_b64_tr_b16 v[86:87], v3 offset:29696\n"
  "ds_read_b64_tr_b16 v[88:89], v4 offset:28672\n"
  "ds_read_b64_tr_b16 v[90:91], v4 offset:29696\n"
  "ds_read_b64_tr_b16 v[92:93], v3 offset:30720\n"
  "ds_read_b64_tr_b16 v[94:95], v3 offset:31744\n"
  "ds_read_b64_tr_b16 v[96:97], v4 offset:30720\n"
  "ds_read_b64_tr_b16 v[98:99], v4 offset:31744\n"
  "ds_read_b64_tr_b16 v[100:101], v3 offset:32256\n"
  "ds_read_b64_tr_b16 v[102:103], v3 offset:33280\n"
  "ds_read_b64_tr_b16 v[104:105], v4 offset:32256\n"
  "ds_read_b64_tr_b16 v[106:107], v4 offset:33280\n"
  "ds_read_b64_tr_b16 v[108:109], v3 offset:34304\n"
  "ds_read_b64_tr_b16 v[110:111], v3 offset:35328\n"
  "ds_read_b64_tr_b16 v[112:113], v4 offset:34304\n"
  "ds_read_b64_tr_b16 v[114:115], v4 offset:35328\n"
  "buffer_load_dwordx2 v[152:153], v5, s[44:47], s15 offen sc1\n"
  "ds_read_b64_tr_b16 v[116:117], v3 offset:35840\n"
  "ds_read_b64_tr_b16 v[118:119], v3 offset:36864\n"
  "ds_read_b64_tr_b16 v[120:121], v4 offset:35840\n"
  "ds_read_b64_tr_b16 v[122:123], v4 offset:36864\n"
  "ds_read_b64_tr_b16 v[124:125], v3 offset:37888\n"
  "ds_read_b64_tr_b16 v[126:127], v3 offset:38912\n"
  "ds_read_b64_tr_b16 v[128:129], v4 offset:37888\n"
  "ds_read_b64_tr_b16 v[130:131], v4 offset:38912\n"
  "ds_read_b64_tr_b16 v[132:133], v3 offset:39424\n"
  "ds_read_b64_tr_b16 v[134:135], v3 offset:40448\n"
  "ds_read_b64_tr_b16 v[136:137], v4 offset:39424\n"
  "ds_read_b64_tr_b16 v[138:139], v4 offset:40448\n"
  "ds_read_b64_tr_b16 v[140:141], v3 offset:41472\n"
  "ds_read_b64_tr_b16 v[142:143], v18 offset:42496\n"
  "ds_read_b64_tr_b16 v[144:145], v4 offset:41472\n"
  "ds_read_b64_tr_b16 v[146:147], v19 offset:42496\n"
  "s_waitcnt vmcnt(0)\n"
  "v_xor_b32 v152, v152, v6\n"
  "v_xor_b32 v153, v153, v7\n"
  "v_or_b32 v152, v152, v153\n"
  "v_cmp_ne_u32 vcc, 0, v152\n"
  "s_cbranch_vccz LDc%=\n"
  "s_mov_b32 s69, 0x40000\n"
  "LPc%=:\n"
  "s_sleep 1\n"
  "buffer_load_dwordx2 v[152:153], v5, s[44:47], s15 offen sc1\n"
  "s_waitcnt vmcnt(0)\n"
  "v_xor_b32 v152, v152, v6\n"
  "v_xor_b32 v153, v153, v7\n"
  "v_or_b32 v152, v152, v153\n"
  "v_cmp_ne_u32 vcc, 0, v152\n"
  "s_cbranch_vccz LDc%=\n"
  "s_sub_u32 s69, s69, 1\n"
  "s_cmp_lg_u32 s69, 0\n"
  "s_cbranch_scc1 LPc%=\n"
  "LDc%=:\n"
  "s_mov_b32 s69, 0x10000\n"
  "LP0%=:\n"
  "global_load_dwordx4 v[160:163], v1, s[36:37] offset:0 sc1\n"
  "global_load_dwordx4 v[164:167], v1, s[36:37] offset:1024 sc1\n"
  "global_load_dwordx4 v[168:171], v1, s[36:37] offset:2048 sc1\n"
  "global_load_dwordx4 v[172:175], v1, s[36:37] offset:3072 sc1\n"
  "global_load_dwordx4 v[176:179], v1, s[38:39] offset:0 sc1\n"
  "global_load_dwordx4 v[180:183], v1, s[38:39] offset:1024 sc1\n"
  "global_load_dwordx4 v[184:187], v1, s[38:39] offset:2048 sc1\n"
  "global_load_dwordx4 v[188:191], v1, s[38:39] offset:3072 sc1\n"
  "global_load_dwordx4 v[192:195], v1, s[40:41] offset:0 sc1\n"
  "global_load_dwordx4 v[196:199], v1, s[40:41] offset:1024 sc1\n"
  "s_mul_i32 m0, s54, 10240\n"
  "s_mul_i32 s65, s54, 1280\n"
  "v_bfe_u32 v8, v0, 3, 1\n"
  "v_mul_u32_u24 v150, 272, v8\n"
  "v_bfe_u32 v8, v0, 4, 1\n"
  "v_mul_u32_u24 v8, 640, v8\n"
  "v_add3_u32 v150, v150, v8, s65\n"
  "v_lshrrev_b32 v8, 5, v0\n"
  "v_lshl_add_u32 v150, v8, 7, v150\n"
  "v_and_b32 v8, 3, v0\n"
  "v_lshl_add_u32 v150, v8, 5, v150\n"
  "v_bfe_u32 v8, v0, 2, 1\n"
  "v_mul_u32_u24 v8, 5120, v8\n"
  "v_add_u32 v150, v150, v8\n"
  "v_lshrrev_b32 v8, 3, v0\n"
  "v_lshl_add_u32 v8, s54, 3, v8\n"
  "v_mul_u32_u24 v9, 0x700, v8\n"
  "v_add_u32 v151, v149, v9\n"
  "s_add_u32 s65, s16, 57344\n"
  "v_mov_b32 v2, 0\n"
  "s_sub_u32 s69, s69, 1\n"
  "s_waitcnt lgkmcnt(0)\n"
  "s_waitcnt vmcnt(9)\n"
  "v_mfma_f32_32x32x16_bf16 v[224:239], v[160:163], v[20:23], 0\n"
  "global_load_dwordx4 v[200:203], v1, s[40:41] offset:2048 sc1\n"
  "v_max3_u32 v2, v160, v161, v2\n"
  "v_mfma_f32_32x32x16_bf16 v[240:255], v[160:163], v[24:27], 0\n"
  "v_max3_u32 v2, v162, v163, v2\n"
  "s_waitcnt vmcnt(9)\n"
  "v_mfma_f32_32x32x16_bf16 v[224:239], v[164:167], v[28:31], v[224:239]\n"
  "global_load_dwordx4 v[204:207], v1, s[40:41] offset:3072 sc1\n"
  "v_max3_u32 v2, v164, v165, v2\n"
  "v_mfma_f32_32x32x16_bf16 v[240:255], v[164:167], v[32:35], v[240:255]\n"
  "v_max3_u32 v2, v166, v167, v2\n"
  "s_waitcnt vmcnt(9)\n"
  "v_mfma_f32_32x32x16_bf16 v[224:239], v[168:171], v[36:39], v[224:239]\n"
  "global_load_dwordx4 v[208:211], v1, s[42:43] offset:0 sc1\n"
  "v_max3_u32 v2, v168, v169, v2\n"
  "v_mfma_f32_32x32x16_bf16 v[240:255], v[168:171], v[40:43], v[240:255]\n"
  "v_max3_u32 v2, v170, v171, v2\n"
  "s_waitcnt vmcnt(9)\n"
  "v_mfma_f32_32x32x16_bf16 v[224:239], v[172:175], v[44:47], v[224:239]\n"
  "global_load_dwordx4 v[212:215], v1, s[42:43] offset:1024 sc1\n"
  "v_max3_u32 v2, v172, v173, v2\n"
  "v_mfma_f32_32x32x16_bf16 v[240:255], v[172:175], v[48:51], v[240:255]\n"
  "v_max3_u32 v2, v174, v175, v2\n"
  "s_waitcnt vmcnt(9)\n"
  "v_mfma_f32_32x32x16_bf16 v[224:239], v[176:179], v[52:55], v[224:239]\n"
  "global_load_dwordx4 v[216:219], v1, s[42:43] offset:2048 sc1\n"
  "v_max3_u32 v2, v176, v177, v2\n"
  "v_mfma_f32_32x32x16_bf16 v[240:255], v[176:179], v[56:59], v[240:255]\n"
  "v_max3_u32 v2, v178, v179, v2\n"
  "s_waitcnt vmcnt(9)\n"
  "v_mfma_f32_32x32x16_bf16 v[224:239], v[180:183], v[60:63], v[224:239]\n"
  "global_load_dwordx4 v[220:223], v1, s[42:43] offset:3072 sc1\n"
  "v_max3_u32 v2, v180, v181, v2\n"
  "v_mfma_f32_32x32x16_bf16 v[240:255], v[180:183], v[64:67], v[240:255]\n"
  "v_max3_u32 v2, v182, v183, v2\n"
  "s_waitcnt vmcnt(9)\n"
  "v_mfma_f32_32x32x16_bf16 v[224:239], v[184:187], v[68:71], v[224:239]\n"
  "v_max3_u32 v2, v184, v185, v2\n"
  "v_mfma_f32_32x32x16_bf16 v[240:255], v[184:187], v[72:75], v[240:255]\n"
  "v_max3_u32 v2, v186, v187, v2\n"
  "s_waitcnt vmcnt(8)\n"
  "v_mfma_f32_32x32x16_bf16 v[224:239], v[188:191], v[76:79], v[224:239]\n"
  "v_max3_u32 v2, v188, v189, v2\n"
  "v_mfma_f32_32x32x16_bf16 v[240:255], v[188:191], v[80:83], v[240:255]\n"
  "v_max3_u32 v2, v190, v191, v2\n"
  "s_waitcnt vmcnt(7)\n"
  "v_mfma_f32_32x32x16_bf16 v[224:239], v[192:195], v[84:87], v[224:239]\n"
  "v_max3_u32 v2, v192, v193, v2\n"
  "v_mfma_f32_32x32x16_bf16 v[240:255], v[192:195], v[88:91], v[240:255]\n"
  "v_max3_u32 v2, v194, v195, v2\n"
  "s_waitcnt vmcnt(6)\n"
  "v_mfma_f32_32x32x16_bf16 v[224:239], v[196:199], v[92:95], v[224:239]\n"
  "v_max3_u32 v2, v196, v197, v2\n"
  "v_mfma_f32_32x32x16_bf16 v[240:255], v[196:199], v[96:99], v[240:255]\n"
  "v_max3_u32 v2, v198, v199, v2\n"
  "s_waitcnt vmcnt(5)\n"
  "v_mfma_f32_32x32x16_bf16 v[224:239], v[200:203], v[100:103], v[224:239]\n"
  "v_max3_u32 v2, v200, v201, v2\n"
  "v_mfma_f32_32x32x16_bf16 v[240:255], v[200:203], v[104:107], v[240:255]\n"
  "v_max3_u32 v2, v202, v203, v2\n"
  "s_waitcnt vmcnt(4)\n"
  "v_mfma_f32_32x32x16_bf16 v[224:239], v[204:207], v[108:111], v[224:239]\n"
  "v_max3_u32 v2, v204, v205, v2\n"
  "v_mfma_f32_32x32x16_bf16 v[240:255], v[204:207], v[112:115], v[240:255]\n"
  "v_max3_u32 v2, v206, v207, v2\n"
  "s_waitcnt vmcnt(3)\n"
  "v_mfma_f32_32x32x16_bf16 v[224:239], v[208:211], v[116:119], v[224:239]\n"
  "v_max3_u32 v2, v208, v209, v2\n"
  "v_mfma_f32_32x32x16_bf16 v[240:255], v[208:211], v[120:123], v[240:255]\n"
  "v_max3_u32 v2, v210, v211, v2\n"
  "s_waitcnt vmcnt(2)\n"
  "v_mfma_f32_32x32x16_bf16 v[224:239], v[212:215], v[124:127], v[224:239]\n"
  "v_max3_u32 v2, v212, v213, v2\n"
  "v_mfma_f32_32x32x16_bf16 v[240:255], v[212:215], v[128:131], v[240:255]\n"
  "v_max3_u32 v2, v214, v215, v2\n"
  "s_waitcnt vmcnt(1)\n"
  "v_mfma_f32_32x32x16_bf16 v[224:239], v[216:219], v[132:135], v[224:239]\n"
  "v_max3_u32 v2, v216, v217, v2\n"
  "v_mfma_f32_32x32x16_bf16 v[240:255], v[216:219], v[136:139], v[240:255]\n"
  "v_max3_u32 v2, v218, v219, v2\n"
  "s_waitcnt vmcnt(0)\n"
  "v_mfma_f32_32x32x16_bf16 v[224:239], v[220:223], v[140:143], v[224:239]\n"
  "v_max3_u32 v2, v220, v221, v2\n"
  "v_mfma_f32_32x32x16_bf16 v[240:255], v[220:223], v[144:147], v[240:255]\n"
  "v_max3_u32 v2, v222, v223, v2\n"
  "v_cmp_eq_u32 vcc, -1, v2\n"
  "s_cbranch_vccz LP1%=\n"
  "s_cmp_lg_u32 s69, 0\n"
  "s_cbranch_scc1 LP0%=\n"
  "LP1%=:\n"
  "s_barrier\n"
  "s_nop 5\n"
  "ds_write_addtid_b32 v224 offset:0\n"
  "ds_write_addtid_b32 v225 offset:272\n"
  "ds_write_addtid_b32 v226 offset:640\n"
  "ds_write_addtid_b32 v227 offset:912\n"
  "ds_write_addtid_b32 v228 offset:1280\n"
  "ds_write_addtid_b32 v229 offset:1552\n"
  "ds_write_addtid_b32 v230 offset:1920\n"
  "ds_write_addtid_b32 v231 offset:2192\n"
  "ds_write_addtid_b32 v232 offset:2560\n"
  "ds_write_addtid_b32 v233 offset:2832\n"
  "ds_write_addtid_b32 v234 offset:3200\n"
  "ds_write_addtid_b32 v235 offset:3472\n"
  "ds_write_addtid_b32 v236 offset:3840\n"
  "ds_write_addtid_b32 v237 offset:4112\n"
  "ds_write_addtid_b32 v238 offset:4480\n"
  "ds_write_addtid_b32 v239 offset:4752\n"
  "ds_write_addtid_b32 v240 offset:5120\n"
  "ds_write_addtid_b32 v241 offset:5392\n"
  "ds_write_addtid_b32 v242 offset:5760\n"
  "ds_write_addtid_b32 v243 offset:6032\n"
  "ds_write_addtid_b32 v244 offset:6400\n"
  "ds_write_addtid_b32 v245 offset:6672\n"
  "ds_write_addtid_b32 v246 offset:7040\n"
  "ds_write_addtid_b32 v247 offset:7312\n"
  "ds_write_addtid_b32 v248 offset:7680\n"
  "ds_write_addtid_b32 v249 offset:7952\n"
  "ds_write_addtid_b32 v250 offset:8320\n"
  "ds_write_addtid_b32 v251 offset:8592\n"
  "ds_write_addtid_b32 v252 offset:8960\n"
  "ds_write_addtid_b32 v253 offset:9232\n"
  "ds_write_addtid_b32 v254 offset:9600\n"
  "ds_write_addtid_b32 v255 offset:9872\n"
  "s_waitcnt lgkmcnt(0)\n"
  "s_barrier\n"
  "ds_read_b128 v[20:23], v150 offset:0\n"
  "ds_read_b128 v[28:31], v150 offset:20480\n"
  "ds_read_b128 v[24:27], v150 offset:16\n"
  "ds_read_b128 v[32:35], v150 offset:20496\n"
  "ds_read_b128 v[36:39], v150 offset:10240\n"
  "ds_read_b128 v[44:47], v150 offset:30720\n"
  "ds_read_b128 v[40:43], v150 offset:10256\n"
  "ds_read_b128 v[48:51], v150 offset:30736\n"
  "s_waitcnt lgkmcnt(4)\n"
  "v_add_f32 v20, v20, v28\n"
  "v_add_f32 v21, v21, v29\n"
  "v_add_f32 v22, v22, v30\n"
  "v_add_f32 v23, v23, v31\n"
  "v_add_f32 v24, v24, v32\n"
  "v_add_f32 v25, v25, v33\n"
  "v_add_f32 v26, v26, v34\n"
  "v_add_f32 v27, v27, v35\n"
  "v_cvt_pk_bf16_f32 v60, v20, v21\n"
  "v_cvt_pk_bf16_f32 v61, v22, v23\n"
  "v_cvt_pk_bf16_f32 v62, v24, v25\n"
  "v_cvt_pk_bf16_f32 v63, v26, v27\n"
  "buffer_store_dwordx4 v[60:63], v151, s[8:11], s16 offen sc1\n"
  "s_waitcnt lgkmcnt(0)\n"
  "v_add_f32 v36, v36, v44\n"
  "v_add_f32 v37, v37, v45\n"
  "v_add_f32 v38, v38, v46\n"
  "v_add_f32 v39, v39, v47\n"
  "v_add_f32 v40, v40, v48\n"
  "v_add_f32 v41, v41, v49\n"
  "v_add_f32 v42, v42, v50\n"
  "v_add_f32 v43, v43, v51\n"
  "v_cvt_pk_bf16_f32 v64, v36, v37\n"
  "v_cvt_pk_bf16_f32 v65, v38, v39\n"
  "v_cvt_pk_bf16_f32 v66, v40, v41\n"
  "v_cvt_pk_bf16_f32 v67, v42, v43\n"
  "buffer_store_dwordx4 v[64:67], v151, s[8:11], s65 offen sc1\n"
  "LX%=:\n"
  "s_cmp_eq_u32 s2, 56\n"
  "s_cbranch_scc1 LZ%=\n"
  "s_endpgm\n"
  "LF%=:\n"
  "s_mov_b32 vcc_lo, 0\n"
  "s_mov_b32 vcc_hi, -1\n"
  "v_mov_b32 v0, 0xff7fffff\n"
  "v_cndmask_b32_e64 v94, v94, 0, vcc\n"
  "v_cndmask_b32_e64 v95, v95, 0, vcc\n"
  "v_mov_b32 v144, 0\n"
  "v_mov_b32 v145, 0\n"
  "v_mov_b32 v146, 0\n"
  "v_mov_b32 v147, 0\n"
  "v_mov_b32 v148, 0\n"
  "v_mov_b32 v149, 0\n"
  "v_mov_b32 v150, 0\n"
  "v_mov_b32 v151, 0\n"
  "v_mov_b32 v152, 0\n"
  "v_mov_b32 v153, 0\n"
  "v_mov_b32 v154, 0\n"
  "v_mov_b32 v155, 0\n"
  "v_mov_b32 v156, 0\n"
  "v_mov_b32 v157, 0\n"
  "v_mov_b32 v158, 0\n"
  "v_mov_b32 v159, 0\n"
  "s_mov_b32 s65, 1048576\n"
  "LG%=:\n"
  "s_add_u32 s30, s44, s65\n"
  "s_addc_u32 s31, s45, 0\n"
  "global_load_dwordx4 v[16:19], v233, s[30:31] sc1\n"
  "global_load_dwordx4 v[20:23], v234, s[30:31] sc1\n"
  "s_mov_b64 exec, s[70:71]\n"
  "global_load_dwordx4 v[24:27], v235, s[30:31] sc1\n"
  "global_load_dwordx4 v[28:31], v236, s[30:31] sc1\n"
  "s_mov_b64 exec, -1\n"
  "s_waitcnt vmcnt(0)\n"
  "v_cndmask_b32_e64 v22, v22, 0, vcc\n"
  "v_cndmask_b32_e64 v23, v23, 0, vcc\n"
  "v_mfma_f32_32x32x16_bf16 v[160:175], v[16:19], v[88:91], 0\n"
  "s_nop 0\n"
  "v_mfma_f32_32x32x16_bf16 v[160:175], v[20:23], v[92:95], v[160:175]\n"
  "s_nop 11\n"
  "v_max3_f32 v4, v160, v161, v0\n"
  "v_max3_f32 v4, v162, v163, v4\n"
  "v_max3_f32 v4, v164, v165, v4\n"
  "v_max3_f32 v4, v166, v167, v4\n"
  "v_max3_f32 v4, v168, v169, v4\n"
  "v_max3_f32 v4, v170, v171, v4\n"
  "v_max3_f32 v4, v172, v173, v4\n"
  "v_max3_f32 v4, v174, v175, v4\n"
  "v_mov_b32 v5, v4\n"
  "s_nop 1\n"
  "v_permlane32_swap_b32 v4, v5\n"
  "v_max_f32 v1, v4, v5\n"
  "v_sub_f32 v2, v0, v1\n"
  "v_exp_f32 v2, v2\n"
  "v_mov_b32 v0, v1\n"
  "v_sub_f32 v192, v160, v1\n"
  "v_sub_f32 v193, v161, v1\n"
  "v_sub_f32 v194, v162, v1\n"
  "v_sub_f32 v195, v163, v1\n"
  "v_sub_f32 v196, v164, v1\n"
  "v_sub_f32 v197, v165, v1\n"
  "v_sub_f32 v198, v166, v1\n"
  "v_sub_f32 v199, v167, v1\n"
  "v_sub_f32 v200, v168, v1\n"
  "v_sub_f32 v201, v169, v1\n"
  "v_sub_f32 v202, v170, v1\n"
  "v_sub_f32 v203, v171, v1\n"
  "v_sub_f32 v204, v172, v1\n"
  "v_sub_f32 v205, v173, v1\n"
  "v_sub_f32 v206, v174, v1\n"
  "v_sub_f32 v207, v175, v1\n"
  "v_exp_f32 v192, v192\n"
  "v_exp_f32 v193, v193\n"
  "v_exp_f32 v194, v194\n"
  "v_exp_f32 v195, v195\n"
  "v_exp_f32 v196, v196\n"
  "v_exp_f32 v197, v197\n"
  "v_exp_f32 v198, v198\n"
  "v_exp_f32 v199, v199\n"
  "v_exp_f32 v200, v200\n"
  "v_exp_f32 v201, v201\n"
  "v_exp_f32 v202, v202\n"
  "v_exp_f32 v203, v203\n"
  "v_exp_f32 v204, v204\n"
  "v_exp_f32 v205, v205\n"
  "v_exp_f32 v206, v206\n"
  "v_exp_f32 v207, v207\n"
  "v_mul_f32 v144, v144, v2\n"
  "v_mul_f32 v145, v145, v2\n"
  "v_mul_f32 v146, v146, v2\n"
  "v_mul_f32 v147, v147, v2\n"
  "v_mul_f32 v148, v148, v2\n"
  "v_mul_f32 v149, v149, v2\n"
  "v_mul_f32 v150, v150, v2\n"
  "v_mul_f32 v151, v151, v2\n"
  "v_mul_f32 v152, v152, v2\n"
  "v_mul_f32 v153, v153, v2\n"
  "v_mul_f32 v154, v154, v2\n"
  "v_mul_f32 v155, v155, v2\n"
  "v_mul_f32 v156, v156, v2\n"
  "v_mul_f32 v157, v157, v2\n"
  "v_mul_f32 v158, v158, v2\n"
  "v_mul_f32 v159, v159, v2\n"
  "s_nop 1\n"
  "v_smfmac_f32_32x32x32_bf16 v[144:159], v[24:27], v[192:199], v237\n"
  "v_smfmac_f32_32x32x32_bf16 v[144:159], v[28:31], v[200:207], v237\n"
  "s_add_u32 s65, s65, 57344\n"
  "s_cmp_lg_u32 s65, 4718592\n"
  "s_nop 11\n"
  "s_cbranch_scc1 LG%=\n"
  "v_mov_b32 v8, v156\n"
  "v_mov_b32 v9, v156\n"
  "s_nop 1\n"
  "v_permlane32_swap_b32 v8, v9\n"
  "v_rcp_f32 v8, v9\n"
  "s_nop 1\n"
  "s_branch LR%=\n"
  "LA%=:\n"
  "s_load_dwordx4 s[48:51], s[0:1], 24\n"
  "s_load_dwordx2 s[36:37], s[0:1], 16\n"
  "s_load_dwordx2 s[52:53], s[0:1], 40\n"
  "s_load_dwordx8 s[56:63], s[0:1], 72\n"
  "s_load_dwordx2 s[54:55], s[0:1], 64\n"
  "s_load_dwordx2 s[64:65], s[0:1], 104\n"
  "s_load_dwordx2 s[40:41], s[0:1], 56\n"
  "s_setprio 2\n"
  "v_readfirstlane_b32 s19, v0\n"
  "s_bfe_u32 s17, s2, 0x30003\n"
  "s_and_b32 s28, s2, 1\n"
  "s_add_u32 s17, s17, s28\n"
  "s_add_u32 s17, s17, 3\n"
  "s_and_b32 s17, s17, 7\n"
  "s_bfe_u32 s28, s2, 0x20006\n"
  "s_and_b32 s18, s2, 7\n"
  "s_lshl3_add_u32 s18, s28, s18\n"
  "v_lshrrev_b32 v1, 3, v0\n"
  "v_and_b32 v2, 7, v0\n"
  "s_lshr_b32 s19, s19, 6\n"
  "s_lshl_b32 s28, s17, 5\n"
  "s_add_u32 s20, s28, s18\n"
  "s_lshl_b32 s28, s17, 7\n"
  "v_lshl_add_u32 v3, v2, 4, s28\n"
  "v_lshl_add_u32 v4, s18, 6, v1\n"
  "s_add_u32 s16, s28, 128\n"
  "s_lshr_b32 s21, s19, 1\n"
  "s_lshl_b32 s28, s20, 10\n"
  "v_lshl_add_u32 v162, v0, 2, s28\n"
  "s_lshl_b32 s28, s20, 2\n"
  "s_cmp_eq_u32 s17, 7\n"
  "s_waitcnt lgkmcnt(0)\n"
  "s_cbranch_scc1 LC%=\n"
  "global_load_dword v250, v162, s[48:49]\n"
  "global_load_dword v251, v162, s[50:51]\n"
  "global_load_dword v252, v162, s[56:57]\n"
  "global_load_dword v253, v162, s[58:59]\n"
  "global_load_dword v254, v162, s[60:61]\n"
  "global_load_dword v255, v162, s[62:63]\n"
  "s_load_dword s31, s[64:65], s28\n"
  "s_load_dword s6, s[52:53], s28\n"
  "s_load_dwordx2 s[66:67], s[36:37], s16\n"
  "LC%=:\n"
  "s_and_b32 s28, s2, 255\n"
  "s_lshl2_add_u32 s28, s28, s19\n"
  "s_lshl_b32 s28, s28, 7\n"
  "v_mbcnt_lo_u32_b32 v226, -1, 0\n"
  "v_lshl_add_u32 v226, v226, 17, s28\n"
  "s_cmp_eq_u32 s17, 7\n"
  "s_cselect_b64 exec, 15, 3\n"
  "global_load_dword v227, v226, s[36:37] sc1\n"
  "s_mov_b64 exec, -1\n"
  "s_cmp_eq_u32 s17, 7\n"
  "s_cbranch_scc1 LQ%=\n"
  "v_mul_u32_u24 v9, 260, v2\n"
  "v_add_lshl_u32 v244, v9, v1, 2\n"
  "v_lshrrev_b32 v9, 4, v0\n"
  "v_and_b32 v10, 15, v0\n"
  "v_mul_u32_u24 v11, 260, v9\n"
  "v_lshl_add_u32 v245, v10, 4, v11\n"
  "v_lshlrev_b32 v10, 4, v10\n"
  "v_lshl_add_u32 v246, v9, 13, v10\n"
  "v_mul_u32_u24 v9, 0x700, v4\n"
  "v_add_u32 v247, v9, v3\n"
  "s_lshl_b32 s28, s17, 18\n"
  "s_lshl_b32 s29, s18, 8\n"
  "s_add_u32 s28, s28, s29\n"
  "s_add_u32 s24, s28, 7340032\n"
  "s_add_u32 s25, s28, 9437184\n"
  "v_add_u32 v163, 67584, v244\n"
  "v_add_u32 v164, 67584, v245\n"
  "v_add_u32 v165, 71744, v245\n"
  "v_mul_u32_u24 v5, 896, v4\n"
  "v_add_u32 v160, v5, v3\n"
  "v_add_u32 v161, 0x7000, v160\n"
  "global_load_dwordx4 v[232:235], v160, s[54:55]\n"
  "global_load_dwordx4 v[236:239], v161, s[54:55]\n"
  "s_and_b32 s22, s19, 1\n"
  "v_and_b32 v14, 63, v0\n"
  "v_and_b32 v15, 31, v0\n"
  "v_bfe_u32 v16, v0, 5, 1\n"
  "v_and_b32 v17, 127, v0\n"
  "s_mul_i32 s27, s19, 16896\n"
  "v_mul_u32_u24 v9, 528, v15\n"
  "v_lshl_add_u32 v10, v16, 4, s27\n"
  "v_add_u32 v205, v9, v10\n"
  "v_lshrrev_b32 v9, 0, v14\n"
  "v_lshl_add_u32 v9, v9, 23, v14\n"
  "v_lshlrev_b32 v218, 3, v9\n"
  "v_lshlrev_b32 v217, 4, v0\n"
  "s_lshl2_add_u32 s28, s20, s19\n"
  "s_lshl_b32 s28, s28, 7\n"
  "s_add_u32 s8, s28, 131072\n"
  "s_add_u32 s12, s28, 393216\n"
  "s_bfe_u32 s28, s18, 0x20002\n"
  "s_lshl_b32 s29, s17, 7\n"
  "s_add_u32 s28, s28, s29\n"
  "s_lshl4_add_u32 s28, s19, s28\n"
  "s_lshl_b32 s28, s28, 7\n"
  "s_add_u32 s9, s28, 131072\n"
  "s_add_u32 s23, s28, 393216\n"
  "v_and_b32 v9, 3, v0\n"
  "v_bfe_u32 v10, v0, 2, 1\n"
  "v_lshlrev_b32 v9, 9, v9\n"
  "v_lshl_add_u32 v219, v10, 13, v9\n"
  "s_lshl_b32 s28, s20, 13\n"
  "s_add_u32 s10, s28, 7340032\n"
  "s_add_u32 s11, s28, 9437184\n"
  "s_mul_i32 s28, s19, 3328\n"
  "s_add_u32 s28, s28, 67584\n"
  "v_lshl_add_u32 v9, v15, 1, v16\n"
  "v_lshl_add_u32 v206, v9, 4, s28\n"
  "v_sub_u32 v9, 4, v16\n"
  "v_lshlrev_b32 v9, v9, v15\n"
  "v_lshl_add_u32 v9, v16, 9, v9\n"
  "s_add_u32 s29, s28, 1024\n"
  "v_add_u32 v207, s29, v9\n"
  "v_mul_u32_u24 v9, 248, v16\n"
  "v_add3_u32 v208, v207, v9, 8\n"
  "v_lshl_add_u32 v9, v16, 7, v15\n"
  "s_add_u32 s29, s28, 2048\n"
  "v_lshl_add_u32 v209, v9, 2, s29\n"
  "s_mul_i32 s28, s21, 10496\n"
  "s_lshl_b32 s29, s22, 8\n"
  "s_add_u32 s26, s28, s29\n"
  "s_xor_b32 s29, s22, 1\n"
  "s_lshl_b32 s29, s29, 20\n"
  "s_add_u32 s29, s29, s28\n"
  "s_add_u32 s29, s29, 2556\n"
  "v_lshrrev_b32 v9, 4, v14\n"
  "v_mul_u32_u24 v9, 2624, v9\n"
  "v_and_b32 v10, 15, v0\n"
  "v_lshl_add_u32 v9, v10, 2, v9\n"
  "v_add_u32 v224, s29, v9\n"
  "v_sub_u32 v9, 3, v15\n"
  "v_and_b32 v9, 3, v9\n"
  "v_mul_u32_u24 v9, 2624, v9\n"
  "v_lshrrev_b32 v10, 2, v15\n"
  "v_lshlrev_b32 v10, 3, v10\n"
  "v_sub_u32 v9, v9, v10\n"
  "v_lshl_add_u32 v9, v16, 4, v9\n"
  "s_lshl_b32 s29, s22, 5\n"
  "s_sub_u32 s28, s28, s29\n"
  "s_add_u32 s28, s28, 472\n"
  "v_add_u32 v213, s28, v9\n"
  "s_mul_i32 s28, s21, 7680\n"
  "s_add_u32 s28, s28, 33280\n"
  "v_lshrrev_b32 v9, 2, v17\n"
  "v_mul_u32_u24 v9, 80, v9\n"
  "v_and_b32 v10, 3, v0\n"
  "v_lshl_add_u32 v9, v10, 4, v9\n"
  "v_add_u32 v211, s28, v9\n"
  "v_and_b32 v18, 15, v0\n"
  "v_bfe_u32 v19, v0, 4, 2\n"
  "v_lshl_add_u32 v9, v18, 1, v19\n"
  "v_lshlrev_b32 v9, 3, v9\n"
  "s_mul_i32 s14, s22, 640\n"
  "s_add_u32 s15, s14, s28\n"
  "v_add_u32 v10, 1004, v9\n"
  "v_lshrrev_b32 v11, 5, v10\n"
  "v_lshlrev_b32 v11, 4, v11\n"
  "v_lshl_add_u32 v10, v10, 1, s15\n"
  "v_add_u32 v166, v10, v11\n"
  "v_add_u32 v10, 1008, v9\n"
  "v_lshrrev_b32 v11, 5, v10\n"
  "v_lshlrev_b32 v11, 4, v11\n"
  "v_lshl_add_u32 v10, v10, 1, s15\n"
  "v_add_u32 v167, v10, v11\n"
  "s_lshl_b32 s14, s14, 1\n"
  "s_sub_u32 s14, 1920, s14\n"
  "v_add_u32 v168, s14, v166\n"
  "v_add_u32 v169, s14, v167\n"
  "v_lshlrev_b32 v9, 3, v19\n"
  "v_mov_b32 v10, 0xfe00\n"
  "v_lshlrev_b32 v10, v18, v10\n"
  "v_lshrrev_b32 v10, v9, v10\n"
  "v_mov_b32 v11, 0x1ff\n"
  "v_lshlrev_b32 v11, v18, v11\n"
  "v_lshrrev_b32 v11, v9, v11\n"
  "v_bfe_u32 v9, v10, 0, 2\n"
  "v_mul_u32_u24 v9, 0x8001, v9\n"
  "v_and_b32 v9, 0x10001, v9\n"
  "v_mul_u32_u24 v192, 0x3f80, v9\n"
  "v_bfe_u32 v9, v10, 2, 2\n"
  "v_mul_u32_u24 v9, 0x8001, v9\n"
  "v_and_b32 v9, 0x10001, v9\n"
  "v_mul_u32_u24 v193, 0x3f80, v9\n"
  "v_bfe_u32 v9, v10, 4, 2\n"
  "v_mul_u32_u24 v9, 0x8001, v9\n"
  "v_and_b32 v9, 0x10001, v9\n"
  "v_mul_u32_u24 v194, 0x3f80, v9\n"
  "v_bfe_u32 v9, v10, 6, 2\n"
  "v_mul_u32_u24 v9, 0x8001, v9\n"
  "v_and_b32 v9, 0x10001, v9\n"
  "v_mul_u32_u24 v195, 0x3f80, v9\n"
  "v_bfe_u32 v9, v11, 0, 2\n"
  "v_mul_u32_u24 v9, 0x8001, v9\n"
  "v_and_b32 v9, 0x10001, v9\n"
  "v_mul_u32_u24 v196, 0x3f80, v9\n"
  "v_bfe_u32 v9, v11, 2, 2\n"
  "v_mul_u32_u24 v9, 0x8001, v9\n"
  "v_and_b32 v9, 0x10001, v9\n"
  "v_mul_u32_u24 v197, 0x3f80, v9\n"
  "v_bfe_u32 v9, v11, 4, 2\n"
  "v_mul_u32_u24 v9, 0x8001, v9\n"
  "v_and_b32 v9, 0x10001, v9\n"
  "v_mul_u32_u24 v198, 0x3f80, v9\n"
  "v_bfe_u32 v9, v11, 6, 2\n"
  "v_mul_u32_u24 v9, 0x8001, v9\n"
  "v_and_b32 v9, 0x10001, v9\n"
  "v_mul_u32_u24 v199, 0x3f80, v9\n"
  "v_and_b32 v9, 3, v0\n"
  "v_bfe_u32 v10, v0, 2, 1\n"
  "v_lshl_add_u32 v9, v9, 1, v10\n"
  "v_and_or_b32 v9, v0, 8, v9\n"
  "v_lshlrev_b32 v10, 2, v19\n"
  "v_sub_u32 v9, v9, v10\n"
  "s_lshl_b32 s7, s19, 9\n"
  "s_add_u32 s7, s7, 21504\n"
  "s_lshl_b32 s14, s22, 6\n"
  "s_sub_u32 s15, s7, s14\n"
  "s_add_u32 s15, s15, 116\n"
  "v_lshl_add_u32 v170, v9, 2, s15\n"
  "s_add_u32 s15, s7, s14\n"
  "s_add_u32 s15, s15, 52\n"
  "v_lshl_add_u32 v171, v9, 2, s15\n"
  "s_mul_i32 s14, s21, 6656\n"
  "s_add_u32 s14, s14, 69636\n"
  "v_lshl_add_u32 v200, v14, 3, s14\n"
  "v_add_u32 v201, 3328, v200\n"
  "s_sub_u32 s28, s28, s29\n"
  "s_sub_u32 s28, s28, 48\n"
  "v_mul_u32_u24 v9, 80, v15\n"
  "v_lshl_add_u32 v9, v16, 4, v9\n"
  "v_add_u32 v214, s28, v9\n"
  "v_mul_u32_u24 v9, 80, v1\n"
  "v_lshl_add_u32 v9, v2, 3, v9\n"
  "v_add_u32 v212, 35840, v9\n"
  "v_and_b32 v9, 3, v1\n"
  "v_lshl_add_u32 v9, v9, 1, v16\n"
  "v_lshl_add_u32 v9, v9, 3, v2\n"
  "s_lshl_b32 s28, s19, 10\n"
  "s_add_u32 s28, s28, 49152\n"
  "v_lshl_add_u32 v216, v9, 4, s28\n"
  "v_add_u32 v204, 4160, v245\n"
  "v_lshlrev_b32 v9, 1, v17\n"
  "v_sub_u32 v203, 1279, v9\n"
  "s_or_b32 s28, s18, s19\n"
  "s_cselect_b32 s16, 0x1000000, s16\n"
  "v_cvt_f32_i32 v144, v203\n"
  "s_mul_i32 s28, s21, 6656\n"
  "s_add_u32 s28, s28, 67584\n"
  "s_sub_u32 s29, s28, 68096\n"
  "v_mov_b32 v146, s29\n"
  "s_add_u32 s29, s28, 2688\n"
  "v_mov_b32 v147, s29\n"
  "s_add_u32 s29, s28, 2048\n"
  "s_sub_u32 s28, s28, 4096\n"
  "v_add_f32 v148, 0x00000000, v144\n"
  "v_add_f32 v149, 0xbf800000, v144\n"
  "v_cvt_pk_bf16_f32 v148, v148, v149\n"
  "v_mad_u32_u16 v176, v148, 4, v146\n"
  "v_mad_u32_u16 v177, v148, 4, v146 op_sel:[1,0,0,0]\n"
  "v_add_f32 v148, 0xc0000000, v144\n"
  "v_add_f32 v149, 0xc3800000, v144\n"
  "v_cvt_pk_bf16_f32 v148, v148, v149\n"
  "v_mad_u32_u16 v178, v148, 4, v146\n"
  "v_mad_u32_u16 v179, v148, 4, v146 op_sel:[1,0,0,0]\n"
  "v_add_f32 v148, 0xc3808000, v144\n"
  "v_add_f32 v149, 0xc3810000, v144\n"
  "v_cvt_pk_bf16_f32 v148, v148, v149\n"
  "v_mad_u32_u16 v180, v148, 4, v146\n"
  "v_mad_u32_u16 v181, v148, 4, v146 op_sel:[1,0,0,0]\n"
  "v_add_f32 v148, 0xc4000000, v144\n"
  "v_add_f32 v149, 0xc4004000, v144\n"
  "v_cvt_pk_bf16_f32 v148, v148, v149\n"
  "v_mad_u32_u16 v182, v148, 4, v146\n"
  "v_mad_u32_u16 v183, v148, 4, v146 op_sel:[1,0,0,0]\n"
  "v_add_f32 v148, 0xc4008000, v144\n"
  "v_add_f32 v149, 0xc4400000, v144\n"
  "v_cvt_pk_bf16_f32 v148, v148, v149\n"
  "v_mad_u32_u16 v184, v148, 4, v146\n"
  "v_mad_u32_u16 v185, v148, 4, v146 op_sel:[1,0,0,0]\n"
  "v_add_f32 v148, 0xc4404000, v144\n"
  "v_add_f32 v149, 0xc4408000, v144\n"
  "v_cvt_pk_bf16_f32 v148, v148, v149\n"
  "v_mad_u32_u16 v186, v148, 4, v146\n"
  "v_mad_u32_u16 v187, v148, 4, v146 op_sel:[1,0,0,0]\n"
  "v_cmp_lt_u32 vcc, s29, v176\n"
  "s_nop 1\n"
  "v_cndmask_b32 v176, v176, v147, vcc\n"
  "v_cmp_lt_u32 vcc, s29, v177\n"
  "s_nop 1\n"
  "v_cndmask_b32 v177, v177, v147, vcc\n"
  "v_cmp_lt_u32 vcc, s29, v178\n"
  "s_nop 1\n"
  "v_cndmask_b32 v178, v178, v147, vcc\n"
  "v_lshl_add_u32 v188, v203, 2, s28\n"
  "v_add_u32 v189, -4, v188\n"
  "v_add_u32 v190, -8, v188\n"
  "s_mov_b32 s38, 11534336\n"
  "s_mov_b32 s39, 0x20000\n"
  "s_mov_b32 s42, 3670016\n"
  "s_mov_b32 s43, 0x20000\n"
  "v_mov_b32 v152, 0\n"
  "v_mov_b32 v153, 0\n"
  "v_mov_b32 v154, 0\n"
  "v_mov_b32 v155, 0\n"
  "v_mov_b32 v156, 67568\n"
  "ds_write_b128 v156, v[152:155]\n"
  "s_waitcnt lgkmcnt(0)\n"
  "s_lshl_b32 s29, s21, 4\n"
  "s_lshr_b32 s6, s6, s29\n"
  "s_lshl_b32 s6, s6, 16\n"
  "s_lshr_b32 s31, s31, s29\n"
  "s_lshl_b32 s31, s31, 16\n"
  "v_cmp_eq_u32 vcc, 0, v17\n"
  "v_mov_b32 v9, s31\n"
  "s_nop 0\n"
  "v_cndmask_b32 v215, 0, v9, vcc\n"
  "s_add_u32 s66, s66, 1\n"
  "s_addc_u32 s67, s67, 0\n"
  "v_mov_b32 v30, s6\n"
  "v_mov_b32 v220, s66\n"
  "v_mov_b32 v221, s67\n"
  "s_mov_b32 s29, 0x3fb8aa3b\n"
  "s_mov_b32 s44, 0x3a000000\n"
  "v_max_f32 v30, 0xc3480000, v30\n"
  "v_min_f32 v30, 0x43480000, v30\n"
  "v_mul_f32 v31, s29, v30\n"
  "v_fma_f32 v32, v30, s29, -v31\n"
  "v_rndne_f32 v33, v31\n"
  "v_fmac_f32 v32, 0x32a5705f, v30\n"
  "v_sub_f32 v31, v31, v33\n"
  "v_add_f32 v31, v31, v32\n"
  "v_exp_f32 v31, v31\n"
  "v_cvt_i32_f32 v33, v33\n"
  "s_waitcnt vmcnt(7)\n"
  "v_ldexp_f32 v31, v31, v33\n"
  "v_lshlrev_b32 v34, 16, v250\n"
  "v_cvt_pk_bf16_f32 v31, v31, v31\n"
  "v_and_b32 v35, 0xffff0000, v250\n"
  "v_lshlrev_b32 v30, 16, v31\n"
  "v_lshlrev_b32 v36, 16, v251\n"
  "v_and_b32 v37, 0xffff0000, v251\n"
  "v_mul_f32 v34, v34, v30\n"
  "v_mul_f32 v35, v35, v30\n"
  "v_mul_f32 v36, v36, v30\n"
  "v_mul_f32 v37, v37, v30\n"
  "v_mul_f32 v34, s29, v34\n"
  "v_mul_f32 v35, s29, v35\n"
  "v_mul_f32 v38, 0.15915494, v36\n"
  "v_mul_f32 v39, 0.15915494, v37\n"
  "v_fma_f32 v40, v36, 0.15915494, -v38\n"
  "v_fma_f32 v41, v37, 0.15915494, -v39\n"
  "v_fmac_f32 v40, 0x31dc9c88, v36\n"
  "v_fmac_f32 v41, 0x31dc9c88, v37\n"
  "s_mov_b32 m0, s27\n"
  "v_max_f32 v46, v34, v35\n"
  "v_cmp_nle_f32 s[46:47], v46, s44\n"
  "v_mul_f32 v42, 0x42000000, v38\n"
  "v_mul_f32 v43, 0x42000000, v39\n"
  "v_mul_f32 v44, 0x42000000, v34\n"
  "v_mul_f32 v45, 0x42000000, v35\n"
  "v_fract_f32 v42, v42\n"
  "v_fract_f32 v43, v43\n"
  "v_exp_f32 v44, v44\n"
  "v_exp_f32 v45, v45\n"
  "v_fmac_f32 v42, 0x42000000, v40\n"
  "v_fmac_f32 v43, 0x42000000, v41\n"
  "v_cos_f32 v84, v42\n"
  "v_sin_f32 v85, v42\n"
  "v_cos_f32 v86, v43\n"
  "v_sin_f32 v87, v43\n"
  "v_mul_f32 v84, v84, v44\n"
  "v_mul_f32 v85, v85, v44\n"
  "v_mul_f32 v86, v86, v45\n"
  "v_mul_f32 v87, v87, v45\n"
  "v_mul_f32 v42, 1.0, v38\n"
  "v_mul_f32 v43, 1.0, v39\n"
  "v_mul_f32 v44, 1.0, v34\n"
  "v_mul_f32 v45, 1.0, v35\n"
  "v_fract_f32 v42, v42\n"
  "v_fract_f32 v43, v43\n"
  "v_exp_f32 v44, v44\n"
  "v_exp_f32 v45, v45\n"
  "v_fmac_f32 v42, 1.0, v40\n"
  "v_fmac_f32 v43, 1.0, v41\n"
  "v_cos_f32 v64, v42\n"
  "v_sin_f32 v65, v42\n"
  "v_cos_f32 v66, v43\n"
  "v_sin_f32 v67, v43\n"
  "v_mul_f32 v64, v64, v44\n"
  "v_mul_f32 v65, v65, v44\n"
  "v_mul_f32 v66, v66, v45\n"
  "v_mul_f32 v67, v67, v45\n"
  "v_mul_f32 v68, v64, v64\n"
  "v_mul_f32 v69, v64, v65\n"
  "v_mul_f32 v70, v66, v66\n"
  "v_mul_f32 v71, v66, v67\n"
  "v_mul_f32 v88, v84, v84\n"
  "v_mul_f32 v89, v84, v85\n"
  "v_mul_f32 v90, v86, v86\n"
  "v_mul_f32 v91, v86, v87\n"
  "v_fma_f32 v68, -v65, v65, v68\n"
  "v_fmac_f32 v69, v65, v64\n"
  "v_fma_f32 v70, -v67, v67, v70\n"
  "v_fmac_f32 v71, v67, v66\n"
  "v_fma_f32 v88, -v85, v85, v88\n"
  "v_fmac_f32 v89, v85, v84\n"
  "v_fma_f32 v90, -v87, v87, v90\n"
  "v_fmac_f32 v91, v87, v86\n"
  "v_mul_f32 v72, v68, v68\n"
  "v_mul_f32 v73, v68, v69\n"
  "v_mul_f32 v74, v70, v70\n"
  "v_mul_f32 v75, v70, v71\n"
  "v_mul_f32 v92, v88, v88\n"
  "v_mul_f32 v93, v88, v89\n"
  "v_mul_f32 v94, v90, v90\n"
  "v_mul_f32 v95, v90, v91\n"
  "v_fma_f32 v72, -v69, v69, v72\n"
  "v_fmac_f32 v73, v69, v68\n"
  "v_fma_f32 v74, -v71, v71, v74\n"
  "v_fmac_f32 v75, v71, v70\n"
  "v_fma_f32 v92, -v89, v89, v92\n"
  "v_fmac_f32 v93, v89, v88\n"
  "v_fma_f32 v94, -v91, v91, v94\n"
  "v_fmac_f32 v95, v91, v90\n"
  "v_mul_f32 v76, v72, v72\n"
  "v_mul_f32 v77, v72, v73\n"
  "v_mul_f32 v78, v74, v74\n"
  "v_mul_f32 v79, v74, v75\n"
  "v_mul_f32 v96, v92, v92\n"
  "v_mul_f32 v97, v92, v93\n"
  "v_mul_f32 v98, v94, v94\n"
  "v_mul_f32 v99, v94, v95\n"
  "v_fma_f32 v76, -v73, v73, v76\n"
  "v_fmac_f32 v77, v73, v72\n"
  "v_fma_f32 v78, -v75, v75, v78\n"
  "v_fmac_f32 v79, v75, v74\n"
  "v_fma_f32 v96, -v93, v93, v96\n"
  "v_fmac_f32 v97, v93, v92\n"
  "v_fma_f32 v98, -v95, v95, v98\n"
  "v_fmac_f32 v99, v95, v94\n"
  "v_mul_f32 v80, v76, v76\n"
  "v_mul_f32 v81, v76, v77\n"
  "v_mul_f32 v82, v78, v78\n"
  "v_mul_f32 v83, v78, v79\n"
  "v_mul_f32 v100, v96, v96\n"
  "v_mul_f32 v101, v96, v97\n"
  "v_mul_f32 v102, v98, v98\n"
  "v_mul_f32 v103, v98, v99\n"
  "v_fma_f32 v80, -v77, v77, v80\n"
  "v_fmac_f32 v81, v77, v76\n"
  "v_fma_f32 v82, -v79, v79, v82\n"
  "v_fmac_f32 v83, v79, v78\n"
  "v_fma_f32 v100, -v97, v97, v100\n"
  "v_fmac_f32 v101, v97, v96\n"
  "v_fma_f32 v102, -v99, v99, v102\n"
  "v_fmac_f32 v103, v99, v98\n"
  "v_mul_f32 v104, v100, v100\n"
  "v_mul_f32 v105, v100, v101\n"
  "v_mul_f32 v106, v102, v102\n"
  "v_mul_f32 v107, v102, v103\n"
  "v_fma_f32 v104, -v101, v101, v104\n"
  "v_fmac_f32 v105, v101, v100\n"
  "v_fma_f32 v106, -v103, v103, v106\n"
  "v_fmac_f32 v107, v103, v102\n"
  "v_readfirstlane_b32 s45, v30\n"
  "s_cmp_lg_u64 s[46:47], 0\n"
  "s_cbranch_scc1 LW%=\n"
  "v_mov_b32 v0, 0x3c00\n"
  "v_cvt_pk_f16_f32 v2, v76, v77\n"
  "v_cvt_pk_f16_f32 v3, v78, v79\n"
  "v_cvt_pk_f16_f32 v4, v80, v81\n"
  "v_cvt_pk_f16_f32 v5, v82, v83\n"
  "v_cvt_pk_f16_f32 v8, v84, v85\n"
  "v_cvt_pk_f16_f32 v9, v86, v87\n"
  "v_cvt_pk_f16_f32 v16, v88, v89\n"
  "v_cvt_pk_f16_f32 v17, v90, v91\n"
  "v_cvt_pk_f16_f32 v32, v92, v93\n"
  "v_cvt_pk_f16_f32 v33, v94, v95\n"
  "ds_write_addtid_b32 v0 offset:0\n"
  "ds_write_addtid_b32 v0 offset:256\n"
  "ds_write_addtid_b32 v2 offset:528\n"
  "ds_write_addtid_b32 v3 offset:784\n"
  "ds_write_addtid_b32 v4 offset:1056\n"
  "ds_write_addtid_b32 v5 offset:1312\n"
  "ds_write_addtid_b32 v8 offset:2112\n"
  "ds_write_addtid_b32 v9 offset:2368\n"
  "ds_write_addtid_b32 v16 offset:4224\n"
  "ds_write_addtid_b32 v17 offset:4480\n"
  "ds_write_addtid_b32 v32 offset:8448\n"
  "ds_write_addtid_b32 v33 offset:8704\n"
  "v_pk_mul_f16 v6, v4, v2 op_sel_hi:[0,1]\n"
  "v_pk_mul_f16 v7, v5, v3 op_sel_hi:[0,1]\n"
  "v_pk_mul_f16 v10, v8, v2 op_sel_hi:[0,1]\n"
  "v_pk_mul_f16 v11, v9, v3 op_sel_hi:[0,1]\n"
  "v_pk_fma_f16 v6, v4, v2, v6 op_sel:[1,1,0] op_sel_hi:[1,0,1] neg_lo:[0,1,0]\n"
  "v_pk_fma_f16 v7, v5, v3, v7 op_sel:[1,1,0] op_sel_hi:[1,0,1] neg_lo:[0,1,0]\n"
  "v_pk_fma_f16 v10, v8, v2, v10 op_sel:[1,1,0] op_sel_hi:[1,0,1] neg_lo:[0,1,0]\n"
  "v_pk_fma_f16 v11, v9, v3, v11 op_sel:[1,1,0] op_sel_hi:[1,0,1] neg_lo:[0,1,0]\n"
  "v_pk_mul_f16 v12, v8, v4 op_sel_hi:[0,1]\n"
  "v_pk_mul_f16 v13, v9, v5 op_sel_hi:[0,1]\n"
  "v_pk_mul_f16 v14, v8, v6 op_sel_hi:[0,1]\n"
  "v_pk_mul_f16 v15, v9, v7 op_sel_hi:[0,1]\n"
  "v_pk_fma_f16 v12, v8, v4, v12 op_sel:[1,1,0] op_sel_hi:[1,0,1] neg_lo:[0,1,0]\n"
  "v_pk_fma_f16 v13, v9, v5, v13 op_sel:[1,1,0] op_sel_hi:[1,0,1] neg_lo:[0,1,0]\n"
  "v_pk_fma_f16 v14, v8, v6, v14 op_sel:[1,1,0] op_sel_hi:[1,0,1] neg_lo:[0,1,0]\n"
  "v_pk_fma_f16 v15, v9, v7, v15 op_sel:[1,1,0] op_sel_hi:[1,0,1] neg_lo:[0,1,0]\n"
  "v_pk_mul_f16 v18, v16, v2 op_sel_hi:[0,1]\n"
  "v_pk_mul_f16 v19, v17, v3 op_sel_hi:[0,1]\n"
  "v_pk_mul_f16 v24, v16, v8 op_sel_hi:[0,1]\n"
  "v_pk_mul_f16 v25, v17, v9 op_sel_hi:[0,1]\n"
  "v_pk_fma_f16 v18, v16, v2, v18 op_sel:[1,1,0] op_sel_hi:[1,0,1] neg_lo:[0,1,0]\n"
  "v_pk_fma_f16 v19, v17, v3, v19 op_sel:[1,1,0] op_sel_hi:[1,0,1] neg_lo:[0,1,0]\n"
  "v_pk_fma_f16 v24, v16, v8, v24 op_sel:[1,1,0] op_sel_hi:[1,0,1] neg_lo:[0,1,0]\n"
  "v_pk_fma_f16 v25, v17, v9, v25 op_sel:[1,1,0] op_sel_hi:[1,0,1] neg_lo:[0,1,0]\n"
  "v_pk_mul_f16 v20, v16, v4 op_sel_hi:[0,1]\n"
  "v_pk_mul_f16 v21, v17, v5 op_sel_hi:[0,1]\n"
  "v_pk_mul_f16 v22, v16, v6 op_sel_hi:[0,1]\n"
  "v_pk_mul_f16 v23, v17, v7 op_sel_hi:[0,1]\n"
  "v_pk_fma_f16 v20, v16, v4, v20 op_sel:[1,1,0] op_sel_hi:[1,0,1] neg_lo:[0,1,0]\n"
  "v_pk_fma_f16 v21, v17, v5, v21 op_sel:[1,1,0] op_sel_hi:[1,0,1] neg_lo:[0,1,0]\n"
  "v_pk_fma_f16 v22, v16, v6, v22 op_sel:[1,1,0] op_sel_hi:[1,0,1] neg_lo:[0,1,0]\n"
  "v_pk_fma_f16 v23, v17, v7, v23 op_sel:[1,1,0] op_sel_hi:[1,0,1] neg_lo:[0,1,0]\n"
  "v_pk_mul_f16 v26, v24, v2 op_sel_hi:[0,1]\n"
  "v_pk_mul_f16 v27, v25, v3 op_sel_hi:[0,1]\n"
  "v_pk_mul_f16 v28, v24, v4 op_sel_hi:[0,1]\n"
  "v_pk_mul_f16 v29, v25, v5 op_sel_hi:[0,1]\n"
  "v_pk_fma_f16 v26, v24, v2, v26 op_sel:[1,1,0] op_sel_hi:[1,0,1] neg_lo:[0,1,0]\n"
  "v_pk_fma_f16 v27, v25, v3, v27 op_sel:[1,1,0] op_sel_hi:[1,0,1] neg_lo:[0,1,0]\n"
  "v_pk_fma_f16 v28, v24, v4, v28 op_sel:[1,1,0] op_sel_hi:[1,0,1] neg_lo:[0,1,0]\n"
  "v_pk_fma_f16 v29, v25, v5, v29 op_sel:[1,1,0] op_sel_hi:[1,0,1] neg_lo:[0,1,0]\n"
  "v_pk_mul_f16 v30, v24, v6 op_sel_hi:[0,1]\n"
  "v_pk_mul_f16 v31, v25, v7 op_sel_hi:[0,1]\n"
  "v_pk_mul_f16 v34, v32, v2 op_sel_hi:[0,1]\n"
  "v_pk_mul_f16 v35, v33, v3 op_sel_hi:[0,1]\n"
  "v_pk_fma_f16 v30, v24, v6, v30 op_sel:[1,1,0] op_sel_hi:[1,0,1] neg_lo:[0,1,0]\n"
  "v_pk_fma_f16 v31, v25, v7, v31 op_sel:[1,1,0] op_sel_hi:[1,0,1] neg_lo:[0,1,0]\n"
  "v_pk_fma_f16 v34, v32, v2, v34 op_sel:[1,1,0] op_sel_hi:[1,0,1] neg_lo:[0,1,0]\n"
  "v_pk_fma_f16 v35, v33, v3, v35 op_sel:[1,1,0] op_sel_hi:[1,0,1] neg_lo:[0,1,0]\n"
  "v_pk_mul_f16 v36, v32, v4 op_sel_hi:[0,1]\n"
  "v_pk_mul_f16 v37, v33, v5 op_sel_hi:[0,1]\n"
  "v_pk_mul_f16 v40, v32, v8 op_sel_hi:[0,1]\n"
  "v_pk_mul_f16 v41, v33, v9 op_sel_hi:[0,1]\n"
  "v_pk_fma_f16 v36, v32, v4, v36 op_sel:[1,1,0] op_sel_hi:[1,0,1] neg_lo:[0,1,0]\n"
  "v_pk_fma_f16 v37, v33, v5, v37 op_sel:[1,1,0] op_sel_hi:[1,0,1] neg_lo:[0,1,0]\n"
  "v_pk_fma_f16 v40, v32, v8, v40 op_sel:[1,1,0] op_sel_hi:[1,0,1] neg_lo:[0,1,0]\n"
  "v_pk_fma_f16 v41, v33, v9, v41 op_sel:[1,1,0] op_sel_hi:[1,0,1] neg_lo:[0,1,0]\n"
  "v_pk_mul_f16 v38, v32, v6 op_sel_hi:[0,1]\n"
  "v_pk_mul_f16 v39, v33, v7 op_sel_hi:[0,1]\n"
  "v_pk_mul_f16 v42, v40, v2 op_sel_hi:[0,1]\n"
  "v_pk_mul_f16 v43, v41, v3 op_sel_hi:[0,1]\n"
  "v_pk_fma_f16 v38, v32, v6, v38 op_sel:[1,1,0] op_sel_hi:[1,0,1] neg_lo:[0,1,0]\n"
  "v_pk_fma_f16 v39, v33, v7, v39 op_sel:[1,1,0] op_sel_hi:[1,0,1] neg_lo:[0,1,0]\n"
  "v_pk_fma_f16 v42, v40, v2, v42 op_sel:[1,1,0] op_sel_hi:[1,0,1] neg_lo:[0,1,0]\n"
  "v_pk_fma_f16 v43, v41, v3, v43 op_sel:[1,1,0] op_sel_hi:[1,0,1] neg_lo:[0,1,0]\n"
  "v_pk_mul_f16 v44, v40, v4 op_sel_hi:[0,1]\n"
  "v_pk_mul_f16 v45, v41, v5 op_sel_hi:[0,1]\n"
  "v_pk_mul_f16 v48, v32, v16 op_sel_hi:[0,1]\n"
  "v_pk_mul_f16 v49, v33, v17 op_sel_hi:[0,1]\n"
  "v_pk_fma_f16 v44, v40, v4, v44 op_sel:[1,1,0] op_sel_hi:[1,0,1] neg_lo:[0,1,0]\n"
  "v_pk_fma_f16 v45, v41, v5, v45 op_sel:[1,1,0] op_sel_hi:[1,0,1] neg_lo:[0,1,0]\n"
  "v_pk_fma_f16 v48, v32, v16, v48 op_sel:[1,1,0] op_sel_hi:[1,0,1] neg_lo:[0,1,0]\n"
  "v_pk_fma_f16 v49, v33, v17, v49 op_sel:[1,1,0] op_sel_hi:[1,0,1] neg_lo:[0,1,0]\n"
  "v_pk_mul_f16 v46, v40, v6 op_sel_hi:[0,1]\n"
  "v_pk_mul_f16 v47, v41, v7 op_sel_hi:[0,1]\n"
  "v_pk_mul_f16 v50, v48, v2 op_sel_hi:[0,1]\n"
  "v_pk_mul_f16 v51, v49, v3 op_sel_hi:[0,1]\n"
  "v_pk_fma_f16 v46, v40, v6, v46 op_sel:[1,1,0] op_sel_hi:[1,0,1] neg_lo:[0,1,0]\n"
  "v_pk_fma_f16 v47, v41, v7, v47 op_sel:[1,1,0] op_sel_hi:[1,0,1] neg_lo:[0,1,0]\n"
  "v_pk_fma_f16 v50, v48, v2, v50 op_sel:[1,1,0] op_sel_hi:[1,0,1] neg_lo:[0,1,0]\n"
  "v_pk_fma_f16 v51, v49, v3, v51 op_sel:[1,1,0] op_sel_hi:[1,0,1] neg_lo:[0,1,0]\n"
  "v_pk_mul_f16 v52, v48, v4 op_sel_hi:[0,1]\n"
  "v_pk_mul_f16 v53, v49, v5 op_sel_hi:[0,1]\n"
  "v_pk_mul_f16 v56, v32, v24 op_sel_hi:[0,1]\n"
  "v_pk_mul_f16 v57, v33, v25 op_sel_hi:[0,1]\n"
  "v_pk_fma_f16 v52, v48, v4, v52 op_sel:[1,1,0] op_sel_hi:[1,0,1] neg_lo:[0,1,0]\n"
  "v_pk_fma_f16 v53, v49, v5, v53 op_sel:[1,1,0] op_sel_hi:[1,0,1] neg_lo:[0,1,0]\n"
  "v_pk_fma_f16 v56, v32, v24, v56 op_sel:[1,1,0] op_sel_hi:[1,0,1] neg_lo:[0,1,0]\n"
  "v_pk_fma_f16 v57, v33, v25, v57 op_sel:[1,1,0] op_sel_hi:[1,0,1] neg_lo:[0,1,0]\n"
  "v_pk_mul_f16 v54, v48, v6 op_sel_hi:[0,1]\n"
  "v_pk_mul_f16 v55, v49, v7 op_sel_hi:[0,1]\n"
  "v_pk_mul_f16 v58, v56, v2 op_sel_hi:[0,1]\n"
  "v_pk_mul_f16 v59, v57, v3 op_sel_hi:[0,1]\n"
  "v_pk_fma_f16 v54, v48, v6, v54 op_sel:[1,1,0] op_sel_hi:[1,0,1] neg_lo:[0,1,0]\n"
  "v_pk_fma_f16 v55, v49, v7, v55 op_sel:[1,1,0] op_sel_hi:[1,0,1] neg_lo:[0,1,0]\n"
  "v_pk_fma_f16 v58, v56, v2, v58 op_sel:[1,1,0] op_sel_hi:[1,0,1] neg_lo:[0,1,0]\n"
  "v_pk_fma_f16 v59, v57, v3, v59 op_sel:[1,1,0] op_sel_hi:[1,0,1] neg_lo:[0,1,0]\n"
  "v_pk_mul_f16 v60, v56, v4 op_sel_hi:[0,1]\n"
  "v_pk_mul_f16 v61, v57, v5 op_sel_hi:[0,1]\n"
  "v_pk_mul_f16 v62, v56, v6 op_sel_hi:[0,1]\n"
  "v_pk_mul_f16 v63, v57, v7 op_sel_hi:[0,1]\n"
  "v_pk_fma_f16 v60, v56, v4, v60 op_sel:[1,1,0] op_sel_hi:[1,0,1] neg_lo:[0,1,0]\n"
  "v_pk_fma_f16 v61, v57, v5, v61 op_sel:[1,1,0] op_sel_hi:[1,0,1] neg_lo:[0,1,0]\n"
  "v_pk_fma_f16 v62, v56, v6, v62 op_sel:[1,1,0] op_sel_hi:[1,0,1] neg_lo:[0,1,0]\n"
  "v_pk_fma_f16 v63, v57, v7, v63 op_sel:[1,1,0] op_sel_hi:[1,0,1] neg_lo:[0,1,0]\n"
  "ds_write_addtid_b32 v6 offset:1584\n"
  "ds_write_addtid_b32 v7 offset:1840\n"
  "ds_write_addtid_b32 v10 offset:2640\n"
  "ds_write_addtid_b32 v11 offset:2896\n"
  "ds_write_addtid_b32 v12 offset:3168\n"
  "ds_write_addtid_b32 v13 offset:3424\n"
  "ds_write_addtid_b32 v14 offset:3696\n"
  "ds_write_addtid_b32 v15 offset:3952\n"
  "ds_write_addtid_b32 v18 offset:4752\n"
  "ds_write_addtid_b32 v19 offset:5008\n"
  "ds_write_addtid_b32 v24 offset:6336\n"
  "ds_write_addtid_b32 v25 offset:6592\n"
  "ds_write_addtid_b32 v20 offset:5280\n"
  "ds_write_addtid_b32 v21 offset:5536\n"
  "ds_write_addtid_b32 v22 offset:5808\n"
  "ds_write_addtid_b32 v23 offset:6064\n"
  "ds_write_addtid_b32 v26 offset:6864\n"
  "ds_write_addtid_b32 v27 offset:7120\n"
  "ds_write_addtid_b32 v28 offset:7392\n"
  "ds_write_addtid_b32 v29 offset:7648\n"
  "ds_write_addtid_b32 v30 offset:7920\n"
  "ds_write_addtid_b32 v31 offset:8176\n"
  "ds_write_addtid_b32 v34 offset:8976\n"
  "ds_write_addtid_b32 v35 offset:9232\n"
  "ds_write_addtid_b32 v36 offset:9504\n"
  "ds_write_addtid_b32 v37 offset:9760\n"
  "ds_write_addtid_b32 v40 offset:10560\n"
  "ds_write_addtid_b32 v41 offset:10816\n"
  "ds_write_addtid_b32 v38 offset:10032\n"
  "ds_write_addtid_b32 v39 offset:10288\n"
  "ds_write_addtid_b32 v42 offset:11088\n"
  "ds_write_addtid_b32 v43 offset:11344\n"
  "ds_write_addtid_b32 v44 offset:11616\n"
  "ds_write_addtid_b32 v45 offset:11872\n"
  "ds_write_addtid_b32 v48 offset:12672\n"
  "ds_write_addtid_b32 v49 offset:12928\n"
  "ds_write_addtid_b32 v46 offset:12144\n"
  "ds_write_addtid_b32 v47 offset:12400\n"
  "ds_write_addtid_b32 v50 offset:13200\n"
  "ds_write_addtid_b32 v51 offset:13456\n"
  "ds_write_addtid_b32 v52 offset:13728\n"
  "ds_write_addtid_b32 v53 offset:13984\n"
  "ds_write_addtid_b32 v56 offset:14784\n"
  "ds_write_addtid_b32 v57 offset:15040\n"
  "ds_write_addtid_b32 v54 offset:14256\n"
  "ds_write_addtid_b32 v55 offset:14512\n"
  "ds_write_addtid_b32 v58 offset:15312\n"
  "ds_write_addtid_b32 v59 offset:15568\n"
  "ds_write_addtid_b32 v60 offset:15840\n"
  "ds_write_addtid_b32 v61 offset:16096\n"
  "ds_write_addtid_b32 v62 offset:16368\n"
  "ds_write_addtid_b32 v63 offset:16624\n"
  "ds_read_b128 v[0:3], v205 offset:0\n"
  "ds_read_b128 v[4:7], v205 offset:32\n"
  "ds_read_b128 v[8:11], v205 offset:64\n"
  "ds_read_b128 v[12:15], v205 offset:96\n"
  "ds_read_b128 v[16:19], v205 offset:128\n"
  "ds_read_b128 v[20:23], v205 offset:160\n"
  "ds_read_b128 v[24:27], v205 offset:192\n"
  "ds_read_b128 v[28:31], v205 offset:224\n"
  "ds_read_b128 v[32:35], v205 offset:256\n"
  "ds_read_b128 v[36:39], v205 offset:288\n"
  "ds_read_b128 v[40:43], v205 offset:320\n"
  "ds_read_b128 v[44:47], v205 offset:352\n"
  "ds_read_b128 v[48:51], v205 offset:384\n"
  "ds_read_b128 v[52:55], v205 offset:416\n"
  "ds_read_b128 v[56:59], v205 offset:448\n"
  "ds_read_b128 v[60:63], v205 offset:480\n"
  "s_waitcnt vmcnt(3)\n"
  "v_lshlrev_b32 v140, 16, v252\n"
  "v_lshlrev_b32 v141, 16, v253\n"
  "v_and_b32 v142, 0xffff0000, v252\n"
  "v_and_b32 v143, 0xffff0000, v253\n"
  "v_lshlrev_b32 v144, 16, v254\n"
  "v_lshlrev_b32 v145, 16, v255\n"
  "v_and_b32 v146, 0xffff0000, v254\n"
  "v_and_b32 v147, 0xffff0000, v255\n"
  "v_mul_f32 v172, v144, v140\n"
  "v_mul_f32 v173, v144, v141\n"
  "v_mul_f32 v174, v146, v142\n"
  "v_mul_f32 v175, v146, v143\n"
  "v_fma_f32 v172, -v145, v141, v172\n"
  "v_fmac_f32 v173, v145, v140\n"
  "v_fma_f32 v174, -v147, v143, v174\n"
  "v_fmac_f32 v175, v147, v142\n"
  "v_max3_f32 v108, |v172|, |v173|, |v174|\n"
  "v_max_f32_e64 v108, v108, |v175|\n"
  "v_cvt_pk_f16_f32 v110, v64, -v65\n"
  "v_cvt_pk_f16_f32 v111, v66, -v67\n"
  "v_max_f32_dpp v108, v108, v108 row_shr:1 row_mask:0xf bank_mask:0xf\n"
  "v_cvt_pk_f16_f32 v112, v68, -v69\n"
  "v_cvt_pk_f16_f32 v113, v70, -v71\n"
  "v_max_f32_dpp v108, v108, v108 row_shr:2 row_mask:0xf bank_mask:0xf\n"
  "v_cvt_pk_f16_f32 v114, v72, -v73\n"
  "v_cvt_pk_f16_f32 v115, v74, -v75\n"
  "v_max_f32_dpp v108, v108, v108 row_shr:4 row_mask:0xf bank_mask:0xf\n"
  "v_cvt_pk_f16_f32 v116, v96, -v97\n"
  "v_cvt_pk_f16_f32 v117, v98, -v99\n"
  "v_max_f32_dpp v108, v108, v108 row_shr:8 row_mask:0xf bank_mask:0xf\n"
  "v_cvt_pk_f16_f32 v118, v100, -v101\n"
  "v_cvt_pk_f16_f32 v119, v102, -v103\n"
  "v_max_f32_dpp v108, v108, v108 row_bcast:15 row_mask:0xa bank_mask:0xf\n"
  "v_cvt_pk_f16_f32 v120, v104, -v105\n"
  "v_cvt_pk_f16_f32 v121, v106, -v107\n"
  "v_max_f32_dpp v108, v108, v108 row_bcast:31 row_mask:0xc bank_mask:0xf\n"
  "s_nop 0\n"
  "v_readlane_b32 s44, v108, 63\n"
  "s_nop 1\n"
  "s_lshr_b32 s44, s44, 23\n"
  "s_max_u32 s44, s44, 27\n"
  "s_min_u32 s44, s44, 240\n"
  "s_sub_u32 s28, 267, s44\n"
  "s_lshl_b32 s28, s28, 23\n"
  "s_sub_u32 s44, s44, 13\n"
  "s_lshl_b32 s44, s44, 23\n"
  "v_mul_f32 v172, s28, v172\n"
  "v_mul_f32 v173, s28, v173\n"
  "v_mul_f32 v174, s28, v174\n"
  "v_mul_f32 v175, s28, v175\n"
  "v_mov_b32 v150, s44\n"
  "v_mul_f32 v150, s45, v150\n"
  "v_cvt_pk_f16_f32 v64, v172, -v173\n"
  "v_cvt_pk_f16_f32 v65, v174, -v175\n"
  "ds_write_addtid_b32 v64 offset:0\n"
  "ds_write_addtid_b32 v65 offset:256\n"
  "s_waitcnt vmcnt(0)\n"
  "ds_write2_b32 v163, v232, v233 offset0:0 offset1:65\n"
  "ds_write2_b32 v163, v234, v235 offset0:130 offset1:195\n"
  "ds_write2_b32 v163, v236, v237 offset0:32 offset1:97\n"
  "ds_write2_b32 v163, v238, v239 offset0:162 offset1:227\n"
  "v_pk_mul_f16 v66, v64, v110 op_sel_hi:[0,1]\n"
  "v_pk_mul_f16 v67, v65, v111 op_sel_hi:[0,1]\n"
  "v_pk_mul_f16 v68, v64, v112 op_sel_hi:[0,1]\n"
  "v_pk_mul_f16 v69, v65, v113 op_sel_hi:[0,1]\n"
  "v_pk_fma_f16 v66, v64, v110, v66 op_sel:[1,1,0] op_sel_hi:[1,0,1] neg_lo:[0,1,0]\n"
  "v_pk_fma_f16 v67, v65, v111, v67 op_sel:[1,1,0] op_sel_hi:[1,0,1] neg_lo:[0,1,0]\n"
  "v_pk_fma_f16 v68, v64, v112, v68 op_sel:[1,1,0] op_sel_hi:[1,0,1] neg_lo:[0,1,0]\n"
  "v_pk_fma_f16 v69, v65, v113, v69 op_sel:[1,1,0] op_sel_hi:[1,0,1] neg_lo:[0,1,0]\n"
  "s_waitcnt lgkmcnt(0)\n"
  "s_barrier\n"
  "ds_read2_b32 v[132:133], v164 offset0:0 offset1:1\n"
  "ds_read2_b32 v[134:135], v164 offset0:2 offset1:3\n"
  "ds_read2_b32 v[136:137], v165 offset0:0 offset1:1\n"
  "ds_read2_b32 v[138:139], v165 offset0:2 offset1:3\n"
  "v_pk_mul_f16 v72, v64, v114 op_sel_hi:[0,1]\n"
  "v_pk_mul_f16 v73, v65, v115 op_sel_hi:[0,1]\n"
  "v_pk_mul_f16 v70, v66, v112 op_sel_hi:[0,1]\n"
  "v_pk_mul_f16 v71, v67, v113 op_sel_hi:[0,1]\n"
  "v_pk_fma_f16 v72, v64, v114, v72 op_sel:[1,1,0] op_sel_hi:[1,0,1] neg_lo:[0,1,0]\n"
  "v_pk_fma_f16 v73, v65, v115, v73 op_sel:[1,1,0] op_sel_hi:[1,0,1] neg_lo:[0,1,0]\n"
  "v_pk_fma_f16 v70, v66, v112, v70 op_sel:[1,1,0] op_sel_hi:[1,0,1] neg_lo:[0,1,0]\n"
  "v_pk_fma_f16 v71, v67, v113, v71 op_sel:[1,1,0] op_sel_hi:[1,0,1] neg_lo:[0,1,0]\n"
  "s_add_u32 s15, s24, 131072\n"
  "s_waitcnt lgkmcnt(0)\n"
  "buffer_store_dwordx4 v[132:135], v246, s[36:39], s24 offen sc1\n"
  "buffer_store_dwordx4 v[136:139], v246, s[36:39], s15 offen sc1\n"
  "v_pk_mul_f16 v74, v66, v114 op_sel_hi:[0,1]\n"
  "v_pk_mul_f16 v75, v67, v115 op_sel_hi:[0,1]\n"
  "v_pk_mul_f16 v76, v68, v114 op_sel_hi:[0,1]\n"
  "v_pk_mul_f16 v77, v69, v115 op_sel_hi:[0,1]\n"
  "v_pk_fma_f16 v74, v66, v114, v74 op_sel:[1,1,0] op_sel_hi:[1,0,1] neg_lo:[0,1,0]\n"
  "v_pk_fma_f16 v75, v67, v115, v75 op_sel:[1,1,0] op_sel_hi:[1,0,1] neg_lo:[0,1,0]\n"
  "v_pk_fma_f16 v76, v68, v114, v76 op_sel:[1,1,0] op_sel_hi:[1,0,1] neg_lo:[0,1,0]\n"
  "v_pk_fma_f16 v77, v69, v115, v77 op_sel:[1,1,0] op_sel_hi:[1,0,1] neg_lo:[0,1,0]\n"
  "v_pk_mul_f16 v80, v64, v116 op_sel_hi:[0,1]\n"
  "v_pk_mul_f16 v81, v65, v117 op_sel_hi:[0,1]\n"
  "v_pk_mul_f16 v78, v70, v114 op_sel_hi:[0,1]\n"
  "v_pk_mul_f16 v79, v71, v115 op_sel_hi:[0,1]\n"
  "v_pk_fma_f16 v80, v64, v116, v80 op_sel:[1,1,0] op_sel_hi:[1,0,1] neg_lo:[0,1,0]\n"
  "v_pk_fma_f16 v81, v65, v117, v81 op_sel:[1,1,0] op_sel_hi:[1,0,1] neg_lo:[0,1,0]\n"
  "v_pk_fma_f16 v78, v70, v114, v78 op_sel:[1,1,0] op_sel_hi:[1,0,1] neg_lo:[0,1,0]\n"
  "v_pk_fma_f16 v79, v71, v115, v79 op_sel:[1,1,0] op_sel_hi:[1,0,1] neg_lo:[0,1,0]\n"
  "v_pk_mul_f16 v82, v68, v116 op_sel_hi:[0,1]\n"
  "v_pk_mul_f16 v83, v69, v117 op_sel_hi:[0,1]\n"
  "v_pk_mul_f16 v84, v72, v116 op_sel_hi:[0,1]\n"
  "v_pk_mul_f16 v85, v73, v117 op_sel_hi:[0,1]\n"
  "v_pk_fma_f16 v82, v68, v116, v82 op_sel:[1,1,0] op_sel_hi:[1,0,1] neg_lo:[0,1,0]\n"
  "v_pk_fma_f16 v83, v69, v117, v83 op_sel:[1,1,0] op_sel_hi:[1,0,1] neg_lo:[0,1,0]\n"
  "v_pk_fma_f16 v84, v72, v116, v84 op_sel:[1,1,0] op_sel_hi:[1,0,1] neg_lo:[0,1,0]\n"
  "v_pk_fma_f16 v85, v73, v117, v85 op_sel:[1,1,0] op_sel_hi:[1,0,1] neg_lo:[0,1,0]\n"
  "v_pk_mul_f16 v88, v64, v118 op_sel_hi:[0,1]\n"
  "v_pk_mul_f16 v89, v65, v119 op_sel_hi:[0,1]\n"
  "v_pk_mul_f16 v86, v76, v116 op_sel_hi:[0,1]\n"
  "v_pk_mul_f16 v87, v77, v117 op_sel_hi:[0,1]\n"
  "v_pk_fma_f16 v88, v64, v118, v88 op_sel:[1,1,0] op_sel_hi:[1,0,1] neg_lo:[0,1,0]\n"
  "v_pk_fma_f16 v89, v65, v119, v89 op_sel:[1,1,0] op_sel_hi:[1,0,1] neg_lo:[0,1,0]\n"
  "v_pk_fma_f16 v86, v76, v116, v86 op_sel:[1,1,0] op_sel_hi:[1,0,1] neg_lo:[0,1,0]\n"
  "v_pk_fma_f16 v87, v77, v117, v87 op_sel:[1,1,0] op_sel_hi:[1,0,1] neg_lo:[0,1,0]\n"
  "v_pk_mul_f16 v90, v72, v118 op_sel_hi:[0,1]\n"
  "v_pk_mul_f16 v91, v73, v119 op_sel_hi:[0,1]\n"
  "v_pk_mul_f16 v92, v80, v118 op_sel_hi:[0,1]\n"
  "v_pk_mul_f16 v93, v81, v119 op_sel_hi:[0,1]\n"
  "v_pk_fma_f16 v90, v72, v118, v90 op_sel:[1,1,0] op_sel_hi:[1,0,1] neg_lo:[0,1,0]\n"
  "v_pk_fma_f16 v91, v73, v119, v91 op_sel:[1,1,0] op_sel_hi:[1,0,1] neg_lo:[0,1,0]\n"
  "v_pk_fma_f16 v92, v80, v118, v92 op_sel:[1,1,0] op_sel_hi:[1,0,1] neg_lo:[0,1,0]\n"
  "v_pk_fma_f16 v93, v81, v119, v93 op_sel:[1,1,0] op_sel_hi:[1,0,1] neg_lo:[0,1,0]\n"
  "v_pk_mul_f16 v96, v64, v120 op_sel_hi:[0,1]\n"
  "v_pk_mul_f16 v97, v65, v121 op_sel_hi:[0,1]\n"
  "v_pk_mul_f16 v94, v84, v118 op_sel_hi:[0,1]\n"
  "v_pk_mul_f16 v95, v85, v119 op_sel_hi:[0,1]\n"
  "v_pk_fma_f16 v96, v64, v120, v96 op_sel:[1,1,0] op_sel_hi:[1,0,1] neg_lo:[0,1,0]\n"
  "v_pk_fma_f16 v97, v65, v121, v97 op_sel:[1,1,0] op_sel_hi:[1,0,1] neg_lo:[0,1,0]\n"
  "v_pk_fma_f16 v94, v84, v118, v94 op_sel:[1,1,0] op_sel_hi:[1,0,1] neg_lo:[0,1,0]\n"
  "v_pk_fma_f16 v95, v85, v119, v95 op_sel:[1,1,0] op_sel_hi:[1,0,1] neg_lo:[0,1,0]\n"
  "v_pk_mul_f16 v98, v80, v120 op_sel_hi:[0,1]\n"
  "v_pk_mul_f16 v99, v81, v121 op_sel_hi:[0,1]\n"
  "v_pk_mul_f16 v100, v88, v120 op_sel_hi:[0,1]\n"
  "v_pk_mul_f16 v101, v89, v121 op_sel_hi:[0,1]\n"
  "v_pk_fma_f16 v98, v80, v120, v98 op_sel:[1,1,0] op_sel_hi:[1,0,1] neg_lo:[0,1,0]\n"
  "v_pk_fma_f16 v99, v81, v121, v99 op_sel:[1,1,0] op_sel_hi:[1,0,1] neg_lo:[0,1,0]\n"
  "v_pk_fma_f16 v100, v88, v120, v100 op_sel:[1,1,0] op_sel_hi:[1,0,1] neg_lo:[0,1,0]\n"
  "v_pk_fma_f16 v101, v89, v121, v101 op_sel:[1,1,0] op_sel_hi:[1,0,1] neg_lo:[0,1,0]\n"
  "v_pk_mul_f16 v104, v96, v120 op_sel_hi:[0,1]\n"
  "v_pk_mul_f16 v105, v97, v121 op_sel_hi:[0,1]\n"
  "v_pk_mul_f16 v102, v92, v120 op_sel_hi:[0,1]\n"
  "v_pk_mul_f16 v103, v93, v121 op_sel_hi:[0,1]\n"
  "v_pk_fma_f16 v104, v96, v120, v104 op_sel:[1,1,0] op_sel_hi:[1,0,1] neg_lo:[0,1,0]\n"
  "v_pk_fma_f16 v105, v97, v121, v105 op_sel:[1,1,0] op_sel_hi:[1,0,1] neg_lo:[0,1,0]\n"
  "v_pk_fma_f16 v102, v92, v120, v102 op_sel:[1,1,0] op_sel_hi:[1,0,1] neg_lo:[0,1,0]\n"
  "v_pk_fma_f16 v103, v93, v121, v103 op_sel:[1,1,0] op_sel_hi:[1,0,1] neg_lo:[0,1,0]\n"
  "ds_write_addtid_b32 v66 offset:528\n"
  "ds_write_addtid_b32 v67 offset:784\n"
  "ds_write_addtid_b32 v68 offset:1056\n"
  "ds_write_addtid_b32 v69 offset:1312\n"
  "ds_write_addtid_b32 v72 offset:2112\n"
  "ds_write_addtid_b32 v73 offset:2368\n"
  "ds_write_addtid_b32 v70 offset:1584\n"
  "ds_write_addtid_b32 v71 offset:1840\n"
  "ds_write_addtid_b32 v74 offset:2640\n"
  "ds_write_addtid_b32 v75 offset:2896\n"
  "ds_write_addtid_b32 v76 offset:3168\n"
  "ds_write_addtid_b32 v77 offset:3424\n"
  "ds_write_addtid_b32 v80 offset:4224\n"
  "ds_write_addtid_b32 v81 offset:4480\n"
  "ds_write_addtid_b32 v78 offset:3696\n"
  "ds_write_addtid_b32 v79 offset:3952\n"
  "ds_write_addtid_b32 v82 offset:4752\n"
  "ds_write_addtid_b32 v83 offset:5008\n"
  "ds_write_addtid_b32 v84 offset:5280\n"
  "ds_write_addtid_b32 v85 offset:5536\n"
  "ds_write_addtid_b32 v88 offset:6336\n"
  "ds_write_addtid_b32 v89 offset:6592\n"
  "ds_write_addtid_b32 v86 offset:5808\n"
  "ds_write_addtid_b32 v87 offset:6064\n"
  "ds_write_addtid_b32 v90 offset:6864\n"
  "ds_write_addtid_b32 v91 offset:7120\n"
  "ds_write_addtid_b32 v92 offset:7392\n"
  "ds_write_addtid_b32 v93 offset:7648\n"
  "ds_write_addtid_b32 v96 offset:8448\n"
  "ds_write_addtid_b32 v97 offset:8704\n"
  "ds_write_addtid_b32 v94 offset:7920\n"
  "ds_write_addtid_b32 v95 offset:8176\n"
  "ds_write_addtid_b32 v98 offset:8976\n"
  "ds_write_addtid_b32 v99 offset:9232\n"
  "ds_write_addtid_b32 v100 offset:9504\n"
  "ds_write_addtid_b32 v101 offset:9760\n"
  "ds_write_addtid_b32 v104 offset:10560\n"
  "ds_write_addtid_b32 v105 offset:10816\n"
  "ds_write_addtid_b32 v102 offset:10032\n"
  "ds_write_addtid_b32 v103 offset:10288\n"
  "ds_read_b128 v[64:67], v205 offset:0\n"
  "ds_read_b128 v[68:71], v205 offset:32\n"
  "ds_read_b128 v[72:75], v205 offset:64\n"
  "ds_read_b128 v[76:79], v205 offset:96\n"
  "ds_read_b128 v[80:83], v205 offset:128\n"
  "ds_read_b128 v[84:87], v205 offset:160\n"
  "ds_read_b128 v[88:91], v205 offset:192\n"
  "ds_read_b128 v[92:95], v205 offset:224\n"
  "ds_read_b128 v[96:99], v205 offset:256\n"
  "ds_read_b128 v[100:103], v205 offset:288\n"
  "ds_read_b128 v[104:107], v205 offset:320\n"
  "ds_read_b128 v[108:111], v205 offset:352\n"
  "ds_read_b128 v[112:115], v205 offset:384\n"
  "ds_read_b128 v[116:119], v205 offset:416\n"
  "ds_read_b128 v[120:123], v205 offset:448\n"
  "ds_read_b128 v[124:127], v205 offset:480\n"
  "s_barrier\n"
  "s_waitcnt vmcnt(0)\n"
  "buffer_store_dwordx2 v[220:221], v218, s[36:39], s8 offen sc1\n"
  "s_waitcnt lgkmcnt(14)\n"
  "v_mfma_f32_32x32x16_f16 v[128:143], v[64:67], v[0:3], 0\n"
  "v_mfma_f32_32x32x16_f16 v[128:143], v[68:71], v[4:7], v[128:143]\n"
  "s_waitcnt lgkmcnt(13)\n"
  "v_mfma_f32_32x32x16_f16 v[128:143], v[72:75], v[8:11], v[128:143]\n"
  "s_waitcnt lgkmcnt(12)\n"
  "v_mfma_f32_32x32x16_f16 v[128:143], v[76:79], v[12:15], v[128:143]\n"
  "s_waitcnt lgkmcnt(11)\n"
  "v_mfma_f32_32x32x16_f16 v[128:143], v[80:83], v[16:19], v[128:143]\n"
  "s_waitcnt lgkmcnt(10)\n"
  "v_mfma_f32_32x32x16_f16 v[128:143], v[84:87], v[20:23], v[128:143]\n"
  "s_waitcnt lgkmcnt(9)\n"
  "v_mfma_f32_32x32x16_f16 v[128:143], v[88:91], v[24:27], v[128:143]\n"
  "s_waitcnt lgkmcnt(8)\n"
  "v_mfma_f32_32x32x16_f16 v[128:143], v[92:95], v[28:31], v[128:143]\n"
  "s_waitcnt lgkmcnt(7)\n"
  "v_mfma_f32_32x32x16_f16 v[128:143], v[96:99], v[32:35], v[128:143]\n"
  "s_waitcnt lgkmcnt(6)\n"
  "v_mfma_f32_32x32x16_f16 v[128:143], v[100:103], v[36:39], v[128:143]\n"
  "s_waitcnt lgkmcnt(5)\n"
  "v_mfma_f32_32x32x16_f16 v[128:143], v[104:107], v[40:43], v[128:143]\n"
  "s_waitcnt lgkmcnt(4)\n"
  "v_mfma_f32_32x32x16_f16 v[128:143], v[108:111], v[44:47], v[128:143]\n"
  "s_waitcnt lgkmcnt(3)\n"
  "v_mfma_f32_32x32x16_f16 v[128:143], v[112:115], v[48:51], v[128:143]\n"
  "s_waitcnt lgkmcnt(2)\n"
  "v_mfma_f32_32x32x16_f16 v[128:143], v[116:119], v[52:55], v[128:143]\n"
  "s_waitcnt lgkmcnt(1)\n"
  "v_mfma_f32_32x32x16_f16 v[128:143], v[120:123], v[56:59], v[128:143]\n"
  "s_waitcnt lgkmcnt(0)\n"
  "v_mfma_f32_32x32x16_f16 v[128:143], v[124:127], v[60:63], v[128:143]\n"
  "LJ%=:\n"
  "s_nop 11\n"
  "v_mul_f32 v129, v150, v129\n"
  "v_mul_f32 v130, v150, v130\n"
  "v_mul_f32 v131, v150, v131\n"
  "v_mul_f32 v132, v150, v132\n"
  "v_mul_f32 v133, v150, v133\n"
  "v_mul_f32 v134, v150, v134\n"
  "v_mul_f32 v135, v150, v135\n"
  "v_mul_f32 v136, v150, v136\n"
  "v_mul_f32 v137, v150, v137\n"
  "v_mul_f32 v138, v150, v138\n"
  "v_mul_f32 v139, v150, v139\n"
  "v_fma_f32 v128, v150, v128, v215\n"
  "s_add_u32 s14, s10, 4096\n"
  "ds_write_b128 v206, v[128:131]\n"
  "ds_write_b64 v207, v[132:133]\n"
  "ds_write_b64 v208, v[134:135]\n"
  "ds_write_b32 v209, v136 offset:0\n"
  "ds_write_b32 v209, v137 offset:128\n"
  "ds_write_b32 v209, v138 offset:256\n"
  "ds_write_b32 v209, v139 offset:384\n"
  "ds_write_b32 v209, v152 offset:640\n"
  "s_waitcnt lgkmcnt(0)\n"
  "s_barrier\n"
  "s_mov_b32 m0, s7\n"
  "ds_read2_b32 v[32:33], v200 offset1:1\n"
  "ds_read2_b32 v[34:35], v201 offset1:1\n"
  "ds_write_addtid_b32 v152\n"
  "ds_read2st64_b32 v[0:1], v176 offset1:13\n"
  "ds_read2st64_b32 v[2:3], v177 offset1:13\n"
  "ds_read2st64_b32 v[4:5], v178 offset1:13\n"
  "ds_read2st64_b32 v[6:7], v179 offset1:13\n"
  "ds_read2st64_b32 v[8:9], v180 offset1:13\n"
  "ds_read2st64_b32 v[10:11], v181 offset1:13\n"
  "ds_read2st64_b32 v[12:13], v182 offset1:13\n"
  "ds_read2st64_b32 v[14:15], v183 offset1:13\n"
  "ds_read2st64_b32 v[16:17], v184 offset1:13\n"
  "ds_read2st64_b32 v[18:19], v185 offset1:13\n"
  "ds_read2st64_b32 v[20:21], v186 offset1:13\n"
  "ds_read2st64_b32 v[22:23], v187 offset1:13\n"
  "ds_read2st64_b32 v[24:25], v188 offset1:13\n"
  "ds_read2st64_b32 v[26:27], v189 offset1:13\n"
  "ds_read2st64_b32 v[28:29], v190 offset1:13\n"
  "s_waitcnt lgkmcnt(14)\n"
  "v_add_f32 v32, v32, v34\n"
  "v_add_f32 v33, v33, v35\n"
  "v_cvt_pk_bf16_f32 v32, v32, v33\n"
  "ds_write_addtid_b32 v32 offset:128\n"
  "ds_write_b32 v224, v152\n"
  "s_mov_b32 m0, s26\n"
  "buffer_load_dwordx2 v[222:223], v219, s[36:39], s9 offen sc1\n"
  "s_waitcnt lgkmcnt(14)\n"
  "v_add_f32 v0, v0, v1\n"
  "v_add_f32 v2, v2, v3\n"
  "v_add_f32 v4, v4, v5\n"
  "v_cvt_pk_bf16_f32 v64, v0, v2\n"
  "v_cvt_pk_bf16_f32 v65, v2, v4\n"
  "s_waitcnt lgkmcnt(11)\n"
  "v_add_f32 v6, v6, v7\n"
  "v_add_f32 v8, v8, v9\n"
  "v_add_f32 v10, v10, v11\n"
  "v_cvt_pk_bf16_f32 v66, v6, v8\n"
  "v_cvt_pk_bf16_f32 v67, v8, v10\n"
  "s_waitcnt lgkmcnt(8)\n"
  "v_add_f32 v12, v12, v13\n"
  "v_add_f32 v14, v14, v15\n"
  "v_add_f32 v16, v16, v17\n"
  "v_cvt_pk_bf16_f32 v68, v12, v14\n"
  "v_cvt_pk_bf16_f32 v69, v14, v16\n"
  "s_waitcnt lgkmcnt(5)\n"
  "v_add_f32 v18, v18, v19\n"
  "v_add_f32 v20, v20, v21\n"
  "v_add_f32 v22, v22, v23\n"
  "v_cvt_pk_bf16_f32 v70, v18, v20\n"
  "v_cvt_pk_bf16_f32 v71, v20, v22\n"
  "s_waitcnt lgkmcnt(2)\n"
  "v_add_f32 v24, v24, v25\n"
  "v_add_f32 v26, v26, v27\n"
  "v_add_f32 v28, v28, v29\n"
  "v_cvt_pk_bf16_f32 v72, v24, v26\n"
  "v_cvt_pk_bf16_f32 v73, v26, v28\n"
  "ds_write_addtid_b32 v64 offset:0\n"
  "ds_write_addtid_b32 v65 offset:2624\n"
  "ds_write_addtid_b32 v64 offset:5244\n"
  "ds_write_addtid_b32 v65 offset:7868\n"
  "ds_write_addtid_b32 v66 offset:512\n"
  "ds_write_addtid_b32 v67 offset:3136\n"
  "ds_write_addtid_b32 v66 offset:5756\n"
  "ds_write_addtid_b32 v67 offset:8380\n"
  "ds_write_addtid_b32 v68 offset:1024\n"
  "ds_write_addtid_b32 v69 offset:3648\n"
  "ds_write_addtid_b32 v68 offset:6268\n"
  "ds_write_addtid_b32 v69 offset:8892\n"
  "ds_write_addtid_b32 v70 offset:1536\n"
  "ds_write_addtid_b32 v71 offset:4160\n"
  "ds_write_addtid_b32 v70 offset:6780\n"
  "ds_write_addtid_b32 v71 offset:9404\n"
  "ds_write_addtid_b32 v72 offset:2048\n"
  "ds_write_addtid_b32 v73 offset:4672\n"
  "ds_write_addtid_b32 v72 offset:7292\n"
  "ds_write_addtid_b32 v73 offset:9916\n"
  "ds_read2_b32 v[108:109], v170 offset0:3 offset1:2\n"
  "ds_read2_b32 v[110:111], v170 offset0:1 offset1:0\n"
  "ds_read2_b32 v[112:113], v170 offset0:19 offset1:18\n"
  "ds_read2_b32 v[114:115], v170 offset0:17 offset1:16\n"
  "ds_read2_b32 v[116:117], v170 offset0:35 offset1:34\n"
  "ds_read2_b32 v[118:119], v170 offset0:33 offset1:32\n"
  "ds_read2_b32 v[120:121], v170 offset0:51 offset1:50\n"
  "ds_read2_b32 v[122:123], v170 offset0:49 offset1:48\n"
  "ds_read2_b32 v[124:125], v171 offset0:3 offset1:2\n"
  "ds_read2_b32 v[126:127], v171 offset0:1 offset1:0\n"
  "ds_read2_b32 v[128:129], v171 offset0:19 offset1:18\n"
  "ds_read2_b32 v[130:131], v171 offset0:17 offset1:16\n"
  "ds_write_b128 v211, v[152:155]\n"
  "s_waitcnt vmcnt(0)\n"
  "v_xor_b32 v222, v222, v220\n"
  "v_xor_b32 v223, v223, v221\n"
  "v_or_b32 v222, v222, v223\n"
  "v_cmp_ne_u32 vcc, 0, v222\n"
  "s_cbranch_vccz LDu%=\n"
  "s_mov_b32 s13, 0x40000\n"
  "LPu%=:\n"
  "s_sleep 8\n"
  "buffer_load_dwordx2 v[222:223], v219, s[36:39], s9 offen sc1\n"
  "s_waitcnt vmcnt(0)\n"
  "v_xor_b32 v222, v222, v220\n"
  "v_xor_b32 v223, v223, v221\n"
  "v_or_b32 v222, v222, v223\n"
  "v_cmp_ne_u32 vcc, 0, v222\n"
  "s_cbranch_vccz LDu%=\n"
  "s_sub_u32 s13, s13, 1\n"
  "s_cmp_lg_u32 s13, 0\n"
  "s_cbranch_scc1 LPu%=\n"
  "LDu%=:\n"
  "buffer_load_dwordx4 v[144:147], v217, s[36:39], s10 offen sc1\n"
  "buffer_load_dwordx4 v[148:151], v217, s[36:39], s14 offen sc1\n"
  "s_mov_b32 s14, 0x05040100\n"
  "s_mov_b32 s15, 0x07060302\n"
  "s_waitcnt vmcnt(0)\n"
  "v_perm_b32 v100, v145, v144, s14\n"
  "v_perm_b32 v101, v147, v146, s14\n"
  "ds_write_b64 v212, v[100:101] offset:0\n"
  "v_perm_b32 v102, v145, v144, s15\n"
  "v_perm_b32 v103, v147, v146, s15\n"
  "ds_write_b64 v212, v[102:103] offset:7680\n"
  "v_perm_b32 v104, v149, v148, s14\n"
  "v_perm_b32 v105, v151, v150, s14\n"
  "ds_write_b64 v212, v[104:105] offset:2560\n"
  "v_perm_b32 v106, v149, v148, s15\n"
  "v_perm_b32 v107, v151, v150, s15\n"
  "ds_write_b64 v212, v[106:107] offset:10240\n"
  "s_waitcnt lgkmcnt(0)\n"
  "s_barrier\n"
  "buffer_store_dwordx2 v[220:221], v218, s[36:39], s16 offen sc1\n"
  "s_mov_b64 exec, 12\n"
  "global_load_dword v227, v226, s[36:37] sc1\n"
  "s_mov_b64 exec, -1\n"
  "ds_read_b64 v[48:49], v166\n"
  "ds_read_b64 v[50:51], v167\n"
  "ds_read_b64 v[52:53], v168\n"
  "ds_read_b64 v[54:55], v169\n"
  "ds_read_b64 v[0:1], v213 offset:0\n"
  "ds_read_b64 v[2:3], v213 offset:8\n"
  "ds_read_b128 v[8:11], v214 offset:2560\n"
  "ds_read_b64 v[12:13], v213 offset:64\n"
  "ds_read_b64 v[14:15], v213 offset:72\n"
  "ds_read_b128 v[20:23], v214 offset:2640\n"
  "ds_read_b64 v[24:25], v213 offset:128\n"
  "ds_read_b64 v[26:27], v213 offset:136\n"
  "ds_read_b128 v[32:35], v214 offset:2720\n"
  "ds_read_b128 v[28:31], v214 offset:160\n"
  "s_waitcnt lgkmcnt(10)\n"
  "v_mfma_f32_16x16x32_bf16 v[132:135], v[48:51], v[192:195], 0\n"
  "v_mfma_f32_16x16x32_bf16 v[136:139], v[48:51], v[196:199], 0\n"
  "v_mfma_f32_16x16x32_bf16 v[140:143], v[52:55], v[192:195], 0\n"
  "v_mfma_f32_16x16x32_bf16 v[144:147], v[52:55], v[196:199], 0\n"
  "s_nop 5\n"
  "v_cvt_pk_bf16_f32 v56, v132, v136\n"
  "v_cvt_pk_bf16_f32 v57, v133, v137\n"
  "v_cvt_pk_bf16_f32 v58, v134, v138\n"
  "v_cvt_pk_bf16_f32 v59, v135, v139\n"
  "v_cvt_pk_bf16_f32 v60, v140, v144\n"
  "v_cvt_pk_bf16_f32 v61, v141, v145\n"
  "v_cvt_pk_bf16_f32 v62, v142, v146\n"
  "v_cvt_pk_bf16_f32 v63, v143, v147\n"
  "v_mfma_f32_16x16x32_bf16 v[80:83], v[108:111], v[56:59], 0\n"
  "v_mfma_f32_16x16x32_bf16 v[84:87], v[112:115], v[56:59], 0\n"
  "v_mfma_f32_16x16x32_bf16 v[88:91], v[116:119], v[56:59], 0\n"
  "v_mfma_f32_16x16x32_bf16 v[92:95], v[120:123], v[56:59], 0\n"
  "v_mfma_f32_16x16x32_bf16 v[88:91], v[124:127], v[60:63], v[88:91]\n"
  "v_mfma_f32_16x16x32_bf16 v[92:95], v[128:131], v[60:63], v[92:95]\n"
  "s_setprio 0\n"
  "s_waitcnt lgkmcnt(7)\n"
  "s_nop 3\n"
  "v_mfma_f32_32x32x16_bf16 v[80:95], v[8:11], v[0:3], v[80:95]\n"
  "ds_read_b64 v[36:37], v213 offset:192\n"
  "ds_read_b64 v[38:39], v213 offset:200\n"
  "ds_read_b128 v[44:47], v214 offset:2800\n"
  "ds_read_b128 v[40:43], v214 offset:240\n"
  "s_waitcnt lgkmcnt(8)\n"
  "v_mfma_f32_32x32x16_bf16 v[80:95], v[20:23], v[12:15], v[80:95]\n"
  "ds_read_b64 v[0:1], v213 offset:256\n"
  "ds_read_b64 v[2:3], v213 offset:264\n"
  "ds_read_b128 v[8:11], v214 offset:2880\n"
  "ds_read_b128 v[4:7], v214 offset:320\n"
  "s_waitcnt lgkmcnt(8)\n"
  "v_mfma_f32_32x32x16_bf16 v[80:95], v[32:35], v[24:27], v[80:95]\n"
  "ds_read_b64 v[12:13], v213 offset:320\n"
  "ds_read_b64 v[14:15], v213 offset:328\n"
  "v_mfma_f32_32x32x16_bf16 v[64:79], v[28:31], v[24:27], 0\n"
  "ds_read_b128 v[20:23], v214 offset:2960\n"
  "ds_read_b128 v[16:19], v214 offset:400\n"
  "s_waitcnt lgkmcnt(8)\n"
  "v_mfma_f32_32x32x16_bf16 v[80:95], v[44:47], v[36:39], v[80:95]\n"
  "ds_read_b64 v[24:25], v213 offset:384\n"
  "ds_read_b64 v[26:27], v213 offset:392\n"
  "v_mfma_f32_32x32x16_bf16 v[64:79], v[40:43], v[36:39], v[64:79]\n"
  "ds_read_b128 v[32:35], v214 offset:3040\n"
  "ds_read_b128 v[28:31], v214 offset:480\n"
  "s_waitcnt lgkmcnt(8)\n"
  "v_mfma_f32_32x32x16_bf16 v[80:95], v[8:11], v[0:3], v[80:95]\n"
  "ds_read_b64 v[36:37], v213 offset:448\n"
  "ds_read_b64 v[38:39], v213 offset:456\n"
  "v_mfma_f32_32x32x16_bf16 v[64:79], v[4:7], v[0:3], v[64:79]\n"
  "ds_read_b128 v[44:47], v214 offset:3120\n"
  "ds_read_b128 v[40:43], v214 offset:560\n"
  "s_waitcnt lgkmcnt(8)\n"
  "v_mfma_f32_32x32x16_bf16 v[80:95], v[20:23], v[12:15], v[80:95]\n"
  "ds_read_b64 v[0:1], v213 offset:512\n"
  "ds_read_b64 v[2:3], v213 offset:520\n"
  "v_mfma_f32_32x32x16_bf16 v[64:79], v[16:19], v[12:15], v[64:79]\n"
  "ds_read_b128 v[8:11], v214 offset:3200\n"
  "ds_read_b128 v[4:7], v214 offset:640\n"
  "s_waitcnt lgkmcnt(8)\n"
  "v_mfma_f32_32x32x16_bf16 v[80:95], v[32:35], v[24:27], v[80:95]\n"
  "ds_read_b64 v[12:13], v213 offset:576\n"
  "ds_read_b64 v[14:15], v213 offset:584\n"
  "v_mfma_f32_32x32x16_bf16 v[64:79], v[28:31], v[24:27], v[64:79]\n"
  "ds_read_b128 v[20:23], v214 offset:3280\n"
  "ds_read_b128 v[16:19], v214 offset:720\n"
  "s_waitcnt lgkmcnt(8)\n"
  "v_mfma_f32_32x32x16_bf16 v[80:95], v[44:47], v[36:39], v[80:95]\n"
  "ds_read_b64 v[24:25], v213 offset:640\n"
  "ds_read_b64 v[26:27], v213 offset:648\n"
  "v_mfma_f32_32x32x16_bf16 v[64:79], v[40:43], v[36:39], v[64:79]\n"
  "ds_read_b128 v[32:35], v214 offset:3360\n"
  "ds_read_b128 v[28:31], v214 offset:800\n"
  "s_waitcnt lgkmcnt(8)\n"
  "v_mfma_f32_32x32x16_bf16 v[80:95], v[8:11], v[0:3], v[80:95]\n"
  "ds_read_b64 v[36:37], v213 offset:704\n"
  "ds_read_b64 v[38:39], v213 offset:712\n"
  "v_mfma_f32_32x32x16_bf16 v[64:79], v[4:7], v[0:3], v[64:79]\n"
  "ds_read_b128 v[44:47], v214 offset:3440\n"
  "ds_read_b128 v[40:43], v214 offset:880\n"
  "s_waitcnt lgkmcnt(8)\n"
  "v_mfma_f32_32x32x16_bf16 v[80:95], v[20:23], v[12:15], v[80:95]\n"
  "ds_read_b64 v[0:1], v213 offset:768\n"
  "ds_read_b64 v[2:3], v213 offset:776\n"
  "v_mfma_f32_32x32x16_bf16 v[64:79], v[16:19], v[12:15], v[64:79]\n"
  "ds_read_b128 v[8:11], v214 offset:3520\n"
  "ds_read_b128 v[4:7], v214 offset:960\n"
  "s_waitcnt lgkmcnt(8)\n"
  "v_mfma_f32_32x32x16_bf16 v[80:95], v[32:35], v[24:27], v[80:95]\n"
  "ds_read_b64 v[12:13], v213 offset:832\n"
  "ds_read_b64 v[14:15], v213 offset:840\n"
  "v_mfma_f32_32x32x16_bf16 v[64:79], v[28:31], v[24:27], v[64:79]\n"
  "ds_read_b128 v[20:23], v214 offset:3600\n"
  "ds_read_b128 v[16:19], v214 offset:1040\n"
  "s_waitcnt lgkmcnt(8)\n"
  "v_mfma_f32_32x32x16_bf16 v[80:95], v[44:47], v[36:39], v[80:95]\n"
  "ds_read_b64 v[24:25], v213 offset:896\n"
  "ds_read_b64 v[26:27], v213 offset:904\n"
  "v_mfma_f32_32x32x16_bf16 v[64:79], v[40:43], v[36:39], v[64:79]\n"
  "ds_read_b128 v[32:35], v214 offset:3680\n"
  "ds_read_b128 v[28:31], v214 offset:1120\n"
  "s_waitcnt lgkmcnt(8)\n"
  "v_mfma_f32_32x32x16_bf16 v[80:95], v[8:11], v[0:3], v[80:95]\n"
  "ds_read_b64 v[36:37], v213 offset:960\n"
  "ds_read_b64 v[38:39], v213 offset:968\n"
  "v_mfma_f32_32x32x16_bf16 v[64:79], v[4:7], v[0:3], v[64:79]\n"
  "ds_read_b128 v[44:47], v214 offset:3760\n"
  "ds_read_b128 v[40:43], v214 offset:1200\n"
  "s_waitcnt lgkmcnt(8)\n"
  "v_mfma_f32_32x32x16_bf16 v[80:95], v[20:23], v[12:15], v[80:95]\n"
  "ds_read_b64 v[0:1], v213 offset:1024\n"
  "ds_read_b64 v[2:3], v213 offset:1032\n"
  "v_mfma_f32_32x32x16_bf16 v[64:79], v[16:19], v[12:15], v[64:79]\n"
  "ds_read_b128 v[8:11], v214 offset:3840\n"
  "ds_read_b128 v[4:7], v214 offset:1280\n"
  "s_waitcnt lgkmcnt(8)\n"
  "v_mfma_f32_32x32x16_bf16 v[80:95], v[32:35], v[24:27], v[80:95]\n"
  "ds_read_b64 v[12:13], v213 offset:1088\n"
  "ds_read_b64 v[14:15], v213 offset:1096\n"
  "v_mfma_f32_32x32x16_bf16 v[64:79], v[28:31], v[24:27], v[64:79]\n"
  "ds_read_b128 v[20:23], v214 offset:3920\n"
  "ds_read_b128 v[16:19], v214 offset:1360\n"
  "s_waitcnt lgkmcnt(8)\n"
  "v_mfma_f32_32x32x16_bf16 v[80:95], v[44:47], v[36:39], v[80:95]\n"
  "ds_read_b64 v[24:25], v213 offset:1152\n"
  "ds_read_b64 v[26:27], v213 offset:1160\n"
  "v_mfma_f32_32x32x16_bf16 v[64:79], v[40:43], v[36:39], v[64:79]\n"
  "ds_read_b128 v[32:35], v214 offset:4000\n"
  "ds_read_b128 v[28:31], v214 offset:1440\n"
  "s_waitcnt lgkmcnt(8)\n"
  "v_mfma_f32_32x32x16_bf16 v[80:95], v[8:11], v[0:3], v[80:95]\n"
  "ds_read_b64 v[36:37], v213 offset:1216\n"
  "ds_read_b64 v[38:39], v213 offset:1224\n"
  "v_mfma_f32_32x32x16_bf16 v[64:79], v[4:7], v[0:3], v[64:79]\n"
  "ds_read_b128 v[44:47], v214 offset:4080\n"
  "ds_read_b128 v[40:43], v214 offset:1520\n"
  "s_waitcnt lgkmcnt(8)\n"
  "v_mfma_f32_32x32x16_bf16 v[80:95], v[20:23], v[12:15], v[80:95]\n"
  "ds_read_b64 v[0:1], v213 offset:1280\n"
  "ds_read_b64 v[2:3], v213 offset:1288\n"
  "v_mfma_f32_32x32x16_bf16 v[64:79], v[16:19], v[12:15], v[64:79]\n"
  "ds_read_b128 v[8:11], v214 offset:4160\n"
  "ds_read_b128 v[4:7], v214 offset:1600\n"
  "s_waitcnt lgkmcnt(8)\n"
  "v_mfma_f32_32x32x16_bf16 v[80:95], v[32:35], v[24:27], v[80:95]\n"
  "ds_read_b64 v[12:13], v213 offset:1344\n"
  "ds_read_b64 v[14:15], v213 offset:1352\n"
  "v_mfma_f32_32x32x16_bf16 v[64:79], v[28:31], v[24:27], v[64:79]\n"
  "ds_read_b128 v[20:23], v214 offset:4240\n"
  "ds_read_b128 v[16:19], v214 offset:1680\n"
  "s_waitcnt lgkmcnt(8)\n"
  "v_mfma_f32_32x32x16_bf16 v[80:95], v[44:47], v[36:39], v[80:95]\n"
  "ds_read_b64 v[24:25], v213 offset:1408\n"
  "ds_read_b64 v[26:27], v213 offset:1416\n"
  "v_mfma_f32_32x32x16_bf16 v[64:79], v[40:43], v[36:39], v[64:79]\n"
  "ds_read_b128 v[32:35], v214 offset:4320\n"
  "ds_read_b128 v[28:31], v214 offset:1760\n"
  "s_waitcnt lgkmcnt(8)\n"
  "v_mfma_f32_32x32x16_bf16 v[80:95], v[8:11], v[0:3], v[80:95]\n"
  "ds_read_b64 v[36:37], v213 offset:1472\n"
  "ds_read_b64 v[38:39], v213 offset:1480\n"
  "v_mfma_f32_32x32x16_bf16 v[64:79], v[4:7], v[0:3], v[64:79]\n"
  "ds_read_b128 v[44:47], v214 offset:4400\n"
  "ds_read_b128 v[40:43], v214 offset:1840\n"
  "s_waitcnt lgkmcnt(8)\n"
  "v_mfma_f32_32x32x16_bf16 v[80:95], v[20:23], v[12:15], v[80:95]\n"
  "ds_read_b64 v[0:1], v213 offset:1536\n"
  "ds_read_b64 v[2:3], v213 offset:1544\n"
  "v_mfma_f32_32x32x16_bf16 v[64:79], v[16:19], v[12:15], v[64:79]\n"
  "ds_read_b128 v[8:11], v214 offset:4480\n"
  "ds_read_b128 v[4:7], v214 offset:1920\n"
  "s_waitcnt lgkmcnt(8)\n"
  "v_mfma_f32_32x32x16_bf16 v[80:95], v[32:35], v[24:27], v[80:95]\n"
  "ds_read_b64 v[12:13], v213 offset:1600\n"
  "ds_read_b64 v[14:15], v213 offset:1608\n"
  "v_mfma_f32_32x32x16_bf16 v[64:79], v[28:31], v[24:27], v[64:79]\n"
  "ds_read_b128 v[20:23], v214 offset:4560\n"
  "ds_read_b128 v[16:19], v214 offset:2000\n"
  "s_waitcnt lgkmcnt(8)\n"
  "v_mfma_f32_32x32x16_bf16 v[80:95], v[44:47], v[36:39], v[80:95]\n"
  "ds_read_b64 v[24:25], v213 offset:1664\n"
  "ds_read_b64 v[26:27], v213 offset:1672\n"
  "v_mfma_f32_32x32x16_bf16 v[64:79], v[40:43], v[36:39], v[64:79]\n"
  "ds_read_b128 v[32:35], v214 offset:4640\n"
  "ds_read_b128 v[28:31], v214 offset:2080\n"
  "s_waitcnt lgkmcnt(8)\n"
  "v_mfma_f32_32x32x16_bf16 v[80:95], v[8:11], v[0:3], v[80:95]\n"
  "ds_read_b64 v[36:37], v213 offset:1728\n"
  "ds_read_b64 v[38:39], v213 offset:1736\n"
  "v_mfma_f32_32x32x16_bf16 v[64:79], v[4:7], v[0:3], v[64:79]\n"
  "ds_read_b128 v[44:47], v214 offset:4720\n"
  "ds_read_b128 v[40:43], v214 offset:2160\n"
  "s_waitcnt lgkmcnt(8)\n"
  "v_mfma_f32_32x32x16_bf16 v[80:95], v[20:23], v[12:15], v[80:95]\n"
  "ds_read_b64 v[0:1], v213 offset:1792\n"
  "ds_read_b64 v[2:3], v213 offset:1800\n"
  "v_mfma_f32_32x32x16_bf16 v[64:79], v[16:19], v[12:15], v[64:79]\n"
  "ds_read_b128 v[8:11], v214 offset:4800\n"
  "ds_read_b128 v[4:7], v214 offset:2240\n"
  "s_waitcnt lgkmcnt(8)\n"
  "v_mfma_f32_32x32x16_bf16 v[80:95], v[32:35], v[24:27], v[80:95]\n"
  "ds_read_b64 v[12:13], v213 offset:1856\n"
  "ds_read_b64 v[14:15], v213 offset:1864\n"
  "v_mfma_f32_32x32x16_bf16 v[64:79], v[28:31], v[24:27], v[64:79]\n"
  "ds_read_b128 v[20:23], v214 offset:4880\n"
  "ds_read_b128 v[16:19], v214 offset:2320\n"
  "s_waitcnt lgkmcnt(8)\n"
  "v_mfma_f32_32x32x16_bf16 v[80:95], v[44:47], v[36:39], v[80:95]\n"
  "ds_read_b64 v[24:25], v213 offset:1920\n"
  "ds_read_b64 v[26:27], v213 offset:1928\n"
  "v_mfma_f32_32x32x16_bf16 v[64:79], v[40:43], v[36:39], v[64:79]\n"
  "ds_read_b128 v[32:35], v214 offset:4960\n"
  "ds_read_b128 v[28:31], v214 offset:2400\n"
  "s_waitcnt lgkmcnt(8)\n"
  "v_mfma_f32_32x32x16_bf16 v[80:95], v[8:11], v[0:3], v[80:95]\n"
  "ds_read_b64 v[36:37], v213 offset:1984\n"
  "ds_read_b64 v[38:39], v213 offset:1992\n"
  "v_mfma_f32_32x32x16_bf16 v[64:79], v[4:7], v[0:3], v[64:79]\n"
  "ds_read_b128 v[44:47], v214 offset:5040\n"
  "ds_read_b128 v[40:43], v214 offset:2480\n"
  "s_waitcnt lgkmcnt(8)\n"
  "v_mfma_f32_32x32x16_bf16 v[80:95], v[20:23], v[12:15], v[80:95]\n"
  "ds_read_b64 v[0:1], v213 offset:2048\n"
  "ds_read_b64 v[2:3], v213 offset:2056\n"
  "v_mfma_f32_32x32x16_bf16 v[64:79], v[16:19], v[12:15], v[64:79]\n"
  "ds_read_b128 v[8:11], v214 offset:5120\n"
  "ds_read_b128 v[4:7], v214 offset:2560\n"
  "s_waitcnt lgkmcnt(8)\n"
  "v_mfma_f32_32x32x16_bf16 v[80:95], v[32:35], v[24:27], v[80:95]\n"
  "ds_read_b64 v[12:13], v213 offset:2112\n"
  "ds_read_b64 v[14:15], v213 offset:2120\n"
  "v_mfma_f32_32x32x16_bf16 v[64:79], v[28:31], v[24:27], v[64:79]\n"
  "ds_read_b128 v[20:23], v214 offset:5200\n"
  "ds_read_b128 v[16:19], v214 offset:2640\n"
  "s_waitcnt lgkmcnt(8)\n"
  "v_mfma_f32_32x32x16_bf16 v[80:95], v[44:47], v[36:39], v[80:95]\n"
  "v_mfma_f32_32x32x16_bf16 v[64:79], v[40:43], v[36:39], v[64:79]\n"
  "s_waitcnt lgkmcnt(4)\n"
  "v_mfma_f32_32x32x16_bf16 v[80:95], v[8:11], v[0:3], v[80:95]\n"
  "v_mfma_f32_32x32x16_bf16 v[64:79], v[4:7], v[0:3], v[64:79]\n"
  "s_waitcnt lgkmcnt(0)\n"
  "v_mfma_f32_32x32x16_bf16 v[80:95], v[20:23], v[12:15], v[80:95]\n"
  "v_mfma_f32_32x32x16_bf16 v[64:79], v[16:19], v[12:15], v[64:79]\n"
  "s_setprio 2\n"
  "s_lshl_b32 m0, s19, 13\n"
  "s_add_u32 s14, s11, 4096\n"
  "s_nop 8\n"
  "ds_write_addtid_b32 v64 offset:49152\n"
  "ds_write_addtid_b32 v65 offset:49408\n"
  "ds_write_addtid_b32 v66 offset:49664\n"
  "ds_write_addtid_b32 v67 offset:49920\n"
  "ds_write_addtid_b32 v68 offset:50176\n"
  "ds_write_addtid_b32 v69 offset:50432\n"
  "ds_write_addtid_b32 v70 offset:50688\n"
  "ds_write_addtid_b32 v71 offset:50944\n"
  "ds_write_addtid_b32 v72 offset:51200\n"
  "ds_write_addtid_b32 v73 offset:51456\n"
  "ds_write_addtid_b32 v74 offset:51712\n"
  "ds_write_addtid_b32 v75 offset:51968\n"
  "ds_write_addtid_b32 v76 offset:52224\n"
  "ds_write_addtid_b32 v77 offset:52480\n"
  "ds_write_addtid_b32 v78 offset:52736\n"
  "ds_write_addtid_b32 v79 offset:52992\n"
  "ds_write_addtid_b32 v80 offset:53248\n"
  "ds_write_addtid_b32 v81 offset:53504\n"
  "ds_write_addtid_b32 v82 offset:53760\n"
  "ds_write_addtid_b32 v83 offset:54016\n"
  "ds_write_addtid_b32 v84 offset:54272\n"
  "ds_write_addtid_b32 v85 offset:54528\n"
  "ds_write_addtid_b32 v86 offset:54784\n"
  "ds_write_addtid_b32 v87 offset:55040\n"
  "ds_write_addtid_b32 v88 offset:55296\n"
  "ds_write_addtid_b32 v89 offset:55552\n"
  "ds_write_addtid_b32 v90 offset:55808\n"
  "ds_write_addtid_b32 v91 offset:56064\n"
  "ds_write_addtid_b32 v92 offset:56320\n"
  "ds_write_addtid_b32 v93 offset:56576\n"
  "ds_write_addtid_b32 v94 offset:56832\n"
  "ds_write_addtid_b32 v95 offset:57088\n"
  "s_waitcnt lgkmcnt(0)\n"
  "s_barrier\n"
  "ds_read_b128 v[0:3], v216 offset:0\n"
  "ds_read_b128 v[4:7], v216 offset:8192\n"
  "ds_read_b128 v[8:11], v216 offset:16384\n"
  "ds_read_b128 v[12:15], v216 offset:24576\n"
  "ds_read_b128 v[16:19], v216 offset:4096\n"
  "ds_read_b128 v[20:23], v216 offset:12288\n"
  "ds_read_b128 v[24:27], v216 offset:20480\n"
  "ds_read_b128 v[28:31], v216 offset:28672\n"
  "s_waitcnt lgkmcnt(4)\n"
  "v_add_f32 v0, v0, v4\n"
  "v_add_f32 v1, v1, v5\n"
  "v_add_f32 v2, v2, v6\n"
  "v_add_f32 v3, v3, v7\n"
  "v_add_f32 v8, v8, v12\n"
  "v_add_f32 v9, v9, v13\n"
  "v_add_f32 v10, v10, v14\n"
  "v_add_f32 v11, v11, v15\n"
  "v_cvt_pk_bf16_f32 v32, v0, v8\n"
  "v_cvt_pk_bf16_f32 v33, v1, v9\n"
  "v_cvt_pk_bf16_f32 v34, v2, v10\n"
  "v_cvt_pk_bf16_f32 v35, v3, v11\n"
  "buffer_store_dwordx4 v[32:35], v217, s[36:39], s11 offen sc1\n"
  "s_waitcnt lgkmcnt(0)\n"
  "v_add_f32 v16, v16, v20\n"
  "v_add_f32 v17, v17, v21\n"
  "v_add_f32 v18, v18, v22\n"
  "v_add_f32 v19, v19, v23\n"
  "v_add_f32 v24, v24, v28\n"
  "v_add_f32 v25, v25, v29\n"
  "v_add_f32 v26, v26, v30\n"
  "v_add_f32 v27, v27, v31\n"
  "v_cvt_pk_bf16_f32 v36, v16, v24\n"
  "v_cvt_pk_bf16_f32 v37, v17, v25\n"
  "v_cvt_pk_bf16_f32 v38, v18, v26\n"
  "v_cvt_pk_bf16_f32 v39, v19, v27\n"
  "buffer_store_dwordx4 v[36:39], v217, s[36:39], s14 offen sc1\n"
  "s_waitcnt vmcnt(0)\n"
  "buffer_store_dwordx2 v[220:221], v218, s[36:39], s12 offen sc1\n"
  "s_add_u32 s28, s25, 131072\n"
  "s_mov_b32 s29, 57344\n"
  "s_sleep 16\n"
  "buffer_load_dwordx2 v[222:223], v219, s[36:39], s23 offen sc1\n"
  "s_waitcnt vmcnt(0)\n"
  "v_xor_b32 v222, v222, v220\n"
  "v_xor_b32 v223, v223, v221\n"
  "v_or_b32 v222, v222, v223\n"
  "v_cmp_ne_u32 vcc, 0, v222\n"
  "s_cbranch_vccz LDy%=\n"
  "s_mov_b32 s13, 0x40000\n"
  "LPy%=:\n"
  "s_sleep 8\n"
  "buffer_load_dwordx2 v[222:223], v219, s[36:39], s23 offen sc1\n"
  "s_waitcnt vmcnt(0)\n"
  "v_xor_b32 v222, v222, v220\n"
  "v_xor_b32 v223, v223, v221\n"
  "v_or_b32 v222, v222, v223\n"
  "v_cmp_ne_u32 vcc, 0, v222\n"
  "s_cbranch_vccz LDy%=\n"
  "s_sub_u32 s13, s13, 1\n"
  "s_cmp_lg_u32 s13, 0\n"
  "s_cbranch_scc1 LPy%=\n"
  "LDy%=:\n"
  "buffer_load_dwordx4 v[0:3], v246, s[36:39], s25 offen sc1\n"
  "buffer_load_dwordx4 v[4:7], v246, s[36:39], s28 offen sc1\n"
  "s_waitcnt vmcnt(1)\n"
  "ds_write2_b32 v245, v0, v1 offset0:0 offset1:1\n"
  "ds_write2_b32 v245, v2, v3 offset0:2 offset1:3\n"
  "s_waitcnt vmcnt(0)\n"
  "ds_write2_b32 v204, v4, v5 offset0:0 offset1:1\n"
  "ds_write2_b32 v204, v6, v7 offset0:2 offset1:3\n"
  "s_waitcnt lgkmcnt(0)\n"
  "s_barrier\n"
  "ds_read2_b32 v[16:17], v244 offset0:0 offset1:65\n"
  "ds_read2_b32 v[18:19], v244 offset0:130 offset1:195\n"
  "ds_read2_b32 v[20:21], v244 offset0:32 offset1:97\n"
  "ds_read2_b32 v[22:23], v244 offset0:162 offset1:227\n"
  "s_waitcnt lgkmcnt(2)\n"
  "buffer_store_dwordx4 v[16:19], v247, s[40:43], 0 offen sc1\n"
  "s_waitcnt lgkmcnt(0)\n"
  "buffer_store_dwordx4 v[20:23], v247, s[40:43], s29 offen sc1\n"
  "LQ%=:\n"
  "s_endpgm\n"
  "LW%=:\n"
  "v_mov_b32 v148, 0x3f80\n"
  "ds_write_addtid_b32 v148 offset:0\n"
  "ds_write_addtid_b32 v148 offset:256\n"
  "v_cvt_pk_bf16_f32 v148, v76, v77\n"
  "v_cvt_pk_bf16_f32 v149, v78, v79\n"
  "ds_write_addtid_b32 v148 offset:528\n"
  "ds_write_addtid_b32 v149 offset:784\n"
  "v_cvt_pk_bf16_f32 v148, v80, v81\n"
  "v_cvt_pk_bf16_f32 v149, v82, v83\n"
  "ds_write_addtid_b32 v148 offset:1056\n"
  "ds_write_addtid_b32 v149 offset:1312\n"
  "v_cvt_pk_bf16_f32 v148, v84, v85\n"
  "v_cvt_pk_bf16_f32 v149, v86, v87\n"
  "ds_write_addtid_b32 v148 offset:2112\n"
  "ds_write_addtid_b32 v149 offset:2368\n"
  "v_cvt_pk_bf16_f32 v148, v88, v89\n"
  "v_cvt_pk_bf16_f32 v149, v90, v91\n"
  "ds_write_addtid_b32 v148 offset:4224\n"
  "ds_write_addtid_b32 v149 offset:4480\n"
  "v_cvt_pk_bf16_f32 v148, v92, v93\n"
  "v_cvt_pk_bf16_f32 v149, v94, v95\n"
  "ds_write_addtid_b32 v148 offset:8448\n"
  "ds_write_addtid_b32 v149 offset:8704\n"
  "v_mul_f32 v0, v80, v76\n"
  "v_mul_f32 v1, v80, v77\n"
  "v_mul_f32 v2, v82, v78\n"
  "v_mul_f32 v3, v82, v79\n"
  "v_fma_f32 v0, -v81, v77, v0\n"
  "v_fmac_f32 v1, v81, v76\n"
  "v_fma_f32 v2, -v83, v79, v2\n"
  "v_fmac_f32 v3, v83, v78\n"
  "v_cvt_pk_bf16_f32 v148, v0, v1\n"
  "v_cvt_pk_bf16_f32 v149, v2, v3\n"
  "ds_write_addtid_b32 v148 offset:1584\n"
  "ds_write_addtid_b32 v149 offset:1840\n"
  "v_mul_f32 v20, v84, v76\n"
  "v_mul_f32 v21, v84, v77\n"
  "v_mul_f32 v22, v86, v78\n"
  "v_mul_f32 v23, v86, v79\n"
  "v_fma_f32 v20, -v85, v77, v20\n"
  "v_fmac_f32 v21, v85, v76\n"
  "v_fma_f32 v22, -v87, v79, v22\n"
  "v_fmac_f32 v23, v87, v78\n"
  "v_cvt_pk_bf16_f32 v148, v20, v21\n"
  "v_cvt_pk_bf16_f32 v149, v22, v23\n"
  "ds_write_addtid_b32 v148 offset:2640\n"
  "ds_write_addtid_b32 v149 offset:2896\n"
  "v_mul_f32 v20, v84, v80\n"
  "v_mul_f32 v21, v84, v81\n"
  "v_mul_f32 v22, v86, v82\n"
  "v_mul_f32 v23, v86, v83\n"
  "v_fma_f32 v20, -v85, v81, v20\n"
  "v_fmac_f32 v21, v85, v80\n"
  "v_fma_f32 v22, -v87, v83, v22\n"
  "v_fmac_f32 v23, v87, v82\n"
  "v_cvt_pk_bf16_f32 v148, v20, v21\n"
  "v_cvt_pk_bf16_f32 v149, v22, v23\n"
  "ds_write_addtid_b32 v148 offset:3168\n"
  "ds_write_addtid_b32 v149 offset:3424\n"
  "v_mul_f32 v20, v84, v0\n"
  "v_mul_f32 v21, v84, v1\n"
  "v_mul_f32 v22, v86, v2\n"
  "v_mul_f32 v23, v86, v3\n"
  "v_fma_f32 v20, -v85, v1, v20\n"
  "v_fmac_f32 v21, v85, v0\n"
  "v_fma_f32 v22, -v87, v3, v22\n"
  "v_fmac_f32 v23, v87, v2\n"
  "v_cvt_pk_bf16_f32 v148, v20, v21\n"
  "v_cvt_pk_bf16_f32 v149, v22, v23\n"
  "ds_write_addtid_b32 v148 offset:3696\n"
  "ds_write_addtid_b32 v149 offset:3952\n"
  "v_mul_f32 v20, v88, v76\n"
  "v_mul_f32 v21, v88, v77\n"
  "v_mul_f32 v22, v90, v78\n"
  "v_mul_f32 v23, v90, v79\n"
  "v_fma_f32 v20, -v89, v77, v20\n"
  "v_fmac_f32 v21, v89, v76\n"
  "v_fma_f32 v22, -v91, v79, v22\n"
  "v_fmac_f32 v23, v91, v78\n"
  "v_cvt_pk_bf16_f32 v148, v20, v21\n"
  "v_cvt_pk_bf16_f32 v149, v22, v23\n"
  "ds_write_addtid_b32 v148 offset:4752\n"
  "ds_write_addtid_b32 v149 offset:5008\n"
  "v_mul_f32 v4, v88, v84\n"
  "v_mul_f32 v5, v88, v85\n"
  "v_mul_f32 v6, v90, v86\n"
  "v_mul_f32 v7, v90, v87\n"
  "v_fma_f32 v4, -v89, v85, v4\n"
  "v_fmac_f32 v5, v89, v84\n"
  "v_fma_f32 v6, -v91, v87, v6\n"
  "v_fmac_f32 v7, v91, v86\n"
  "v_cvt_pk_bf16_f32 v148, v4, v5\n"
  "v_cvt_pk_bf16_f32 v149, v6, v7\n"
  "ds_write_addtid_b32 v148 offset:6336\n"
  "ds_write_addtid_b32 v149 offset:6592\n"
  "v_mul_f32 v20, v88, v80\n"
  "v_mul_f32 v21, v88, v81\n"
  "v_mul_f32 v22, v90, v82\n"
  "v_mul_f32 v23, v90, v83\n"
  "v_fma_f32 v20, -v89, v81, v20\n"
  "v_fmac_f32 v21, v89, v80\n"
  "v_fma_f32 v22, -v91, v83, v22\n"
  "v_fmac_f32 v23, v91, v82\n"
  "v_cvt_pk_bf16_f32 v148, v20, v21\n"
  "v_cvt_pk_bf16_f32 v149, v22, v23\n"
  "ds_write_addtid_b32 v148 offset:5280\n"
  "ds_write_addtid_b32 v149 offset:5536\n"
  "v_mul_f32 v20, v88, v0\n"
  "v_mul_f32 v21, v88, v1\n"
  "v_mul_f32 v22, v90, v2\n"
  "v_mul_f32 v23, v90, v3\n"
  "v_fma_f32 v20, -v89, v1, v20\n"
  "v_fmac_f32 v21, v89, v0\n"
  "v_fma_f32 v22, -v91, v3, v22\n"
  "v_fmac_f32 v23, v91, v2\n"
  "v_cvt_pk_bf16_f32 v148, v20, v21\n"
  "v_cvt_pk_bf16_f32 v149, v22, v23\n"
  "ds_write_addtid_b32 v148 offset:5808\n"
  "ds_write_addtid_b32 v149 offset:6064\n"
  "v_mul_f32 v20, v4, v76\n"
  "v_mul_f32 v21, v4, v77\n"
  "v_mul_f32 v22, v6, v78\n"
  "v_mul_f32 v23, v6, v79\n"
  "v_fma_f32 v20, -v5, v77, v20\n"
  "v_fmac_f32 v21, v5, v76\n"
  "v_fma_f32 v22, -v7, v79, v22\n"
  "v_fmac_f32 v23, v7, v78\n"
  "v_cvt_pk_bf16_f32 v148, v20, v21\n"
  "v_cvt_pk_bf16_f32 v149, v22, v23\n"
  "ds_write_addtid_b32 v148 offset:6864\n"
  "ds_write_addtid_b32 v149 offset:7120\n"
  "v_mul_f32 v20, v4, v80\n"
  "v_mul_f32 v21, v4, v81\n"
  "v_mul_f32 v22, v6, v82\n"
  "v_mul_f32 v23, v6, v83\n"
  "v_fma_f32 v20, -v5, v81, v20\n"
  "v_fmac_f32 v21, v5, v80\n"
  "v_fma_f32 v22, -v7, v83, v22\n"
  "v_fmac_f32 v23, v7, v82\n"
  "v_cvt_pk_bf16_f32 v148, v20, v21\n"
  "v_cvt_pk_bf16_f32 v149, v22, v23\n"
  "ds_write_addtid_b32 v148 offset:7392\n"
  "ds_write_addtid_b32 v149 offset:7648\n"
  "v_mul_f32 v20, v4, v0\n"
  "v_mul_f32 v21, v4, v1\n"
  "v_mul_f32 v22, v6, v2\n"
  "v_mul_f32 v23, v6, v3\n"
  "v_fma_f32 v20, -v5, v1, v20\n"
  "v_fmac_f32 v21, v5, v0\n"
  "v_fma_f32 v22, -v7, v3, v22\n"
  "v_fmac_f32 v23, v7, v2\n"
  "v_cvt_pk_bf16_f32 v148, v20, v21\n"
  "v_cvt_pk_bf16_f32 v149, v22, v23\n"
  "ds_write_addtid_b32 v148 offset:7920\n"
  "ds_write_addtid_b32 v149 offset:8176\n"
  "v_mul_f32 v20, v92, v76\n"
  "v_mul_f32 v21, v92, v77\n"
  "v_mul_f32 v22, v94, v78\n"
  "v_mul_f32 v23, v94, v79\n"
  "v_fma_f32 v20, -v93, v77, v20\n"
  "v_fmac_f32 v21, v93, v76\n"
  "v_fma_f32 v22, -v95, v79, v22\n"
  "v_fmac_f32 v23, v95, v78\n"
  "v_cvt_pk_bf16_f32 v148, v20, v21\n"
  "v_cvt_pk_bf16_f32 v149, v22, v23\n"
  "ds_write_addtid_b32 v148 offset:8976\n"
  "ds_write_addtid_b32 v149 offset:9232\n"
  "v_mul_f32 v20, v92, v80\n"
  "v_mul_f32 v21, v92, v81\n"
  "v_mul_f32 v22, v94, v82\n"
  "v_mul_f32 v23, v94, v83\n"
  "v_fma_f32 v20, -v93, v81, v20\n"
  "v_fmac_f32 v21, v93, v80\n"
  "v_fma_f32 v22, -v95, v83, v22\n"
  "v_fmac_f32 v23, v95, v82\n"
  "v_cvt_pk_bf16_f32 v148, v20, v21\n"
  "v_cvt_pk_bf16_f32 v149, v22, v23\n"
  "ds_write_addtid_b32 v148 offset:9504\n"
  "ds_write_addtid_b32 v149 offset:9760\n"
  "v_mul_f32 v8, v92, v84\n"
  "v_mul_f32 v9, v92, v85\n"
  "v_mul_f32 v10, v94, v86\n"
  "v_mul_f32 v11, v94, v87\n"
  "v_fma_f32 v8, -v93, v85, v8\n"
  "v_fmac_f32 v9, v93, v84\n"
  "v_fma_f32 v10, -v95, v87, v10\n"
  "v_fmac_f32 v11, v95, v86\n"
  "v_cvt_pk_bf16_f32 v148, v8, v9\n"
  "v_cvt_pk_bf16_f32 v149, v10, v11\n"
  "ds_write_addtid_b32 v148 offset:10560\n"
  "ds_write_addtid_b32 v149 offset:10816\n"
  "v_mul_f32 v20, v92, v0\n"
  "v_mul_f32 v21, v92, v1\n"
  "v_mul_f32 v22, v94, v2\n"
  "v_mul_f32 v23, v94, v3\n"
  "v_fma_f32 v20, -v93, v1, v20\n"
  "v_fmac_f32 v21, v93, v0\n"
  "v_fma_f32 v22, -v95, v3, v22\n"
  "v_fmac_f32 v23, v95, v2\n"
  "v_cvt_pk_bf16_f32 v148, v20, v21\n"
  "v_cvt_pk_bf16_f32 v149, v22, v23\n"
  "ds_write_addtid_b32 v148 offset:10032\n"
  "ds_write_addtid_b32 v149 offset:10288\n"
  "v_mul_f32 v20, v8, v76\n"
  "v_mul_f32 v21, v8, v77\n"
  "v_mul_f32 v22, v10, v78\n"
  "v_mul_f32 v23, v10, v79\n"
  "v_fma_f32 v20, -v9, v77, v20\n"
  "v_fmac_f32 v21, v9, v76\n"
  "v_fma_f32 v22, -v11, v79, v22\n"
  "v_fmac_f32 v23, v11, v78\n"
  "v_cvt_pk_bf16_f32 v148, v20, v21\n"
  "v_cvt_pk_bf16_f32 v149, v22, v23\n"
  "ds_write_addtid_b32 v148 offset:11088\n"
  "ds_write_addtid_b32 v149 offset:11344\n"
  "v_mul_f32 v20, v8, v80\n"
  "v_mul_f32 v21, v8, v81\n"
  "v_mul_f32 v22, v10, v82\n"
  "v_mul_f32 v23, v10, v83\n"
  "v_fma_f32 v20, -v9, v81, v20\n"
  "v_fmac_f32 v21, v9, v80\n"
  "v_fma_f32 v22, -v11, v83, v22\n"
  "v_fmac_f32 v23, v11, v82\n"
  "v_cvt_pk_bf16_f32 v148, v20, v21\n"
  "v_cvt_pk_bf16_f32 v149, v22, v23\n"
  "ds_write_addtid_b32 v148 offset:11616\n"
  "ds_write_addtid_b32 v149 offset:11872\n"
  "v_mul_f32 v12, v92, v88\n"
  "v_mul_f32 v13, v92, v89\n"
  "v_mul_f32 v14, v94, v90\n"
  "v_mul_f32 v15, v94, v91\n"
  "v_fma_f32 v12, -v93, v89, v12\n"
  "v_fmac_f32 v13, v93, v88\n"
  "v_fma_f32 v14, -v95, v91, v14\n"
  "v_fmac_f32 v15, v95, v90\n"
  "v_cvt_pk_bf16_f32 v148, v12, v13\n"
  "v_cvt_pk_bf16_f32 v149, v14, v15\n"
  "ds_write_addtid_b32 v148 offset:12672\n"
  "ds_write_addtid_b32 v149 offset:12928\n"
  "v_mul_f32 v20, v8, v0\n"
  "v_mul_f32 v21, v8, v1\n"
  "v_mul_f32 v22, v10, v2\n"
  "v_mul_f32 v23, v10, v3\n"
  "v_fma_f32 v20, -v9, v1, v20\n"
  "v_fmac_f32 v21, v9, v0\n"
  "v_fma_f32 v22, -v11, v3, v22\n"
  "v_fmac_f32 v23, v11, v2\n"
  "v_cvt_pk_bf16_f32 v148, v20, v21\n"
  "v_cvt_pk_bf16_f32 v149, v22, v23\n"
  "ds_write_addtid_b32 v148 offset:12144\n"
  "ds_write_addtid_b32 v149 offset:12400\n"
  "v_mul_f32 v20, v12, v76\n"
  "v_mul_f32 v21, v12, v77\n"
  "v_mul_f32 v22, v14, v78\n"
  "v_mul_f32 v23, v14, v79\n"
  "v_fma_f32 v20, -v13, v77, v20\n"
  "v_fmac_f32 v21, v13, v76\n"
  "v_fma_f32 v22, -v15, v79, v22\n"
  "v_fmac_f32 v23, v15, v78\n"
  "v_cvt_pk_bf16_f32 v148, v20, v21\n"
  "v_cvt_pk_bf16_f32 v149, v22, v23\n"
  "ds_write_addtid_b32 v148 offset:13200\n"
  "ds_write_addtid_b32 v149 offset:13456\n"
  "v_mul_f32 v20, v12, v80\n"
  "v_mul_f32 v21, v12, v81\n"
  "v_mul_f32 v22, v14, v82\n"
  "v_mul_f32 v23, v14, v83\n"
  "v_fma_f32 v20, -v13, v81, v20\n"
  "v_fmac_f32 v21, v13, v80\n"
  "v_fma_f32 v22, -v15, v83, v22\n"
  "v_fmac_f32 v23, v15, v82\n"
  "v_cvt_pk_bf16_f32 v148, v20, v21\n"
  "v_cvt_pk_bf16_f32 v149, v22, v23\n"
  "ds_write_addtid_b32 v148 offset:13728\n"
  "ds_write_addtid_b32 v149 offset:13984\n"
  "v_mul_f32 v16, v92, v4\n"
  "v_mul_f32 v17, v92, v5\n"
  "v_mul_f32 v18, v94, v6\n"
  "v_mul_f32 v19, v94, v7\n"
  "v_fma_f32 v16, -v93, v5, v16\n"
  "v_fmac_f32 v17, v93, v4\n"
  "v_fma_f32 v18, -v95, v7, v18\n"
  "v_fmac_f32 v19, v95, v6\n"
  "v_cvt_pk_bf16_f32 v148, v16, v17\n"
  "v_cvt_pk_bf16_f32 v149, v18, v19\n"
  "ds_write_addtid_b32 v148 offset:14784\n"
  "ds_write_addtid_b32 v149 offset:15040\n"
  "v_mul_f32 v20, v12, v0\n"
  "v_mul_f32 v21, v12, v1\n"
  "v_mul_f32 v22, v14, v2\n"
  "v_mul_f32 v23, v14, v3\n"
  "v_fma_f32 v20, -v13, v1, v20\n"
  "v_fmac_f32 v21, v13, v0\n"
  "v_fma_f32 v22, -v15, v3, v22\n"
  "v_fmac_f32 v23, v15, v2\n"
  "v_cvt_pk_bf16_f32 v148, v20, v21\n"
  "v_cvt_pk_bf16_f32 v149, v22, v23\n"
  "ds_write_addtid_b32 v148 offset:14256\n"
  "ds_write_addtid_b32 v149 offset:14512\n"
  "v_mul_f32 v20, v16, v76\n"
  "v_mul_f32 v21, v16, v77\n"
  "v_mul_f32 v22, v18, v78\n"
  "v_mul_f32 v23, v18, v79\n"
  "v_fma_f32 v20, -v17, v77, v20\n"
  "v_fmac_f32 v21, v17, v76\n"
  "v_fma_f32 v22, -v19, v79, v22\n"
  "v_fmac_f32 v23, v19, v78\n"
  "v_cvt_pk_bf16_f32 v148, v20, v21\n"
  "v_cvt_pk_bf16_f32 v149, v22, v23\n"
  "ds_write_addtid_b32 v148 offset:15312\n"
  "ds_write_addtid_b32 v149 offset:15568\n"
  "v_mul_f32 v20, v16, v80\n"
  "v_mul_f32 v21, v16, v81\n"
  "v_mul_f32 v22, v18, v82\n"
  "v_mul_f32 v23, v18, v83\n"
  "v_fma_f32 v20, -v17, v81, v20\n"
  "v_fmac_f32 v21, v17, v80\n"
  "v_fma_f32 v22, -v19, v83, v22\n"
  "v_fmac_f32 v23, v19, v82\n"
  "v_cvt_pk_bf16_f32 v148, v20, v21\n"
  "v_cvt_pk_bf16_f32 v149, v22, v23\n"
  "ds_write_addtid_b32 v148 offset:15840\n"
  "ds_write_addtid_b32 v149 offset:16096\n"
  "v_mul_f32 v20, v16, v0\n"
  "v_mul_f32 v21, v16, v1\n"
  "v_mul_f32 v22, v18, v2\n"
  "v_mul_f32 v23, v18, v3\n"
  "v_fma_f32 v20, -v17, v1, v20\n"
  "v_fmac_f32 v21, v17, v0\n"
  "v_fma_f32 v22, -v19, v3, v22\n"
  "v_fmac_f32 v23, v19, v2\n"
  "v_cvt_pk_bf16_f32 v148, v20, v21\n"
  "v_cvt_pk_bf16_f32 v149, v22, v23\n"
  "ds_write_addtid_b32 v148 offset:16368\n"
  "ds_write_addtid_b32 v149 offset:16624\n"
  "ds_read_b128 v[0:3], v205 offset:0\n"
  "ds_read_b128 v[4:7], v205 offset:32\n"
  "ds_read_b128 v[8:11], v205 offset:64\n"
  "ds_read_b128 v[12:15], v205 offset:96\n"
  "ds_read_b128 v[16:19], v205 offset:128\n"
  "ds_read_b128 v[20:23], v205 offset:160\n"
  "ds_read_b128 v[24:27], v205 offset:192\n"
  "ds_read_b128 v[28:31], v205 offset:224\n"
  "ds_read_b128 v[32:35], v205 offset:256\n"
  "ds_read_b128 v[36:39], v205 offset:288\n"
  "ds_read_b128 v[40:43], v205 offset:320\n"
  "ds_read_b128 v[44:47], v205 offset:352\n"
  "ds_read_b128 v[48:51], v205 offset:384\n"
  "ds_read_b128 v[52:55], v205 offset:416\n"
  "ds_read_b128 v[56:59], v205 offset:448\n"
  "ds_read_b128 v[60:63], v205 offset:480\n"
  "s_waitcnt vmcnt(3)\n"
  "v_lshlrev_b32 v140, 16, v252\n"
  "v_lshlrev_b32 v141, 16, v253\n"
  "v_and_b32 v142, 0xffff0000, v252\n"
  "v_and_b32 v143, 0xffff0000, v253\n"
  "v_lshlrev_b32 v144, 16, v254\n"
  "v_lshlrev_b32 v145, 16, v255\n"
  "v_and_b32 v146, 0xffff0000, v254\n"
  "v_and_b32 v147, 0xffff0000, v255\n"
  "v_mul_f32 v172, v144, v140\n"
  "v_mul_f32 v173, v144, v141\n"
  "v_mul_f32 v174, v146, v142\n"
  "v_mul_f32 v175, v146, v143\n"
  "v_fma_f32 v172, -v145, v141, v172\n"
  "v_fmac_f32 v173, v145, v140\n"
  "v_fma_f32 v174, -v147, v143, v174\n"
  "v_fmac_f32 v175, v147, v142\n"
  "s_waitcnt vmcnt(0)\n"
  "ds_write2_b32 v163, v232, v233 offset0:0 offset1:65\n"
  "ds_write2_b32 v163, v234, v235 offset0:130 offset1:195\n"
  "ds_write2_b32 v163, v236, v237 offset0:32 offset1:97\n"
  "ds_write2_b32 v163, v238, v239 offset0:162 offset1:227\n"
  "s_waitcnt lgkmcnt(0)\n"
  "s_barrier\n"
  "ds_read2_b32 v[132:133], v164 offset0:0 offset1:1\n"
  "ds_read2_b32 v[134:135], v164 offset0:2 offset1:3\n"
  "ds_read2_b32 v[136:137], v165 offset0:0 offset1:1\n"
  "ds_read2_b32 v[138:139], v165 offset0:2 offset1:3\n"
  "s_add_u32 s15, s24, 131072\n"
  "s_waitcnt lgkmcnt(0)\n"
  "buffer_store_dwordx4 v[132:135], v246, s[36:39], s24 offen sc1\n"
  "buffer_store_dwordx4 v[136:139], v246, s[36:39], s15 offen sc1\n"
  "v_cvt_pk_bf16_f32 v148, v172, -v173\n"
  "v_cvt_pk_bf16_f32 v149, v174, -v175\n"
  "ds_write_addtid_b32 v148 offset:0\n"
  "ds_write_addtid_b32 v149 offset:256\n"
  "v_mul_f32 v76, v172, v64\n"
  "v_mul_f32 v77, v172, v65\n"
  "v_mul_f32 v78, v174, v66\n"
  "v_mul_f32 v79, v174, v67\n"
  "v_fma_f32 v76, -v173, v65, v76\n"
  "v_fmac_f32 v77, v173, v64\n"
  "v_fma_f32 v78, -v175, v67, v78\n"
  "v_fmac_f32 v79, v175, v66\n"
  "v_cvt_pk_bf16_f32 v148, v76, -v77\n"
  "v_cvt_pk_bf16_f32 v149, v78, -v79\n"
  "ds_write_addtid_b32 v148 offset:528\n"
  "ds_write_addtid_b32 v149 offset:784\n"
  "v_mul_f32 v80, v172, v68\n"
  "v_mul_f32 v81, v172, v69\n"
  "v_mul_f32 v82, v174, v70\n"
  "v_mul_f32 v83, v174, v71\n"
  "v_fma_f32 v80, -v173, v69, v80\n"
  "v_fmac_f32 v81, v173, v68\n"
  "v_fma_f32 v82, -v175, v71, v82\n"
  "v_fmac_f32 v83, v175, v70\n"
  "v_cvt_pk_bf16_f32 v148, v80, -v81\n"
  "v_cvt_pk_bf16_f32 v149, v82, -v83\n"
  "ds_write_addtid_b32 v148 offset:1056\n"
  "ds_write_addtid_b32 v149 offset:1312\n"
  "v_mul_f32 v84, v172, v72\n"
  "v_mul_f32 v85, v172, v73\n"
  "v_mul_f32 v86, v174, v74\n"
  "v_mul_f32 v87, v174, v75\n"
  "v_fma_f32 v84, -v173, v73, v84\n"
  "v_fmac_f32 v85, v173, v72\n"
  "v_fma_f32 v86, -v175, v75, v86\n"
  "v_fmac_f32 v87, v175, v74\n"
  "v_cvt_pk_bf16_f32 v148, v84, -v85\n"
  "v_cvt_pk_bf16_f32 v149, v86, -v87\n"
  "ds_write_addtid_b32 v148 offset:2112\n"
  "ds_write_addtid_b32 v149 offset:2368\n"
  "v_mul_f32 v112, v76, v68\n"
  "v_mul_f32 v113, v76, v69\n"
  "v_mul_f32 v114, v78, v70\n"
  "v_mul_f32 v115, v78, v71\n"
  "v_fma_f32 v112, -v77, v69, v112\n"
  "v_fmac_f32 v113, v77, v68\n"
  "v_fma_f32 v114, -v79, v71, v114\n"
  "v_fmac_f32 v115, v79, v70\n"
  "v_cvt_pk_bf16_f32 v148, v112, -v113\n"
  "v_cvt_pk_bf16_f32 v149, v114, -v115\n"
  "ds_write_addtid_b32 v148 offset:1584\n"
  "ds_write_addtid_b32 v149 offset:1840\n"
  "v_mul_f32 v128, v76, v72\n"
  "v_mul_f32 v129, v76, v73\n"
  "v_mul_f32 v130, v78, v74\n"
  "v_mul_f32 v131, v78, v75\n"
  "v_fma_f32 v128, -v77, v73, v128\n"
  "v_fmac_f32 v129, v77, v72\n"
  "v_fma_f32 v130, -v79, v75, v130\n"
  "v_fmac_f32 v131, v79, v74\n"
  "v_cvt_pk_bf16_f32 v148, v128, -v129\n"
  "v_cvt_pk_bf16_f32 v149, v130, -v131\n"
  "ds_write_addtid_b32 v148 offset:2640\n"
  "ds_write_addtid_b32 v149 offset:2896\n"
  "v_mul_f32 v116, v80, v72\n"
  "v_mul_f32 v117, v80, v73\n"
  "v_mul_f32 v118, v82, v74\n"
  "v_mul_f32 v119, v82, v75\n"
  "v_fma_f32 v116, -v81, v73, v116\n"
  "v_fmac_f32 v117, v81, v72\n"
  "v_fma_f32 v118, -v83, v75, v118\n"
  "v_fmac_f32 v119, v83, v74\n"
  "v_cvt_pk_bf16_f32 v148, v116, -v117\n"
  "v_cvt_pk_bf16_f32 v149, v118, -v119\n"
  "ds_write_addtid_b32 v148 offset:3168\n"
  "ds_write_addtid_b32 v149 offset:3424\n"
  "v_mul_f32 v88, v172, v96\n"
  "v_mul_f32 v89, v172, v97\n"
  "v_mul_f32 v90, v174, v98\n"
  "v_mul_f32 v91, v174, v99\n"
  "v_fma_f32 v88, -v173, v97, v88\n"
  "v_fmac_f32 v89, v173, v96\n"
  "v_fma_f32 v90, -v175, v99, v90\n"
  "v_fmac_f32 v91, v175, v98\n"
  "v_cvt_pk_bf16_f32 v148, v88, -v89\n"
  "v_cvt_pk_bf16_f32 v149, v90, -v91\n"
  "ds_write_addtid_b32 v148 offset:4224\n"
  "ds_write_addtid_b32 v149 offset:4480\n"
  "v_mul_f32 v128, v112, v72\n"
  "v_mul_f32 v129, v112, v73\n"
  "v_mul_f32 v130, v114, v74\n"
  "v_mul_f32 v131, v114, v75\n"
  "v_fma_f32 v128, -v113, v73, v128\n"
  "v_fmac_f32 v129, v113, v72\n"
  "v_fma_f32 v130, -v115, v75, v130\n"
  "v_fmac_f32 v131, v115, v74\n"
  "v_cvt_pk_bf16_f32 v148, v128, -v129\n"
  "v_cvt_pk_bf16_f32 v149, v130, -v131\n"
  "ds_write_addtid_b32 v148 offset:3696\n"
  "ds_write_addtid_b32 v149 offset:3952\n"
  "v_mul_f32 v128, v80, v96\n"
  "v_mul_f32 v129, v80, v97\n"
  "v_mul_f32 v130, v82, v98\n"
  "v_mul_f32 v131, v82, v99\n"
  "v_fma_f32 v128, -v81, v97, v128\n"
  "v_fmac_f32 v129, v81, v96\n"
  "v_fma_f32 v130, -v83, v99, v130\n"
  "v_fmac_f32 v131, v83, v98\n"
  "v_cvt_pk_bf16_f32 v148, v128, -v129\n"
  "v_cvt_pk_bf16_f32 v149, v130, -v131\n"
  "ds_write_addtid_b32 v148 offset:4752\n"
  "ds_write_addtid_b32 v149 offset:5008\n"
  "v_mul_f32 v120, v84, v96\n"
  "v_mul_f32 v121, v84, v97\n"
  "v_mul_f32 v122, v86, v98\n"
  "v_mul_f32 v123, v86, v99\n"
  "v_fma_f32 v120, -v85, v97, v120\n"
  "v_fmac_f32 v121, v85, v96\n"
  "v_fma_f32 v122, -v87, v99, v122\n"
  "v_fmac_f32 v123, v87, v98\n"
  "v_cvt_pk_bf16_f32 v148, v120, -v121\n"
  "v_cvt_pk_bf16_f32 v149, v122, -v123\n"
  "ds_write_addtid_b32 v148 offset:5280\n"
  "ds_write_addtid_b32 v149 offset:5536\n"
  "v_mul_f32 v92, v172, v100\n"
  "v_mul_f32 v93, v172, v101\n"
  "v_mul_f32 v94, v174, v102\n"
  "v_mul_f32 v95, v174, v103\n"
  "v_fma_f32 v92, -v173, v101, v92\n"
  "v_fmac_f32 v93, v173, v100\n"
  "v_fma_f32 v94, -v175, v103, v94\n"
  "v_fmac_f32 v95, v175, v102\n"
  "v_cvt_pk_bf16_f32 v148, v92, -v93\n"
  "v_cvt_pk_bf16_f32 v149, v94, -v95\n"
  "ds_write_addtid_b32 v148 offset:6336\n"
  "ds_write_addtid_b32 v149 offset:6592\n"
  "v_mul_f32 v128, v116, v96\n"
  "v_mul_f32 v129, v116, v97\n"
  "v_mul_f32 v130, v118, v98\n"
  "v_mul_f32 v131, v118, v99\n"
  "v_fma_f32 v128, -v117, v97, v128\n"
  "v_fmac_f32 v129, v117, v96\n"
  "v_fma_f32 v130, -v119, v99, v130\n"
  "v_fmac_f32 v131, v119, v98\n"
  "v_cvt_pk_bf16_f32 v148, v128, -v129\n"
  "v_cvt_pk_bf16_f32 v149, v130, -v131\n"
  "ds_write_addtid_b32 v148 offset:5808\n"
  "ds_write_addtid_b32 v149 offset:6064\n"
  "v_mul_f32 v128, v84, v100\n"
  "v_mul_f32 v129, v84, v101\n"
  "v_mul_f32 v130, v86, v102\n"
  "v_mul_f32 v131, v86, v103\n"
  "v_fma_f32 v128, -v85, v101, v128\n"
  "v_fmac_f32 v129, v85, v100\n"
  "v_fma_f32 v130, -v87, v103, v130\n"
  "v_fmac_f32 v131, v87, v102\n"
  "v_cvt_pk_bf16_f32 v148, v128, -v129\n"
  "v_cvt_pk_bf16_f32 v149, v130, -v131\n"
  "ds_write_addtid_b32 v148 offset:6864\n"
  "ds_write_addtid_b32 v149 offset:7120\n"
  "v_mul_f32 v124, v88, v100\n"
  "v_mul_f32 v125, v88, v101\n"
  "v_mul_f32 v126, v90, v102\n"
  "v_mul_f32 v127, v90, v103\n"
  "v_fma_f32 v124, -v89, v101, v124\n"
  "v_fmac_f32 v125, v89, v100\n"
  "v_fma_f32 v126, -v91, v103, v126\n"
  "v_fmac_f32 v127, v91, v102\n"
  "v_cvt_pk_bf16_f32 v148, v124, -v125\n"
  "v_cvt_pk_bf16_f32 v149, v126, -v127\n"
  "ds_write_addtid_b32 v148 offset:7392\n"
  "ds_write_addtid_b32 v149 offset:7648\n"
  "v_mul_f32 v108, v172, v104\n"
  "v_mul_f32 v109, v172, v105\n"
  "v_mul_f32 v110, v174, v106\n"
  "v_mul_f32 v111, v174, v107\n"
  "v_fma_f32 v108, -v173, v105, v108\n"
  "v_fmac_f32 v109, v173, v104\n"
  "v_fma_f32 v110, -v175, v107, v110\n"
  "v_fmac_f32 v111, v175, v106\n"
  "v_cvt_pk_bf16_f32 v148, v108, -v109\n"
  "v_cvt_pk_bf16_f32 v149, v110, -v111\n"
  "ds_write_addtid_b32 v148 offset:8448\n"
  "ds_write_addtid_b32 v149 offset:8704\n"
  "v_mul_f32 v128, v120, v100\n"
  "v_mul_f32 v129, v120, v101\n"
  "v_mul_f32 v130, v122, v102\n"
  "v_mul_f32 v131, v122, v103\n"
  "v_fma_f32 v128, -v121, v101, v128\n"
  "v_fmac_f32 v129, v121, v100\n"
  "v_fma_f32 v130, -v123, v103, v130\n"
  "v_fmac_f32 v131, v123, v102\n"
  "v_cvt_pk_bf16_f32 v148, v128, -v129\n"
  "v_cvt_pk_bf16_f32 v149, v130, -v131\n"
  "ds_write_addtid_b32 v148 offset:7920\n"
  "ds_write_addtid_b32 v149 offset:8176\n"
  "v_mul_f32 v128, v88, v104\n"
  "v_mul_f32 v129, v88, v105\n"
  "v_mul_f32 v130, v90, v106\n"
  "v_mul_f32 v131, v90, v107\n"
  "v_fma_f32 v128, -v89, v105, v128\n"
  "v_fmac_f32 v129, v89, v104\n"
  "v_fma_f32 v130, -v91, v107, v130\n"
  "v_fmac_f32 v131, v91, v106\n"
  "v_cvt_pk_bf16_f32 v148, v128, -v129\n"
  "v_cvt_pk_bf16_f32 v149, v130, -v131\n"
  "ds_write_addtid_b32 v148 offset:8976\n"
  "ds_write_addtid_b32 v149 offset:9232\n"
  "v_mul_f32 v128, v92, v104\n"
  "v_mul_f32 v129, v92, v105\n"
  "v_mul_f32 v130, v94, v106\n"
  "v_mul_f32 v131, v94, v107\n"
  "v_fma_f32 v128, -v93, v105, v128\n"
  "v_fmac_f32 v129, v93, v104\n"
  "v_fma_f32 v130, -v95, v107, v130\n"
  "v_fmac_f32 v131, v95, v106\n"
  "v_cvt_pk_bf16_f32 v148, v128, -v129\n"
  "v_cvt_pk_bf16_f32 v149, v130, -v131\n"
  "ds_write_addtid_b32 v148 offset:9504\n"
  "ds_write_addtid_b32 v149 offset:9760\n"
  "v_mul_f32 v128, v108, v104\n"
  "v_mul_f32 v129, v108, v105\n"
  "v_mul_f32 v130, v110, v106\n"
  "v_mul_f32 v131, v110, v107\n"
  "v_fma_f32 v128, -v109, v105, v128\n"
  "v_fmac_f32 v129, v109, v104\n"
  "v_fma_f32 v130, -v111, v107, v130\n"
  "v_fmac_f32 v131, v111, v106\n"
  "v_cvt_pk_bf16_f32 v148, v128, -v129\n"
  "v_cvt_pk_bf16_f32 v149, v130, -v131\n"
  "ds_write_addtid_b32 v148 offset:10560\n"
  "ds_write_addtid_b32 v149 offset:10816\n"
  "v_mul_f32 v128, v124, v104\n"
  "v_mul_f32 v129, v124, v105\n"
  "v_mul_f32 v130, v126, v106\n"
  "v_mul_f32 v131, v126, v107\n"
  "v_fma_f32 v128, -v125, v105, v128\n"
  "v_fmac_f32 v129, v125, v104\n"
  "v_fma_f32 v130, -v127, v107, v130\n"
  "v_fmac_f32 v131, v127, v106\n"
  "v_cvt_pk_bf16_f32 v148, v128, -v129\n"
  "v_cvt_pk_bf16_f32 v149, v130, -v131\n"
  "ds_write_addtid_b32 v148 offset:10032\n"
  "ds_write_addtid_b32 v149 offset:10288\n"
  "v_mov_b32 v150, s45\n"
  "ds_read_b128 v[64:67], v205 offset:0\n"
  "ds_read_b128 v[68:71], v205 offset:32\n"
  "ds_read_b128 v[72:75], v205 offset:64\n"
  "ds_read_b128 v[76:79], v205 offset:96\n"
  "ds_read_b128 v[80:83], v205 offset:128\n"
  "ds_read_b128 v[84:87], v205 offset:160\n"
  "ds_read_b128 v[88:91], v205 offset:192\n"
  "ds_read_b128 v[92:95], v205 offset:224\n"
  "ds_read_b128 v[96:99], v205 offset:256\n"
  "ds_read_b128 v[100:103], v205 offset:288\n"
  "ds_read_b128 v[104:107], v205 offset:320\n"
  "ds_read_b128 v[108:111], v205 offset:352\n"
  "ds_read_b128 v[112:115], v205 offset:384\n"
  "ds_read_b128 v[116:119], v205 offset:416\n"
  "ds_read_b128 v[120:123], v205 offset:448\n"
  "ds_read_b128 v[124:127], v205 offset:480\n"
  "s_barrier\n"
  "s_waitcnt vmcnt(0)\n"
  "buffer_store_dwordx2 v[220:221], v218, s[36:39], s8 offen sc1\n"
  "s_waitcnt lgkmcnt(14)\n"
  "v_mfma_f32_32x32x16_bf16 v[128:143], v[64:67], v[0:3], 0\n"
  "v_mfma_f32_32x32x16_bf16 v[128:143], v[68:71], v[4:7], v[128:143]\n"
  "s_waitcnt lgkmcnt(13)\n"
  "v_mfma_f32_32x32x16_bf16 v[128:143], v[72:75], v[8:11], v[128:143]\n"
  "s_waitcnt lgkmcnt(12)\n"
  "v_mfma_f32_32x32x16_bf16 v[128:143], v[76:79], v[12:15], v[128:143]\n"
  "s_waitcnt lgkmcnt(11)\n"
  "v_mfma_f32_32x32x16_bf16 v[128:143], v[80:83], v[16:19], v[128:143]\n"
  "s_waitcnt lgkmcnt(10)\n"
  "v_mfma_f32_32x32x16_bf16 v[128:143], v[84:87], v[20:23], v[128:143]\n"
  "s_waitcnt lgkmcnt(9)\n"
  "v_mfma_f32_32x32x16_bf16 v[128:143], v[88:91], v[24:27], v[128:143]\n"
  "s_waitcnt lgkmcnt(8)\n"
  "v_mfma_f32_32x32x16_bf16 v[128:143], v[92:95], v[28:31], v[128:143]\n"
  "s_waitcnt lgkmcnt(7)\n"
  "v_mfma_f32_32x32x16_bf16 v[128:143], v[96:99], v[32:35], v[128:143]\n"
  "s_waitcnt lgkmcnt(6)\n"
  "v_mfma_f32_32x32x16_bf16 v[128:143], v[100:103], v[36:39], v[128:143]\n"
  "s_waitcnt lgkmcnt(5)\n"
  "v_mfma_f32_32x32x16_bf16 v[128:143], v[104:107], v[40:43], v[128:143]\n"
  "s_waitcnt lgkmcnt(4)\n"
  "v_mfma_f32_32x32x16_bf16 v[128:143], v[108:111], v[44:47], v[128:143]\n"
  "s_waitcnt lgkmcnt(3)\n"
  "v_mfma_f32_32x32x16_bf16 v[128:143], v[112:115], v[48:51], v[128:143]\n"
  "s_waitcnt lgkmcnt(2)\n"
  "v_mfma_f32_32x32x16_bf16 v[128:143], v[116:119], v[52:55], v[128:143]\n"
  "s_waitcnt lgkmcnt(1)\n"
  "v_mfma_f32_32x32x16_bf16 v[128:143], v[120:123], v[56:59], v[128:143]\n"
  "s_waitcnt lgkmcnt(0)\n"
  "v_mfma_f32_32x32x16_bf16 v[128:143], v[124:127], v[60:63], v[128:143]\n"
  "s_branch LJ%=\n"
  "LZ%=:\n"
        : "={s[26:27]}"(tag), "={s[44:45]}"(ws), "={s54}"(wv), "+{v0}"(tid)
        : "{s[0:1]}"(__builtin_amdgcn_kernarg_segment_ptr()), "{s2}"(blockIdx.x), "s"((__attribute__((address_space(3))) char*)smem)
        : "v1", "v2", "v3", "v4", "v5", "v6", "v7", "v8", "v9", "v10", "v11", "v12", "v13", "v14", "v15", "v16", "v17", "v18", "v19", "v20", "v21", "v22", "v23", "v24", "v25", "v26", "v27", "v28", "v29", "v30", "v31", "v32", "v33", "v34", "v35", "v36", "v37", "v38", "v39", "v40", "v41", "v42", "v43", "v44", "v45", "v46", "v47", "v48", "v49", "v50", "v51", "v52", "v53", "v54", "v55", "v56", "v57", "v58", "v59", "v60", "v61", "v62", "v63", "v64", "v65", "v66", "v67", "v68", "v69", "v70", "v71", "v72", "v73", "v74", "v75", "v76", "v77", "v78", "v79", "v80", "v81", "v82", "v83", "v84", "v85", "v86", "v87", "v88", "v89", "v90", "v91", "v92", "v93", "v94", "v95", "v96", "v97", "v98", "v99", "v100", "v101", "v102", "v103", "v104", "v105", "v106", "v107", "v108", "v109", "v110", "v111", "v112", "v113", "v114", "v115", "v116", "v117", "v118", "v119", "v120", "v121", "v122", "v123", "v124", "v125", "v126", "v127", "v128", "v129", "v130", "v131", "v132", "v133", "v134", "v135", "v136", "v137", "v138", "v139", "v140", "v141", "v142", "v143", "v144", "v145", "v146", "v147", "v148", "v149", "v150", "v151", "v152", "v153", "v154", "v155", "v156", "v157", "v158", "v159", "v160", "v161", "v162", "v163", "v164", "v165", "v166", "v167", "v168", "v169", "v170", "v171", "v172", "v173", "v174", "v175", "v176", "v177", "v178", "v179", "v180", "v181", "v182", "v183", "v184", "v185", "v186", "v187", "v188", "v189", "v190", "v191", "v192", "v193", "v194", "v195", "v196", "v197", "v198", "v199", "v200", "v201", "v202", "v203", "v204", "v205", "v206", "v207", "v208", "v209", "v210", "v211", "v212", "v213", "v214", "v215", "v216", "v217", "v218", "v219", "v220", "v221", "v222", "v223", "v224", "v225", "v226", "v227", "v228", "v229", "v230", "v231", "v232", "v233", "v234", "v235", "v236", "v237", "v238", "v239", "v240", "v241", "v242", "v243", "v244", "v245", "v246", "v247", "v248", "v249", "v250", "v251", "v252", "v253", "v254", "v255", "s4", "s5", "s6", "s7", "s8", "s9", "s10", "s11", "s12", "s13", "s14", "s15", "s16", "s17", "s18", "s19", "s20", "s21", "s22", "s23", "s24", "s25", "s28", "s29", "s30", "s31", "s36", "s37", "s38", "s39", "s40", "s41", "s42", "s43", "s46", "s47", "s48", "s49", "s50", "s51", "s52", "s53", "s55", "s56", "s57", "s58", "s59", "s60", "s61", "s62", "s63", "s64", "s65", "s66", "s67", "s68", "s69", "s70", "s71", "vcc", "scc", "m0", "memory");
    u32 l; asm volatile("v_mbcnt_lo_u32_b32 %0, -1, 0\n\tv_mbcnt_hi_u32_b32 %0, -1, %0" : "=v"(l));
#pragma clang loop unroll(disable)
    for (int s = 0; s < (1 << 18); ++s) {
        if (__all(__hip_atomic_load((GAS u64*)(ws + 0x11000 + (wv * 64 + l) * 128), __ATOMIC_RELAXED, __HIP_MEMORY_SCOPE_AGENT) == tag)) break;
        __builtin_amdgcn_s_sleep(8);
    }
    asm volatile("s_barrier" ::: "memory");
    if (wv == 0 && l == 0) __hip_atomic_store((GAS u64*)ws, tag, __ATOMIC_RELAXED, __HIP_MEMORY_SCOPE_AGENT);
}

extern "C" void kernel_launch(void* const* d_in, const int* in_sizes, int n_in, void* d_out, int out_size, void* d_ws, size_t ws_size, hipStream_t stream) {
    fused_ssm_mha<<<512, 256, 0, stream>>>((const u16*)d_in[9], (const u16*)d_in[10], (char*)d_ws, (const u16*)d_in[1], (const u16*)d_in[2], (const u16*)d_in[7], (const u16*)d_in[11],
                                           (u16*)d_out, (const u16*)d_in[0], (const u16*)d_in[3], (const u16*)d_in[4], (const u16*)d_in[5], (const u16*)d_in[6], (const u16*)d_in[8]);
}
```
